```python
import torch

def _softmax(x, axis=-1):
    return torch.softmax(x, dim=axis)
def _gelu(x):
    return torch.nn.functional.gelu(x, approximate="tanh")
def _silu(x):
    return torch.nn.functional.silu(x)
def _sigmoid(x):
    return torch.sigmoid(x)
def _layernorm(x, axis=-1, eps=1e-5):
    m = x.mean(dim=axis, keepdim=True)
    v = x.var(dim=axis, keepdim=True, unbiased=False)
    return (x - m) / torch.sqrt(v + eps)
def _scatter_add(tgt, idx, src, axis=0):
    return tgt.clone().index_add_(axis, idx, src)
def _segment_sum(src, seg, n):
    out = torch.zeros((n,) + src.shape[1:], dtype=src.dtype)
    return out.index_add_(0, seg, src)
def _pad(x, pw):
    flat = tuple(v for pair in reversed(pw) for v in pair)
    return torch.nn.functional.pad(x, flat)

kernel_name = 'concat_heads__ssm_conv_h3__grouped_kv_mqa__prompt'

def setup_inputs():

    _w = (lambda a: (lambda t: t.to(torch.bfloat16) if t.is_floating_point() else t)(torch.as_tensor(a)))
    def _A_setup():
        B, S, D = 1, 2048, 576
        ks = 4
        return {
            "q": _w((lambda *s: torch.randn(*s, dtype=torch.bfloat16) * 0.05)(B, S, D)),
            "k": _w((lambda *s: torch.randn(*s, dtype=torch.bfloat16) * 0.2)(B, S, D)),
            "v": _w((lambda *s: torch.randn(*s, dtype=torch.bfloat16) * 0.2)(B, S, D)),
            "shift_k": _w((lambda *s: torch.randn(*s, dtype=torch.bfloat16) * 0.2)(D, ks) * 0.5),
            "ssm_k": _w((lambda *s: torch.randn(*s, dtype=torch.bfloat16) * 0.8)(D, S) * (1.0 / S ** 0.5)),
            "ssm_D": _w((lambda *s: torch.randn(*s, dtype=torch.bfloat16) * 0.2)(D)),
        }
    _A_in = _A_setup()

    def _B_setup():
        B, S, D, H = 1, 2048, 576, 32
        Dh = D // H
        return {
            "x": _w((lambda *s: torch.randn(*s, dtype=torch.bfloat16) * 0.05)(B, S, D)),
            "wq": _w((lambda *s: torch.randn(*s, dtype=torch.bfloat16) * 0.8)(D, H * Dh)),
            "wk": _w((lambda *s: torch.randn(*s, dtype=torch.bfloat16) * 0.8)(D, Dh)),
            "wv": _w((lambda *s: torch.randn(*s, dtype=torch.bfloat16) * 0.05)(D, Dh)),
            "wo": _w((lambda *s: torch.randn(*s, dtype=torch.bfloat16) * 0.05)(H * Dh, D)),
        }
    _B_in = _B_setup()

    return {
        "A_q": _A_in["q"],
        "A_k": _A_in["k"],
        "A_v": _A_in["v"],
        "A_shift_k": _A_in["shift_k"],
        "A_ssm_k": _A_in["ssm_k"],
        "A_ssm_D": _A_in["ssm_D"],
        "B_x": _B_in["x"],
        "B_wq": _B_in["wq"],
        "B_wk": _B_in["wk"],
        "B_wv": _B_in["wv"],
        "B_wo": _B_in["wo"],
    }

def reference(A_q, A_k, A_v, A_shift_k, A_ssm_k, A_ssm_D, B_x, B_wq, B_wk, B_wv, B_wo):
    def _A_ref(q, k, v, shift_k, ssm_k, ssm_D):
        B, S, D = q.shape
        ks = shift_k.shape[-1]
        k_pad = _pad(k, ((0, 0), (ks - 1, 0), (0, 0)))
        k_shift = torch.zeros_like(k)
        for t in range(ks):
            k_shift = k_shift + k_pad[:, t:t + S, :] * shift_k[:, ks - 1 - t][None, None, :]
        kv = k_shift * v
        L = 2 * S
        kv_pad = _pad(kv, ((0, 0), (0, S), (0, 0)))
        kf_pad = _pad(ssm_k, ((0, 0), (0, S)))
        kv_f = (lambda x, axes=(-2,-1): torch.fft.rfft2(x.float(), dim=axes))(kv_pad[:, None, :, :], axes=(1, 2))
        kf_f = (lambda x, axes=(-2,-1): torch.fft.rfft2(x.float(), dim=axes))(kf_pad[:, None, :], axes=(1, 2))
        kf_f = (lambda x, *d: x.permute(*d))(kf_f, 1, 2, 0)
        y_f = kv_f * kf_f[None, :, :, :]
        y = (lambda x, s, axes=(-2,-1): torch.fft.irfft2(x, s=s, dim=axes).to(torch.bfloat16))(y_f, (1, L), axes=(1, 2))[:, 0, :S, :]
        y = y + kv * ssm_D[None, None, :]
        return q * y

    def _B_ref(x, wq, wk, wv, wo):
        B, S, D = x.shape
        H = 32
        Dh = D // H
        q = (lambda x, *d: x.permute(*d))((x @ wq).reshape(B, S, H, Dh), 0, 2, 1, 3)
        k = x @ wk
        v = x @ wv
        scores = torch.einsum("bhqd,bkd->bhqk", q, k) * (Dh ** -0.5)
        p = _softmax(scores, axis=-1)
        o = torch.einsum("bhqk,bkd->bhqd", p, v)
        return (lambda x, *d: x.permute(*d))(o, 0, 2, 1, 3).reshape(B, S, H * Dh) @ wo

    a = _A_ref(A_q, A_k, A_v, A_shift_k, A_ssm_k, A_ssm_D)
    b = _B_ref(B_x, B_wq, B_wk, B_wv, B_wo)
    return torch.concatenate([a, b], axis=-1)
```

```cpp
#include <hip/hip_runtime.h>
#include <hip/hip_bf16.h>

#define GAS __attribute__((address_space(1)))
#define LAS __attribute__((address_space(3)))
typedef unsigned v4u __attribute__((ext_vector_type(4)));
typedef unsigned v2u __attribute__((ext_vector_type(2)));
typedef float f32x2 __attribute__((ext_vector_type(2)));
typedef float f32x4 __attribute__((ext_vector_type(4)));
typedef float f32x8 __attribute__((ext_vector_type(8)));
typedef float f32x12 __attribute__((ext_vector_type(12)));
typedef float f32x16 __attribute__((ext_vector_type(16)));
typedef __bf16 bf16x2 __attribute__((ext_vector_type(2)));
typedef __bf16 bf16x8 __attribute__((ext_vector_type(8)));
typedef __bf16 bf16x16 __attribute__((ext_vector_type(16)));
typedef short s16x4 __attribute__((ext_vector_type(4)));
typedef unsigned long long u64;
typedef GAS u64 gu64;
#define RLX_AGENT __ATOMIC_RELAXED, __HIP_MEMORY_SCOPE_AGENT
#define RLX_WG __ATOMIC_RELAXED, __HIP_MEMORY_SCOPE_WORKGROUP

#define ATTN_ASM \
  "v_mov_b32 v166, 0xdddd\n" \
  "v_mov_b32 v92, 0\n" \
  "v_mov_b32 v93, 0\n" \
  "v_mov_b32 v94, 0\n" \
  "v_mov_b32 v95, 0\n" \
  "v_mov_b32 v96, 0\n" \
  "v_mov_b32 v97, 0\n" \
  "v_mov_b32 v98, 0\n" \
  "v_mov_b32 v99, 0\n" \
  "7:\n" \
  "ds_read_b32 v167, %[af] offset:8\n" \
  "s_waitcnt lgkmcnt(0)\n" \
  "v_readfirstlane_b32 %[st], v167\n" \
  "s_nop 3\n" \
  "s_cmp_ge_u32 %[st], 2\n" \
  "s_cbranch_scc1 18f\n" \
  "s_sleep 1\n" \
  "s_branch 7b\n" \
  "18:\n" \
  "ds_read_b128 v[160:163], %[ak] offset:0\n" \
  "ds_read2_b32 v[164:165], %[ax] offset0:0 offset1:32\n" \
  "v_mov_b32 v109, 0\n" \
  "v_mov_b32 v110, 0\n" \
  "s_mov_b32 %[cnt], 8\n" \
  "19:\n" \
  "ds_read_b32 v167, %[af] offset:68\n" \
  "s_waitcnt lgkmcnt(0)\n" \
  "v_readfirstlane_b32 %[st], v167\n" \
  "s_nop 3\n" \
  "s_cmp_ge_u32 %[st], 8\n" \
  "s_cbranch_scc0 19b\n" \
  "ds_read_b32 v108, %[qe]\n" \
  "ds_read_b32 v111, %[qe]\n" \
  "ds_read2_b32 v[100:101], %[qr] offset0:0 offset1:1\n" \
  "ds_read2_b32 v[102:103], %[qr] offset0:2 offset1:3\n" \
  "v_add_u32 v167, 4608, %[qr]\n" \
  "ds_read2_b32 v[104:105], v167 offset0:0 offset1:1\n" \
  "ds_read2_b32 v[106:107], v167 offset0:2 offset1:3\n" \
  "s_waitcnt lgkmcnt(4)\n" \
  "v_mfma_f32_32x32x4_2b_bf16 v[36:67], v[164:165], v[108:109], 0\n" \
  "v_mov_b32 v167, 1\n" \
  "s_waitcnt lgkmcnt(0)\n" \
  "s_mov_b64 exec, 1\n" \
  "ds_add_u32 %[af], v167 offset:68\n" \
  "s_mov_b64 exec, -1\n" \
  "s_cmp_eq_u32 %[yw], 0\n" \
  "s_cbranch_scc1 2f\n" \
  "s_setprio 1\n" \
  "2:\n" \
  "s_nop 7\n" \
  "s_nop 2\n" \
  "v_mfma_f32_32x32x16_bf16 v[36:51], v[160:163], v[100:103], v[36:51]\n" \
  "v_mfma_f32_32x32x16_bf16 v[52:67], v[160:163], v[104:107], v[52:67]\n" \
  "ds_read_b128 v[160:163], %[ak] offset:1024\n" \
  "s_nop 7\n" \
  "s_nop 3\n" \
  "1:\n" \
  "v_exp_f32 v36, v36\n" \
  "v_exp_f32 v37, v37\n" \
  "v_smfmac_f32_32x32x32_bf16 v[128:143], v[156:159], v[92:99], v166\n" \
  "s_mov_b64 exec, %[vm]\n" \
  "ds_read_b128 v[144:147], %[av] offset:0\n" \
  "ds_read_b128 v[148:151], %[av] offset:576\n" \
  "ds_read_b128 v[152:155], %[av] offset:1152\n" \
  "ds_read_b128 v[156:159], %[av] offset:1728\n" \
  "s_mov_b64 exec, -1\n" \
  "v_exp_f32 v38, v38\n" \
  "v_exp_f32 v39, v39\n" \
  "v_exp_f32 v40, v40\n" \
  "v_exp_f32 v41, v41\n" \
  "v_mfma_f32_32x32x4_2b_bf16 v[68:99], v[164:165], v[110:111], 0\n" \
  "ds_read2_b32 v[164:165], %[ax] offset0:64 offset1:96\n" \
  "v_add_u32 %[ax], 9216, %[ax]\n" \
  "v_exp_f32 v42, v42\n" \
  "v_exp_f32 v43, v43\n" \
  "v_exp_f32 v52, v52\n" \
  "v_exp_f32 v53, v53\n" \
  "v_exp_f32 v54, v54\n" \
  "v_exp_f32 v55, v55\n" \
  "v_exp_f32 v56, v56\n" \
  "v_exp_f32 v57, v57\n" \
  "s_waitcnt lgkmcnt(3)\n" \
  "v_smfmac_f32_32x32x32_bf16 v[112:127], v[144:147], v[36:43], v166\n" \
  "v_exp_f32 v58, v58\n" \
  "v_exp_f32 v59, v59\n" \
  "v_exp_f32 v44, v44\n" \
  "v_exp_f32 v45, v45\n" \
  "v_smfmac_f32_32x32x32_bf16 v[128:143], v[144:147], v[52:59], v166\n" \
  "v_exp_f32 v46, v46\n" \
  "v_exp_f32 v47, v47\n" \
  "v_exp_f32 v48, v48\n" \
  "v_exp_f32 v49, v49\n" \
  "v_mfma_f32_32x32x16_bf16 v[68:83], v[160:163], v[100:103], v[68:83]\n" \
  "v_exp_f32 v50, v50\n" \
  "v_exp_f32 v51, v51\n" \
  "v_exp_f32 v60, v60\n" \
  "v_exp_f32 v61, v61\n" \
  "v_mfma_f32_32x32x16_bf16 v[84:99], v[160:163], v[104:107], v[84:99]\n" \
  "ds_read_b128 v[160:163], %[ak] offset:2048\n" \
  "v_exp_f32 v62, v62\n" \
  "v_exp_f32 v63, v63\n" \
  "v_exp_f32 v64, v64\n" \
  "v_exp_f32 v65, v65\n" \
  "v_smfmac_f32_32x32x32_bf16 v[112:127], v[148:151], v[44:51], v166\n" \
  "v_exp_f32 v66, v66\n" \
  "v_exp_f32 v67, v67\n" \
  "s_bitcmp1_b32 260, %[cnt]\n" \
  "s_cbranch_scc1 11f\n" \
  "9:\n" \
  "v_exp_f32 v68, v68\n" \
  "v_exp_f32 v69, v69\n" \
  "v_smfmac_f32_32x32x32_bf16 v[128:143], v[148:151], v[60:67], v166\n" \
  "v_exp_f32 v70, v70\n" \
  "v_exp_f32 v71, v71\n" \
  "v_exp_f32 v72, v72\n" \
  "v_exp_f32 v73, v73\n" \
  "s_waitcnt lgkmcnt(0)\n" \
  "v_mfma_f32_32x32x4_2b_bf16 v[36:67], v[164:165], v[108:109], 0\n" \
  "v_exp_f32 v74, v74\n" \
  "v_exp_f32 v75, v75\n" \
  "v_exp_f32 v84, v84\n" \
  "v_exp_f32 v85, v85\n" \
  "v_exp_f32 v86, v86\n" \
  "v_exp_f32 v87, v87\n" \
  "v_exp_f32 v88, v88\n" \
  "v_exp_f32 v89, v89\n" \
  "v_smfmac_f32_32x32x32_bf16 v[112:127], v[152:155], v[68:75], v166\n" \
  "v_exp_f32 v90, v90\n" \
  "v_exp_f32 v91, v91\n" \
  "v_exp_f32 v76, v76\n" \
  "v_exp_f32 v77, v77\n" \
  "v_smfmac_f32_32x32x32_bf16 v[128:143], v[152:155], v[84:91], v166\n" \
  "v_exp_f32 v78, v78\n" \
  "v_exp_f32 v79, v79\n" \
  "v_exp_f32 v80, v80\n" \
  "v_exp_f32 v81, v81\n" \
  "v_mfma_f32_32x32x16_bf16 v[36:51], v[160:163], v[100:103], v[36:51]\n" \
  "v_exp_f32 v82, v82\n" \
  "v_exp_f32 v83, v83\n" \
  "v_exp_f32 v92, v92\n" \
  "v_exp_f32 v93, v93\n" \
  "v_mfma_f32_32x32x16_bf16 v[52:67], v[160:163], v[104:107], v[52:67]\n" \
  "ds_read_b128 v[160:163], %[ak] offset:3072\n" \
  "v_exp_f32 v94, v94\n" \
  "v_exp_f32 v95, v95\n" \
  "v_exp_f32 v96, v96\n" \
  "v_exp_f32 v97, v97\n" \
  "v_smfmac_f32_32x32x32_bf16 v[112:127], v[156:159], v[76:83], v166\n" \
  "v_exp_f32 v98, v98\n" \
  "v_exp_f32 v99, v99\n" \
  "v_exp_f32 v36, v36\n" \
  "v_exp_f32 v37, v37\n" \
  "v_smfmac_f32_32x32x32_bf16 v[128:143], v[156:159], v[92:99], v166\n" \
  "s_mov_b64 exec, %[vm]\n" \
  "ds_read_b128 v[144:147], %[av] offset:2304\n" \
  "ds_read_b128 v[148:151], %[av] offset:2880\n" \
  "ds_read_b128 v[152:155], %[av] offset:3456\n" \
  "ds_read_b128 v[156:159], %[av] offset:4032\n" \
  "s_mov_b64 exec, -1\n" \
  "v_exp_f32 v38, v38\n" \
  "v_exp_f32 v39, v39\n" \
  "v_exp_f32 v40, v40\n" \
  "v_exp_f32 v41, v41\n" \
  "v_mfma_f32_32x32x4_2b_bf16 v[68:99], v[164:165], v[110:111], 0\n" \
  "ds_read2_b32 v[164:165], %[ax] offset0:0 offset1:32\n" \
  "v_exp_f32 v42, v42\n" \
  "v_exp_f32 v43, v43\n" \
  "v_exp_f32 v52, v52\n" \
  "v_exp_f32 v53, v53\n" \
  "v_exp_f32 v54, v54\n" \
  "v_exp_f32 v55, v55\n" \
  "v_exp_f32 v56, v56\n" \
  "v_exp_f32 v57, v57\n" \
  "s_waitcnt lgkmcnt(3)\n" \
  "v_smfmac_f32_32x32x32_bf16 v[112:127], v[144:147], v[36:43], v166\n" \
  "v_exp_f32 v58, v58\n" \
  "v_exp_f32 v59, v59\n" \
  "v_exp_f32 v44, v44\n" \
  "v_exp_f32 v45, v45\n" \
  "v_smfmac_f32_32x32x32_bf16 v[128:143], v[144:147], v[52:59], v166\n" \
  "v_exp_f32 v46, v46\n" \
  "v_exp_f32 v47, v47\n" \
  "v_exp_f32 v48, v48\n" \
  "v_exp_f32 v49, v49\n" \
  "v_mfma_f32_32x32x16_bf16 v[68:83], v[160:163], v[100:103], v[68:83]\n" \
  "v_exp_f32 v50, v50\n" \
  "v_exp_f32 v51, v51\n" \
  "v_exp_f32 v60, v60\n" \
  "v_exp_f32 v61, v61\n" \
  "v_mfma_f32_32x32x16_bf16 v[84:99], v[160:163], v[104:107], v[84:99]\n" \
  "ds_read_b128 v[160:163], %[ak] offset:9216\n" \
  "v_exp_f32 v62, v62\n" \
  "v_exp_f32 v63, v63\n" \
  "v_exp_f32 v64, v64\n" \
  "v_exp_f32 v65, v65\n" \
  "v_smfmac_f32_32x32x32_bf16 v[112:127], v[148:151], v[44:51], v166\n" \
  "v_exp_f32 v66, v66\n" \
  "v_exp_f32 v67, v67\n" \
  "v_exp_f32 v68, v68\n" \
  "v_exp_f32 v69, v69\n" \
  "v_smfmac_f32_32x32x32_bf16 v[128:143], v[148:151], v[60:67], v166\n" \
  "v_exp_f32 v70, v70\n" \
  "v_exp_f32 v71, v71\n" \
  "v_exp_f32 v72, v72\n" \
  "v_exp_f32 v73, v73\n" \
  "s_waitcnt lgkmcnt(0)\n" \
  "v_mfma_f32_32x32x4_2b_bf16 v[36:67], v[164:165], v[108:109], 0\n" \
  "v_exp_f32 v74, v74\n" \
  "v_exp_f32 v75, v75\n" \
  "v_exp_f32 v84, v84\n" \
  "v_exp_f32 v85, v85\n" \
  "v_exp_f32 v86, v86\n" \
  "v_exp_f32 v87, v87\n" \
  "v_exp_f32 v88, v88\n" \
  "v_exp_f32 v89, v89\n" \
  "v_smfmac_f32_32x32x32_bf16 v[112:127], v[152:155], v[68:75], v166\n" \
  "v_exp_f32 v90, v90\n" \
  "v_exp_f32 v91, v91\n" \
  "v_exp_f32 v76, v76\n" \
  "v_exp_f32 v77, v77\n" \
  "v_smfmac_f32_32x32x32_bf16 v[128:143], v[152:155], v[84:91], v166\n" \
  "v_exp_f32 v78, v78\n" \
  "v_exp_f32 v79, v79\n" \
  "v_exp_f32 v80, v80\n" \
  "v_exp_f32 v81, v81\n" \
  "v_mfma_f32_32x32x16_bf16 v[36:51], v[160:163], v[100:103], v[36:51]\n" \
  "v_exp_f32 v82, v82\n" \
  "v_exp_f32 v83, v83\n" \
  "v_exp_f32 v92, v92\n" \
  "v_exp_f32 v93, v93\n" \
  "v_mfma_f32_32x32x16_bf16 v[52:67], v[160:163], v[104:107], v[52:67]\n" \
  "ds_read_b128 v[160:163], %[ak] offset:10240\n" \
  "v_exp_f32 v94, v94\n" \
  "v_exp_f32 v95, v95\n" \
  "v_exp_f32 v96, v96\n" \
  "v_exp_f32 v97, v97\n" \
  "v_smfmac_f32_32x32x32_bf16 v[112:127], v[156:159], v[76:83], v166\n" \
  "v_add_u32 %[ak], 9216, %[ak]\n" \
  "v_add_u32 %[av], 9216, %[av]\n" \
  "v_exp_f32 v98, v98\n" \
  "v_exp_f32 v99, v99\n" \
  "s_sub_u32 %[cnt], %[cnt], 1\n" \
  "s_bitcmp1_b32 0xb5, %[cnt]\n" \
  "s_cbranch_scc0 1b\n" \
  "s_cmp_eq_u32 %[cnt], 0\n" \
  "s_cbranch_scc1 12f\n" \
  "s_cmp_eq_u32 %[cnt], 2\n" \
  "s_cbranch_scc0 3f\n" \
  "s_nop 0\n" \
  "v_mov_b32 v167, 1\n" \
  "ds_add_u32 %[af], v167 offset:76\n" \
  "3:\n" \
  "s_cmp_eq_u32 %[cnt], %[sig]\n" \
  "s_cbranch_scc0 6f\n" \
  "v_mov_b32 v167, 1\n" \
  "ds_add_u32 %[al], v167 offset:4\n" \
  "6:\n" \
  "s_cmp_eq_u32 %[cnt], 7\n" \
  "s_cselect_b32 %[thr], 2, 0\n" \
  "s_cmp_eq_u32 %[cnt], 5\n" \
  "s_cselect_b32 %[thr], 3, %[thr]\n" \
  "s_cmp_eq_u32 %[cnt], 4\n" \
  "s_cselect_b32 %[thr], %[thr4], %[thr]\n" \
  "s_cmp_eq_u32 %[thr], 0\n" \
  "s_cbranch_scc1 1b\n" \
  "5:\n" \
  "ds_read_b32 v167, %[al]\n" \
  "s_waitcnt lgkmcnt(0)\n" \
  "v_readfirstlane_b32 %[st], v167\n" \
  "s_nop 3\n" \
  "s_cmp_ge_u32 %[st], %[thr]\n" \
  "s_cbranch_scc0 5b\n" \
  "s_branch 1b\n" \
  "11:\n" \
  "s_cmp_eq_u32 %[cnt], 8\n" \
  "s_cbranch_scc1 20f\n" \
  "s_setprio 0\n" \
  "s_branch 9b\n" \
  "20:\n" \
  "ds_read_b32 v167, %[al]\n" \
  "s_waitcnt lgkmcnt(0)\n" \
  "v_readfirstlane_b32 %[st], v167\n" \
  "s_nop 3\n" \
  "s_cmp_ge_u32 %[st], 1\n" \
  "s_cbranch_scc0 20b\n" \
  "s_branch 9b\n" \
  "12:\n" \
  "s_nop 1\n" \
  "v_smfmac_f32_32x32x32_bf16 v[128:143], v[156:159], v[92:99], v166\n" \
  "8:\n" \
  "ds_read_b32 v167, %[af] offset:76\n" \
  "s_waitcnt lgkmcnt(0)\n" \
  "v_readfirstlane_b32 %[st], v167\n" \
  "s_nop 3\n" \
  "s_cmp_ge_u32 %[st], 512\n" \
  "s_cbranch_scc0 8b\n" \
  "s_cmp_eq_u32 %[yw], 0\n" \
  "s_cbranch_scc1 13f\n" \
  "ds_write_b128 %[exw], v[112:115] offset:0\n" \
  "ds_write_b128 %[exw], v[116:119] offset:1024\n" \
  "ds_write_b128 %[exw], v[120:123] offset:2048\n" \
  "s_branch 14f\n" \
  "13:\n" \
  "s_nop 1\n" \
  "ds_write_b128 %[exw], v[128:131] offset:0\n" \
  "ds_write_b128 %[exw], v[132:135] offset:1024\n" \
  "ds_write_b128 %[exw], v[136:139] offset:2048\n" \
  "14:\n" \
  "s_waitcnt lgkmcnt(0)\n" \
  "s_barrier\n" \
  "ds_read_b128 v[40:43], %[exr] offset:0\n" \
  "ds_read_b128 v[44:47], %[exr] offset:1024\n" \
  "ds_read_b128 v[48:51], %[exr] offset:2048\n" \
  "s_waitcnt lgkmcnt(0)\n"
#define ATTN_CLOB "v36","v37","v38","v39","v52","v53","v54","v55","v56","v57","v58","v59","v60","v61","v62","v63","v64","v65","v66","v67","v68","v69","v70","v71","v72","v73","v74","v75","v76","v77","v78","v79","v80","v81","v82","v83","v84","v85","v86","v87","v88","v89","v90","v91","v92","v93","v94","v95","v96","v97","v98","v99","v160","v161","v162","v163","v164","v165","v166","v167"
#define ONORM \
  "ds_read_b64_tr_b16 v[52:53], %[zp0]\n" \
  "ds_read_b64_tr_b16 v[54:55], %[zp0] offset:512\n" \
  "ds_read_b64_tr_b16 v[56:57], %[zp1]\n" \
  "ds_read_b64_tr_b16 v[58:59], %[zp1] offset:512\n" \
  "s_cmp_eq_u32 %[w7], 0\n" \
  "s_cbranch_scc0 164f\n" \
  "buffer_load_dwordx4 v[64:67], %[q1], %[qrs], 0 offen\n" \
  "164:\n" \
  "buffer_load_dwordx4 v[60:63], %[q0], %[qrs], 0 offen\n" \
  "s_cmp_eq_u32 %[kh], 0\n" \
  "s_cbranch_scc1 160f\n" \
  "v_add_f32 v40, v40, v128\n" \
  "v_add_f32 v41, v41, v129\n" \
  "v_add_f32 v42, v42, v130\n" \
  "v_add_f32 v43, v43, v131\n" \
  "v_add_f32 v44, v44, v132\n" \
  "v_add_f32 v45, v45, v133\n" \
  "v_add_f32 v46, v46, v134\n" \
  "v_add_f32 v47, v47, v135\n" \
  "v_add_f32 v48, v48, v136\n" \
  "v_add_f32 v49, v49, v137\n" \
  "v_add_f32 v50, v50, v138\n" \
  "s_branch 161f\n" \
  "160:\n" \
  "v_add_f32 v40, v40, v112\n" \
  "v_add_f32 v41, v41, v113\n" \
  "v_add_f32 v42, v42, v114\n" \
  "v_add_f32 v43, v43, v115\n" \
  "v_add_f32 v44, v44, v116\n" \
  "v_add_f32 v45, v45, v117\n" \
  "v_add_f32 v46, v46, v118\n" \
  "v_add_f32 v47, v47, v119\n" \
  "v_add_f32 v48, v48, v120\n" \
  "v_add_f32 v49, v49, v121\n" \
  "v_add_f32 v50, v50, v122\n" \
  "161:\n" \
  "v_mov_b32 v51, v50\n" \
  "v_max3_f32 v39, |v40|, |v41|, |v42|\n" \
  "v_max3_f32 v39, |v43|, |v44|, v39\n" \
  "v_permlane32_swap_b32 v50, v51\n" \
  "v_max3_f32 v39, |v45|, |v46|, v39\n" \
  "v_max3_f32 v39, |v47|, |v48|, v39\n" \
  "v_max_f32 v39, |v49|, v39\n" \
  "v_cmp_le_f32 vcc, 0x17800000, v50\n" \
  "s_mov_b64 %[m0], vcc\n" \
  "v_cmp_ge_f32 vcc, 0x67800000, v50\n" \
  "s_and_b64 %[m0], %[m0], vcc\n" \
  "v_cmp_gt_f32 vcc, 0x7f000000, v39\n" \
  "v_rcp_f32 v50, v50\n" \
  "s_and_b64 vcc, vcc, %[m0]\n" \
  "s_cmp_eq_u64 vcc, -1\n" \
  "s_cbranch_scc1 166f\n" \
  "v_mov_b32 v39, %[chk]\n" \
  "ds_write_b32 %[bf], v39\n" \
  "166:\n" \
  "v_mul_f32 v40, v40, v50\n" \
  "v_mul_f32 v41, v41, v50\n" \
  "v_cvt_pk_bf16_f32 v40, v40, v41\n" \
  "v_min_u32 v40, -2, v40\n" \
  "ds_write_b32 %[ot0], v40 offset:0\n" \
  "v_mul_f32 v42, v42, v50\n" \
  "v_mul_f32 v43, v43, v50\n" \
  "v_cvt_pk_bf16_f32 v42, v42, v43\n" \
  "v_min_u32 v42, -2, v42\n" \
  "ds_write_b32 %[ot1], v42 offset:0\n" \
  "v_mul_f32 v44, v44, v50\n" \
  "v_mul_f32 v45, v45, v50\n" \
  "v_cvt_pk_bf16_f32 v44, v44, v45\n" \
  "v_min_u32 v44, -2, v44\n" \
  "ds_write_b32 %[ot0], v44 offset:1024\n" \
  "v_mul_f32 v46, v46, v50\n" \
  "v_mul_f32 v47, v47, v50\n" \
  "v_cvt_pk_bf16_f32 v46, v46, v47\n" \
  "v_min_u32 v46, -2, v46\n" \
  "ds_write_b32 %[ot1], v46 offset:1024\n" \
  "v_mul_f32 v48, v48, v50\n" \
  "v_mul_f32 v49, v49, v50\n" \
  "v_cvt_pk_bf16_f32 v48, v48, v49\n" \
  "v_min_u32 v48, -2, v48\n" \
  "s_mov_b32 exec_hi, 0\n" \
  "ds_write_b32 %[ot0], v48 offset:2048\n" \
  "s_mov_b32 exec_hi, -1\n" \
  "s_waitcnt lgkmcnt(0)\n" \
  "s_barrier\n" \
  "ds_read_b32 v39, %[bf]\n" \
  "ds_read_b128 v[40:43], %[otr]\n" \
  "ds_read_b128 v[44:47], %[otr] offset:1024\n" \
  "s_waitcnt lgkmcnt(2)\n" \
  "v_readfirstlane_b32 %[bad], v39\n" \
  "s_nop 3\n" \
  "s_cmp_lg_u32 %[bad], 0\n" \
  "s_cbranch_scc1 167f\n" \
  "s_cmp_eq_u32 %[w7], 7\n" \
  "s_waitcnt lgkmcnt(1)\n" \
  "buffer_store_dwordx4 v[40:43], %[lv], %[rs], %[sp] offen sc1\n" \
  "s_cbranch_scc0 162f\n" \
  "s_waitcnt lgkmcnt(0)\n" \
  "buffer_store_dwordx4 v[44:47], %[lv], %[rs], %[sp] offen offset:1024 sc1\n" \
  "162:\n" \
  "s_waitcnt lgkmcnt(0)\n" \
  "s_barrier\n" \
  "s_waitcnt vmcnt(2)\n" \
  "s_cbranch_scc1 165f\n" \
  "s_waitcnt vmcnt(1)\n" \
  "165:\n" \
  "v_lshlrev_b32 v40, 16, v52\n" \
  "v_and_b32 v41, 0xffff0000, v52\n" \
  "v_lshlrev_b32 v42, 16, v60\n" \
  "v_and_b32 v43, 0xffff0000, v60\n" \
  "v_mul_f32 v40, v40, v42\n" \
  "v_mul_f32 v41, v41, v43\n" \
  "v_cvt_pk_bf16_f32 v52, v40, v41\n" \
  "v_lshlrev_b32 v40, 16, v53\n" \
  "v_and_b32 v41, 0xffff0000, v53\n" \
  "v_lshlrev_b32 v42, 16, v61\n" \
  "v_and_b32 v43, 0xffff0000, v61\n" \
  "v_mul_f32 v40, v40, v42\n" \
  "v_mul_f32 v41, v41, v43\n" \
  "v_cvt_pk_bf16_f32 v53, v40, v41\n" \
  "v_lshlrev_b32 v40, 16, v54\n" \
  "v_and_b32 v41, 0xffff0000, v54\n" \
  "v_lshlrev_b32 v42, 16, v62\n" \
  "v_and_b32 v43, 0xffff0000, v62\n" \
  "v_mul_f32 v40, v40, v42\n" \
  "v_mul_f32 v41, v41, v43\n" \
  "v_cvt_pk_bf16_f32 v54, v40, v41\n" \
  "v_lshlrev_b32 v40, 16, v55\n" \
  "v_and_b32 v41, 0xffff0000, v55\n" \
  "v_lshlrev_b32 v42, 16, v63\n" \
  "v_and_b32 v43, 0xffff0000, v63\n" \
  "v_mul_f32 v40, v40, v42\n" \
  "v_mul_f32 v41, v41, v43\n" \
  "v_cvt_pk_bf16_f32 v55, v40, v41\n" \
  "buffer_store_dwordx4 v[52:55], %[o0], %[ors], 0 offen\n" \
  "s_cmp_eq_u32 %[w7], 0\n" \
  "s_cbranch_scc0 163f\n" \
  "v_lshlrev_b32 v40, 16, v56\n" \
  "v_and_b32 v41, 0xffff0000, v56\n" \
  "v_lshlrev_b32 v42, 16, v64\n" \
  "v_and_b32 v43, 0xffff0000, v64\n" \
  "v_mul_f32 v40, v40, v42\n" \
  "v_mul_f32 v41, v41, v43\n" \
  "v_cvt_pk_bf16_f32 v56, v40, v41\n" \
  "v_lshlrev_b32 v40, 16, v57\n" \
  "v_and_b32 v41, 0xffff0000, v57\n" \
  "v_lshlrev_b32 v42, 16, v65\n" \
  "v_and_b32 v43, 0xffff0000, v65\n" \
  "v_mul_f32 v40, v40, v42\n" \
  "v_mul_f32 v41, v41, v43\n" \
  "v_cvt_pk_bf16_f32 v57, v40, v41\n" \
  "v_lshlrev_b32 v40, 16, v58\n" \
  "v_and_b32 v41, 0xffff0000, v58\n" \
  "v_lshlrev_b32 v42, 16, v66\n" \
  "v_and_b32 v43, 0xffff0000, v66\n" \
  "v_mul_f32 v40, v40, v42\n" \
  "v_mul_f32 v41, v41, v43\n" \
  "v_cvt_pk_bf16_f32 v58, v40, v41\n" \
  "v_lshlrev_b32 v40, 16, v59\n" \
  "v_and_b32 v41, 0xffff0000, v59\n" \
  "v_lshlrev_b32 v42, 16, v67\n" \
  "v_and_b32 v43, 0xffff0000, v67\n" \
  "v_mul_f32 v40, v40, v42\n" \
  "v_mul_f32 v41, v41, v43\n" \
  "v_cvt_pk_bf16_f32 v59, v40, v41\n" \
  "buffer_store_dwordx4 v[56:59], %[o1], %[ors], 0 offen\n" \
  "163:\n" \
  "s_branch 169f\n" \
  "167:\n" \
  "s_waitcnt vmcnt(0) lgkmcnt(0)\n" \
  "169:\n"
#define ONORM_CLOB "v39","v52","v53","v54","v55","v56","v57","v58","v59","v60","v61","v62","v63","v64","v65","v66","v67"
#define CONV_ASM \
  "s_mov_b32 %[cnt], 3\n" \
  "s_cmp_eq_u32 %[m1], 47\n" \
  "s_cbranch_scc1 61f\n" \
  "s_cmp_eq_u32 %[m1], 200\n" \
  "s_cbranch_scc1 62f\n" \
  "s_cmp_eq_u32 %[m1], 55\n" \
  "s_cbranch_scc1 63f\n" \
  "s_cmp_eq_u32 %[m1], 224\n" \
  "s_cbranch_scc1 64f\n" \
  "s_cmp_eq_u32 %[m1], 160\n" \
  "s_cbranch_scc1 65f\n" \
  "s_cmp_eq_u32 %[m1], 93\n" \
  "s_cbranch_scc1 66f\n" \
  "s_cmp_eq_u32 %[m1], 196\n" \
  "s_cbranch_scc1 67f\n" \
  "s_cmp_eq_u32 %[m1], 91\n" \
  "s_cbranch_scc1 68f\n" \
  "s_cmp_eq_u32 %[m1], 59\n" \
  "s_cbranch_scc1 69f\n" \
  "60:\n" \
  "ds_read2_b32 v[56:57], %[a3] offset0:128 offset1:129\n" \
  "ds_read2_b32 v[58:59], %[a3] offset0:130 offset1:131\n" \
  "ds_read_b128 v[60:63], %[b] offset:448\n" \
  "ds_read2_b32 v[52:53], %[a3] offset0:0 offset1:1\n" \
  "ds_read2_b32 v[54:55], %[a3] offset0:2 offset1:3\n" \
  "ds_read2_b32 v[44:45], %[a2] offset0:0 offset1:1\n" \
  "ds_read2_b32 v[46:47], %[a2] offset0:2 offset1:3\n" \
  "ds_read_b128 v[64:67], %[b] offset:960\n" \
  "ds_read2_b32 v[48:49], %[a2] offset0:128 offset1:129\n" \
  "ds_read2_b32 v[50:51], %[a2] offset0:130 offset1:131\n" \
  "ds_read2_b32 v[40:41], %[a1] offset0:128 offset1:129\n" \
  "ds_read2_b32 v[42:43], %[a1] offset0:130 offset1:131\n" \
  "ds_read_b128 v[68:71], %[b] offset:1472\n" \
  "ds_read2_b32 v[36:37], %[a1] offset0:0 offset1:1\n" \
  "ds_read2_b32 v[38:39], %[a1] offset0:2 offset1:3\n" \
  "ds_read_b128 v[72:75], %[b] offset:1984\n" \
  "ds_read2_b32 v[32:33], %[a0] offset0:128 offset1:129\n" \
  "ds_read2_b32 v[34:35], %[a0] offset0:130 offset1:131\n" \
  "ds_read_b128 v[76:79], %[b] offset:2496\n" \
  "ds_read2_b32 v[28:29], %[a0] offset0:0 offset1:1\n" \
  "ds_read2_b32 v[30:31], %[a0] offset0:2 offset1:3\n" \
  "ds_read_b128 v[80:83], %[b] offset:3008\n" \
  "ds_read_b128 v[84:87], %[b] offset:3520\n" \
  "ds_read_b128 v[88:91], %[b] offset:4032\n" \
  "s_waitcnt lgkmcnt(0)\n" \
  "v_mfma_f32_16x16x32_bf16 v[160:163], v[56:59], v[60:63], 0\n" \
  "ds_read2_b32 v[56:57], %[a3] offset0:144 offset1:145\n" \
  "ds_read2_b32 v[58:59], %[a3] offset0:146 offset1:147\n" \
  "v_mfma_f32_16x16x32_bf16 v[156:159], v[52:55], v[60:63], 0\n" \
  "v_mfma_f32_16x16x32_bf16 v[148:151], v[44:47], v[60:63], 0\n" \
  "ds_read_b128 v[60:63], %[b] offset:384\n" \
  "v_mfma_f32_16x16x32_bf16 v[160:163], v[52:55], v[64:67], v[160:163]\n" \
  "ds_read2_b32 v[52:53], %[a3] offset0:16 offset1:17\n" \
  "ds_read2_b32 v[54:55], %[a3] offset0:18 offset1:19\n" \
  "v_mfma_f32_16x16x32_bf16 v[156:159], v[48:51], v[64:67], v[156:159]\n" \
  "v_mfma_f32_16x16x32_bf16 v[148:151], v[40:43], v[64:67], v[148:151]\n" \
  "ds_read_b128 v[64:67], %[b] offset:896\n" \
  "v_mfma_f32_16x16x32_bf16 v[160:163], v[48:51], v[68:71], v[160:163]\n" \
  "ds_read2_b32 v[48:49], %[a2] offset0:144 offset1:145\n" \
  "ds_read2_b32 v[50:51], %[a2] offset0:146 offset1:147\n" \
  "v_mfma_f32_16x16x32_bf16 v[156:159], v[44:47], v[68:71], v[156:159]\n" \
  "v_mfma_f32_16x16x32_bf16 v[148:151], v[36:39], v[68:71], v[148:151]\n" \
  "ds_read_b128 v[68:71], %[b] offset:1408\n" \
  "v_mfma_f32_16x16x32_bf16 v[160:163], v[44:47], v[72:75], v[160:163]\n" \
  "ds_read2_b32 v[44:45], %[a2] offset0:16 offset1:17\n" \
  "ds_read2_b32 v[46:47], %[a2] offset0:18 offset1:19\n" \
  "v_mfma_f32_16x16x32_bf16 v[156:159], v[40:43], v[72:75], v[156:159]\n" \
  "v_mfma_f32_16x16x32_bf16 v[148:151], v[32:35], v[72:75], v[148:151]\n" \
  "ds_read_b128 v[72:75], %[b] offset:1920\n" \
  "v_mfma_f32_16x16x32_bf16 v[160:163], v[40:43], v[76:79], v[160:163]\n" \
  "ds_read2_b32 v[40:41], %[a1] offset0:144 offset1:145\n" \
  "ds_read2_b32 v[42:43], %[a1] offset0:146 offset1:147\n" \
  "v_mfma_f32_16x16x32_bf16 v[156:159], v[36:39], v[76:79], v[156:159]\n" \
  "v_mfma_f32_16x16x32_bf16 v[148:151], v[28:31], v[76:79], v[148:151]\n" \
  "ds_read_b128 v[76:79], %[b] offset:2432\n" \
  "v_mfma_f32_16x16x32_bf16 v[160:163], v[36:39], v[80:83], v[160:163]\n" \
  "ds_read2_b32 v[36:37], %[a1] offset0:16 offset1:17\n" \
  "ds_read2_b32 v[38:39], %[a1] offset0:18 offset1:19\n" \
  "v_mfma_f32_16x16x32_bf16 v[156:159], v[32:35], v[80:83], v[156:159]\n" \
  "ds_read_b128 v[80:83], %[b] offset:2944\n" \
  "v_mfma_f32_16x16x32_bf16 v[160:163], v[32:35], v[84:87], v[160:163]\n" \
  "ds_read2_b32 v[32:33], %[a0] offset0:144 offset1:145\n" \
  "ds_read2_b32 v[34:35], %[a0] offset0:146 offset1:147\n" \
  "v_mfma_f32_16x16x32_bf16 v[156:159], v[28:31], v[84:87], v[156:159]\n" \
  "ds_read_b128 v[84:87], %[b] offset:3456\n" \
  "v_mfma_f32_16x16x32_bf16 v[160:163], v[28:31], v[88:91], v[160:163]\n" \
  "ds_read2_b32 v[28:29], %[a0] offset0:16 offset1:17\n" \
  "ds_read2_b32 v[30:31], %[a0] offset0:18 offset1:19\n" \
  "ds_read_b128 v[88:91], %[b] offset:3968\n" \
  "80:\n" \
  "s_waitcnt lgkmcnt(0)\n" \
  "v_mfma_f32_16x16x32_bf16 v[160:163], v[56:59], v[60:63], v[160:163]\n" \
  "ds_read2_b32 v[56:57], %[a3] offset0:160 offset1:161\n" \
  "ds_read2_b32 v[58:59], %[a3] offset0:162 offset1:163\n" \
  "v_mfma_f32_16x16x32_bf16 v[156:159], v[52:55], v[60:63], v[156:159]\n" \
  "v_mfma_f32_16x16x32_bf16 v[148:151], v[44:47], v[60:63], v[148:151]\n" \
  "ds_read_b128 v[60:63], %[b] offset:320\n" \
  "v_mfma_f32_16x16x32_bf16 v[160:163], v[52:55], v[64:67], v[160:163]\n" \
  "ds_read2_b32 v[52:53], %[a3] offset0:32 offset1:33\n" \
  "ds_read2_b32 v[54:55], %[a3] offset0:34 offset1:35\n" \
  "v_mfma_f32_16x16x32_bf16 v[156:159], v[48:51], v[64:67], v[156:159]\n" \
  "v_mfma_f32_16x16x32_bf16 v[148:151], v[40:43], v[64:67], v[148:151]\n" \
  "ds_read_b128 v[64:67], %[b] offset:832\n" \
  "v_mfma_f32_16x16x32_bf16 v[160:163], v[48:51], v[68:71], v[160:163]\n" \
  "ds_read2_b32 v[48:49], %[a2] offset0:160 offset1:161\n" \
  "ds_read2_b32 v[50:51], %[a2] offset0:162 offset1:163\n" \
  "v_mfma_f32_16x16x32_bf16 v[156:159], v[44:47], v[68:71], v[156:159]\n" \
  "v_mfma_f32_16x16x32_bf16 v[148:151], v[36:39], v[68:71], v[148:151]\n" \
  "ds_read_b128 v[68:71], %[b] offset:1344\n" \
  "v_mfma_f32_16x16x32_bf16 v[160:163], v[44:47], v[72:75], v[160:163]\n" \
  "ds_read2_b32 v[44:45], %[a2] offset0:32 offset1:33\n" \
  "ds_read2_b32 v[46:47], %[a2] offset0:34 offset1:35\n" \
  "v_mfma_f32_16x16x32_bf16 v[156:159], v[40:43], v[72:75], v[156:159]\n" \
  "v_mfma_f32_16x16x32_bf16 v[148:151], v[32:35], v[72:75], v[148:151]\n" \
  "ds_read_b128 v[72:75], %[b] offset:1856\n" \
  "v_mfma_f32_16x16x32_bf16 v[160:163], v[40:43], v[76:79], v[160:163]\n" \
  "ds_read2_b32 v[40:41], %[a1] offset0:160 offset1:161\n" \
  "ds_read2_b32 v[42:43], %[a1] offset0:162 offset1:163\n" \
  "v_mfma_f32_16x16x32_bf16 v[156:159], v[36:39], v[76:79], v[156:159]\n" \
  "v_mfma_f32_16x16x32_bf16 v[148:151], v[28:31], v[76:79], v[148:151]\n" \
  "ds_read_b128 v[76:79], %[b] offset:2368\n" \
  "v_mfma_f32_16x16x32_bf16 v[160:163], v[36:39], v[80:83], v[160:163]\n" \
  "ds_read2_b32 v[36:37], %[a1] offset0:32 offset1:33\n" \
  "ds_read2_b32 v[38:39], %[a1] offset0:34 offset1:35\n" \
  "v_mfma_f32_16x16x32_bf16 v[156:159], v[32:35], v[80:83], v[156:159]\n" \
  "ds_read_b128 v[80:83], %[b] offset:2880\n" \
  "v_mfma_f32_16x16x32_bf16 v[160:163], v[32:35], v[84:87], v[160:163]\n" \
  "ds_read2_b32 v[32:33], %[a0] offset0:160 offset1:161\n" \
  "ds_read2_b32 v[34:35], %[a0] offset0:162 offset1:163\n" \
  "v_mfma_f32_16x16x32_bf16 v[156:159], v[28:31], v[84:87], v[156:159]\n" \
  "ds_read_b128 v[84:87], %[b] offset:3392\n" \
  "v_mfma_f32_16x16x32_bf16 v[160:163], v[28:31], v[88:91], v[160:163]\n" \
  "ds_read2_b32 v[28:29], %[a0] offset0:32 offset1:33\n" \
  "ds_read2_b32 v[30:31], %[a0] offset0:34 offset1:35\n" \
  "ds_read_b128 v[88:91], %[b] offset:3904\n" \
  "s_waitcnt lgkmcnt(0)\n" \
  "v_mfma_f32_16x16x32_bf16 v[160:163], v[56:59], v[60:63], v[160:163]\n" \
  "ds_read2_b32 v[56:57], %[a3] offset0:176 offset1:177\n" \
  "ds_read2_b32 v[58:59], %[a3] offset0:178 offset1:179\n" \
  "v_mfma_f32_16x16x32_bf16 v[156:159], v[52:55], v[60:63], v[156:159]\n" \
  "v_mfma_f32_16x16x32_bf16 v[148:151], v[44:47], v[60:63], v[148:151]\n" \
  "ds_read_b128 v[60:63], %[b] offset:256\n" \
  "v_mfma_f32_16x16x32_bf16 v[160:163], v[52:55], v[64:67], v[160:163]\n" \
  "ds_read2_b32 v[52:53], %[a3] offset0:48 offset1:49\n" \
  "ds_read2_b32 v[54:55], %[a3] offset0:50 offset1:51\n" \
  "v_mfma_f32_16x16x32_bf16 v[156:159], v[48:51], v[64:67], v[156:159]\n" \
  "v_mfma_f32_16x16x32_bf16 v[148:151], v[40:43], v[64:67], v[148:151]\n" \
  "ds_read_b128 v[64:67], %[b] offset:768\n" \
  "v_mfma_f32_16x16x32_bf16 v[160:163], v[48:51], v[68:71], v[160:163]\n" \
  "ds_read2_b32 v[48:49], %[a2] offset0:176 offset1:177\n" \
  "ds_read2_b32 v[50:51], %[a2] offset0:178 offset1:179\n" \
  "v_mfma_f32_16x16x32_bf16 v[156:159], v[44:47], v[68:71], v[156:159]\n" \
  "v_mfma_f32_16x16x32_bf16 v[148:151], v[36:39], v[68:71], v[148:151]\n" \
  "ds_read_b128 v[68:71], %[b] offset:1280\n" \
  "v_mfma_f32_16x16x32_bf16 v[160:163], v[44:47], v[72:75], v[160:163]\n" \
  "ds_read2_b32 v[44:45], %[a2] offset0:48 offset1:49\n" \
  "ds_read2_b32 v[46:47], %[a2] offset0:50 offset1:51\n" \
  "v_mfma_f32_16x16x32_bf16 v[156:159], v[40:43], v[72:75], v[156:159]\n" \
  "v_mfma_f32_16x16x32_bf16 v[148:151], v[32:35], v[72:75], v[148:151]\n" \
  "ds_read_b128 v[72:75], %[b] offset:1792\n" \
  "v_mfma_f32_16x16x32_bf16 v[160:163], v[40:43], v[76:79], v[160:163]\n" \
  "ds_read2_b32 v[40:41], %[a1] offset0:176 offset1:177\n" \
  "ds_read2_b32 v[42:43], %[a1] offset0:178 offset1:179\n" \
  "v_mfma_f32_16x16x32_bf16 v[156:159], v[36:39], v[76:79], v[156:159]\n" \
  "v_mfma_f32_16x16x32_bf16 v[148:151], v[28:31], v[76:79], v[148:151]\n" \
  "ds_read_b128 v[76:79], %[b] offset:2304\n" \
  "v_mfma_f32_16x16x32_bf16 v[160:163], v[36:39], v[80:83], v[160:163]\n" \
  "ds_read2_b32 v[36:37], %[a1] offset0:48 offset1:49\n" \
  "ds_read2_b32 v[38:39], %[a1] offset0:50 offset1:51\n" \
  "v_mfma_f32_16x16x32_bf16 v[156:159], v[32:35], v[80:83], v[156:159]\n" \
  "ds_read_b128 v[80:83], %[b] offset:2816\n" \
  "v_mfma_f32_16x16x32_bf16 v[160:163], v[32:35], v[84:87], v[160:163]\n" \
  "ds_read2_b32 v[32:33], %[a0] offset0:176 offset1:177\n" \
  "ds_read2_b32 v[34:35], %[a0] offset0:178 offset1:179\n" \
  "v_mfma_f32_16x16x32_bf16 v[156:159], v[28:31], v[84:87], v[156:159]\n" \
  "ds_read_b128 v[84:87], %[b] offset:3328\n" \
  "v_mfma_f32_16x16x32_bf16 v[160:163], v[28:31], v[88:91], v[160:163]\n" \
  "ds_read2_b32 v[28:29], %[a0] offset0:48 offset1:49\n" \
  "ds_read2_b32 v[30:31], %[a0] offset0:50 offset1:51\n" \
  "ds_read_b128 v[88:91], %[b] offset:3840\n" \
  "v_add_u32 %[a0], 128, %[a0]\n" \
  "v_add_u32 %[a1], 128, %[a1]\n" \
  "v_add_u32 %[a2], 128, %[a2]\n" \
  "v_add_u32 %[a3], 128, %[a3]\n" \
  "v_subrev_u32 %[b], 128, %[b]\n" \
  "s_sub_u32 %[cnt], %[cnt], 1\n" \
  "s_cmp_lg_u32 %[cnt], 0\n" \
  "s_cbranch_scc1 80b\n" \
  "s_waitcnt lgkmcnt(0)\n" \
  "v_mfma_f32_16x16x32_bf16 v[160:163], v[56:59], v[60:63], v[160:163]\n" \
  "v_mfma_f32_16x16x32_bf16 v[156:159], v[52:55], v[60:63], v[156:159]\n" \
  "v_mfma_f32_16x16x32_bf16 v[148:151], v[44:47], v[60:63], v[148:151]\n" \
  "v_mfma_f32_16x16x32_bf16 v[160:163], v[52:55], v[64:67], v[160:163]\n" \
  "v_mfma_f32_16x16x32_bf16 v[156:159], v[48:51], v[64:67], v[156:159]\n" \
  "v_mfma_f32_16x16x32_bf16 v[148:151], v[40:43], v[64:67], v[148:151]\n" \
  "v_mfma_f32_16x16x32_bf16 v[160:163], v[48:51], v[68:71], v[160:163]\n" \
  "v_mfma_f32_16x16x32_bf16 v[156:159], v[44:47], v[68:71], v[156:159]\n" \
  "v_mfma_f32_16x16x32_bf16 v[148:151], v[36:39], v[68:71], v[148:151]\n" \
  "v_mfma_f32_16x16x32_bf16 v[160:163], v[44:47], v[72:75], v[160:163]\n" \
  "v_mfma_f32_16x16x32_bf16 v[156:159], v[40:43], v[72:75], v[156:159]\n" \
  "v_mfma_f32_16x16x32_bf16 v[148:151], v[32:35], v[72:75], v[148:151]\n" \
  "v_mfma_f32_16x16x32_bf16 v[160:163], v[40:43], v[76:79], v[160:163]\n" \
  "v_mfma_f32_16x16x32_bf16 v[156:159], v[36:39], v[76:79], v[156:159]\n" \
  "v_mfma_f32_16x16x32_bf16 v[148:151], v[28:31], v[76:79], v[148:151]\n" \
  "v_mfma_f32_16x16x32_bf16 v[160:163], v[36:39], v[80:83], v[160:163]\n" \
  "v_mfma_f32_16x16x32_bf16 v[156:159], v[32:35], v[80:83], v[156:159]\n" \
  "v_mfma_f32_16x16x32_bf16 v[160:163], v[32:35], v[84:87], v[160:163]\n" \
  "v_mfma_f32_16x16x32_bf16 v[156:159], v[28:31], v[84:87], v[156:159]\n" \
  "v_mfma_f32_16x16x32_bf16 v[160:163], v[28:31], v[88:91], v[160:163]\n" \
  "s_nop 3\n" \
  "ds_read_b64 v[28:29], %[ue] offset:2048\n" \
  "ds_read_b64 v[30:31], %[ue] offset:3072\n" \
  "ds_read_b64 v[32:33], %[ue] offset:3584\n" \
  "s_waitcnt lgkmcnt(2)\n" \
  "v_and_b32 v92, 0xffff0000, v29\n" \
  "v_lshlrev_b32 v93, 16, v29\n" \
  "v_and_b32 v94, 0xffff0000, v28\n" \
  "v_lshlrev_b32 v95, 16, v28\n" \
  "v_fmac_f32 v148, %[d1], v92\n" \
  "v_fmac_f32 v149, %[d1], v93\n" \
  "v_fmac_f32 v150, %[d1], v94\n" \
  "v_fmac_f32 v151, %[d1], v95\n" \
  "v_cvt_pk_bf16_f32 v148, v148, v149\n" \
  "v_cvt_pk_bf16_f32 v149, v150, v151\n" \
  "ds_write_b64 %[zw], v[148:149]\n" \
  "ds_read_b128 v[60:63], %[zr]\n" \
  "s_waitcnt lgkmcnt(3)\n" \
  "v_and_b32 v92, 0xffff0000, v31\n" \
  "v_lshlrev_b32 v93, 16, v31\n" \
  "v_and_b32 v94, 0xffff0000, v30\n" \
  "v_lshlrev_b32 v95, 16, v30\n" \
  "v_fmac_f32 v156, %[d1], v92\n" \
  "v_fmac_f32 v157, %[d1], v93\n" \
  "v_fmac_f32 v158, %[d1], v94\n" \
  "v_fmac_f32 v159, %[d1], v95\n" \
  "v_cvt_pk_bf16_f32 v156, v156, v157\n" \
  "v_cvt_pk_bf16_f32 v157, v158, v159\n" \
  "ds_write_b64 %[zw], v[156:157]\n" \
  "ds_read_b128 v[64:67], %[zr]\n" \
  "s_waitcnt lgkmcnt(4)\n" \
  "v_and_b32 v92, 0xffff0000, v33\n" \
  "v_lshlrev_b32 v93, 16, v33\n" \
  "v_and_b32 v94, 0xffff0000, v32\n" \
  "v_lshlrev_b32 v95, 16, v32\n" \
  "v_fmac_f32 v160, %[d1], v92\n" \
  "v_fmac_f32 v161, %[d1], v93\n" \
  "v_fmac_f32 v162, %[d1], v94\n" \
  "v_fmac_f32 v163, %[d1], v95\n" \
  "v_cvt_pk_bf16_f32 v160, v160, v161\n" \
  "v_cvt_pk_bf16_f32 v161, v162, v163\n" \
  "ds_write_b64 %[zw], v[160:161]\n" \
  "ds_read_b128 v[68:71], %[zr]\n" \
  "s_mov_b32 exec_hi, 0\n" \
  "s_waitcnt lgkmcnt(4)\n" \
  "buffer_store_dwordx4 v[60:63], %[zo], %[rs], %[z1] offen offset:2048 sc1\n" \
  "s_waitcnt lgkmcnt(2)\n" \
  "buffer_store_dwordx4 v[64:67], %[zo], %[rs], %[z1] offen offset:3072 sc1\n" \
  "s_waitcnt lgkmcnt(0)\n" \
  "buffer_store_dwordx4 v[68:71], %[zo], %[rs], %[z1] offen offset:3584 sc1\n" \
  "s_mov_b64 exec, -1\n" \
  "s_branch 99f\n" \
  "61:\n" \
  "ds_read2_b32 v[48:49], %[a2] offset0:128 offset1:129\n" \
  "ds_read2_b32 v[50:51], %[a2] offset0:130 offset1:131\n" \
  "ds_read_b128 v[60:63], %[b] offset:448\n" \
  "ds_read2_b32 v[108:109], %[c2] offset0:0 offset1:1\n" \
  "ds_read2_b32 v[110:111], %[c2] offset0:2 offset1:3\n" \
  "ds_read_b128 v[112:115], %[d] offset:448\n" \
  "ds_read2_b32 v[40:41], %[a1] offset0:128 offset1:129\n" \
  "ds_read2_b32 v[42:43], %[a1] offset0:130 offset1:131\n" \
  "ds_read2_b32 v[36:37], %[a1] offset0:0 offset1:1\n" \
  "ds_read2_b32 v[38:39], %[a1] offset0:2 offset1:3\n" \
  "ds_read2_b32 v[32:33], %[a0] offset0:128 offset1:129\n" \
  "ds_read2_b32 v[34:35], %[a0] offset0:130 offset1:131\n" \
  "ds_read2_b32 v[28:29], %[a0] offset0:0 offset1:1\n" \
  "ds_read2_b32 v[30:31], %[a0] offset0:2 offset1:3\n" \
  "ds_read2_b32 v[44:45], %[a2] offset0:0 offset1:1\n" \
  "ds_read2_b32 v[46:47], %[a2] offset0:2 offset1:3\n" \
  "ds_read_b128 v[64:67], %[b] offset:960\n" \
  "ds_read2_b32 v[104:105], %[c1] offset0:128 offset1:129\n" \
  "ds_read2_b32 v[106:107], %[c1] offset0:130 offset1:131\n" \
  "ds_read_b128 v[116:119], %[d] offset:960\n" \
  "ds_read_b128 v[68:71], %[b] offset:1472\n" \
  "ds_read2_b32 v[100:101], %[c1] offset0:0 offset1:1\n" \
  "ds_read2_b32 v[102:103], %[c1] offset0:2 offset1:3\n" \
  "ds_read_b128 v[120:123], %[d] offset:1472\n" \
  "ds_read_b128 v[72:75], %[b] offset:1984\n" \
  "ds_read2_b32 v[96:97], %[c0] offset0:128 offset1:129\n" \
  "ds_read2_b32 v[98:99], %[c0] offset0:130 offset1:131\n" \
  "ds_read_b128 v[124:127], %[d] offset:1984\n" \
  "ds_read_b128 v[76:79], %[b] offset:2496\n" \
  "ds_read2_b32 v[92:93], %[c0] offset0:0 offset1:1\n" \
  "ds_read2_b32 v[94:95], %[c0] offset0:2 offset1:3\n" \
  "ds_read_b128 v[128:131], %[d] offset:2496\n" \
  "ds_read_b128 v[80:83], %[b] offset:3008\n" \
  "s_waitcnt lgkmcnt(0)\n" \
  "v_mfma_f32_16x16x32_bf16 v[152:155], v[48:51], v[60:63], 0\n" \
  "ds_read2_b32 v[48:49], %[a2] offset0:144 offset1:145\n" \
  "ds_read2_b32 v[50:51], %[a2] offset0:146 offset1:147\n" \
  "v_mfma_f32_16x16x32_bf16 v[148:151], v[108:111], v[112:115], 0\n" \
  "ds_read2_b32 v[108:109], %[c2] offset0:16 offset1:17\n" \
  "ds_read2_b32 v[110:111], %[c2] offset0:18 offset1:19\n" \
  "ds_read_b128 v[112:115], %[d] offset:384\n" \
  "v_mfma_f32_16x16x32_bf16 v[144:147], v[40:43], v[60:63], 0\n" \
  "v_mfma_f32_16x16x32_bf16 v[140:143], v[36:39], v[60:63], 0\n" \
  "v_mfma_f32_16x16x32_bf16 v[136:139], v[32:35], v[60:63], 0\n" \
  "v_mfma_f32_16x16x32_bf16 v[132:135], v[28:31], v[60:63], 0\n" \
  "ds_read_b128 v[60:63], %[b] offset:384\n" \
  "v_mfma_f32_16x16x32_bf16 v[152:155], v[44:47], v[64:67], v[152:155]\n" \
  "ds_read2_b32 v[44:45], %[a2] offset0:16 offset1:17\n" \
  "ds_read2_b32 v[46:47], %[a2] offset0:18 offset1:19\n" \
  "v_mfma_f32_16x16x32_bf16 v[148:151], v[104:107], v[116:119], v[148:151]\n" \
  "ds_read2_b32 v[104:105], %[c1] offset0:144 offset1:145\n" \
  "ds_read2_b32 v[106:107], %[c1] offset0:146 offset1:147\n" \
  "ds_read_b128 v[116:119], %[d] offset:896\n" \
  "v_mfma_f32_16x16x32_bf16 v[144:147], v[36:39], v[64:67], v[144:147]\n" \
  "v_mfma_f32_16x16x32_bf16 v[140:143], v[32:35], v[64:67], v[140:143]\n" \
  "v_mfma_f32_16x16x32_bf16 v[136:139], v[28:31], v[64:67], v[136:139]\n" \
  "ds_read_b128 v[64:67], %[b] offset:896\n" \
  "v_mfma_f32_16x16x32_bf16 v[152:155], v[40:43], v[68:71], v[152:155]\n" \
  "ds_read2_b32 v[40:41], %[a1] offset0:144 offset1:145\n" \
  "ds_read2_b32 v[42:43], %[a1] offset0:146 offset1:147\n" \
  "v_mfma_f32_16x16x32_bf16 v[148:151], v[100:103], v[120:123], v[148:151]\n" \
  "ds_read2_b32 v[100:101], %[c1] offset0:16 offset1:17\n" \
  "ds_read2_b32 v[102:103], %[c1] offset0:18 offset1:19\n" \
  "ds_read_b128 v[120:123], %[d] offset:1408\n" \
  "v_mfma_f32_16x16x32_bf16 v[144:147], v[32:35], v[68:71], v[144:147]\n" \
  "v_mfma_f32_16x16x32_bf16 v[140:143], v[28:31], v[68:71], v[140:143]\n" \
  "ds_read_b128 v[68:71], %[b] offset:1408\n" \
  "v_mfma_f32_16x16x32_bf16 v[152:155], v[36:39], v[72:75], v[152:155]\n" \
  "ds_read2_b32 v[36:37], %[a1] offset0:16 offset1:17\n" \
  "ds_read2_b32 v[38:39], %[a1] offset0:18 offset1:19\n" \
  "v_mfma_f32_16x16x32_bf16 v[148:151], v[96:99], v[124:127], v[148:151]\n" \
  "ds_read2_b32 v[96:97], %[c0] offset0:144 offset1:145\n" \
  "ds_read2_b32 v[98:99], %[c0] offset0:146 offset1:147\n" \
  "ds_read_b128 v[124:127], %[d] offset:1920\n" \
  "v_mfma_f32_16x16x32_bf16 v[144:147], v[28:31], v[72:75], v[144:147]\n" \
  "ds_read_b128 v[72:75], %[b] offset:1920\n" \
  "v_mfma_f32_16x16x32_bf16 v[152:155], v[32:35], v[76:79], v[152:155]\n" \
  "ds_read2_b32 v[32:33], %[a0] offset0:144 offset1:145\n" \
  "ds_read2_b32 v[34:35], %[a0] offset0:146 offset1:147\n" \
  "ds_read_b128 v[76:79], %[b] offset:2432\n" \
  "v_mfma_f32_16x16x32_bf16 v[148:151], v[92:95], v[128:131], v[148:151]\n" \
  "ds_read2_b32 v[92:93], %[c0] offset0:16 offset1:17\n" \
  "ds_read2_b32 v[94:95], %[c0] offset0:18 offset1:19\n" \
  "ds_read_b128 v[128:131], %[d] offset:2432\n" \
  "v_mfma_f32_16x16x32_bf16 v[152:155], v[28:31], v[80:83], v[152:155]\n" \
  "ds_read2_b32 v[28:29], %[a0] offset0:16 offset1:17\n" \
  "ds_read2_b32 v[30:31], %[a0] offset0:18 offset1:19\n" \
  "ds_read_b128 v[80:83], %[b] offset:2944\n" \
  "81:\n" \
  "s_waitcnt lgkmcnt(0)\n" \
  "v_mfma_f32_16x16x32_bf16 v[152:155], v[48:51], v[60:63], v[152:155]\n" \
  "ds_read2_b32 v[48:49], %[a2] offset0:160 offset1:161\n" \
  "ds_read2_b32 v[50:51], %[a2] offset0:162 offset1:163\n" \
  "v_mfma_f32_16x16x32_bf16 v[148:151], v[108:111], v[112:115], v[148:151]\n" \
  "ds_read2_b32 v[108:109], %[c2] offset0:32 offset1:33\n" \
  "ds_read2_b32 v[110:111], %[c2] offset0:34 offset1:35\n" \
  "ds_read_b128 v[112:115], %[d] offset:320\n" \
  "v_mfma_f32_16x16x32_bf16 v[144:147], v[40:43], v[60:63], v[144:147]\n" \
  "v_mfma_f32_16x16x32_bf16 v[140:143], v[36:39], v[60:63], v[140:143]\n" \
  "v_mfma_f32_16x16x32_bf16 v[136:139], v[32:35], v[60:63], v[136:139]\n" \
  "v_mfma_f32_16x16x32_bf16 v[132:135], v[28:31], v[60:63], v[132:135]\n" \
  "ds_read_b128 v[60:63], %[b] offset:320\n" \
  "v_mfma_f32_16x16x32_bf16 v[152:155], v[44:47], v[64:67], v[152:155]\n" \
  "ds_read2_b32 v[44:45], %[a2] offset0:32 offset1:33\n" \
  "ds_read2_b32 v[46:47], %[a2] offset0:34 offset1:35\n" \
  "v_mfma_f32_16x16x32_bf16 v[148:151], v[104:107], v[116:119], v[148:151]\n" \
  "ds_read2_b32 v[104:105], %[c1] offset0:160 offset1:161\n" \
  "ds_read2_b32 v[106:107], %[c1] offset0:162 offset1:163\n" \
  "ds_read_b128 v[116:119], %[d] offset:832\n" \
  "v_mfma_f32_16x16x32_bf16 v[144:147], v[36:39], v[64:67], v[144:147]\n" \
  "v_mfma_f32_16x16x32_bf16 v[140:143], v[32:35], v[64:67], v[140:143]\n" \
  "v_mfma_f32_16x16x32_bf16 v[136:139], v[28:31], v[64:67], v[136:139]\n" \
  "ds_read_b128 v[64:67], %[b] offset:832\n" \
  "v_mfma_f32_16x16x32_bf16 v[152:155], v[40:43], v[68:71], v[152:155]\n" \
  "ds_read2_b32 v[40:41], %[a1] offset0:160 offset1:161\n" \
  "ds_read2_b32 v[42:43], %[a1] offset0:162 offset1:163\n" \
  "v_mfma_f32_16x16x32_bf16 v[148:151], v[100:103], v[120:123], v[148:151]\n" \
  "ds_read2_b32 v[100:101], %[c1] offset0:32 offset1:33\n" \
  "ds_read2_b32 v[102:103], %[c1] offset0:34 offset1:35\n" \
  "ds_read_b128 v[120:123], %[d] offset:1344\n" \
  "v_mfma_f32_16x16x32_bf16 v[144:147], v[32:35], v[68:71], v[144:147]\n" \
  "v_mfma_f32_16x16x32_bf16 v[140:143], v[28:31], v[68:71], v[140:143]\n" \
  "ds_read_b128 v[68:71], %[b] offset:1344\n" \
  "v_mfma_f32_16x16x32_bf16 v[152:155], v[36:39], v[72:75], v[152:155]\n" \
  "ds_read2_b32 v[36:37], %[a1] offset0:32 offset1:33\n" \
  "ds_read2_b32 v[38:39], %[a1] offset0:34 offset1:35\n" \
  "v_mfma_f32_16x16x32_bf16 v[148:151], v[96:99], v[124:127], v[148:151]\n" \
  "ds_read2_b32 v[96:97], %[c0] offset0:160 offset1:161\n" \
  "ds_read2_b32 v[98:99], %[c0] offset0:162 offset1:163\n" \
  "ds_read_b128 v[124:127], %[d] offset:1856\n" \
  "v_mfma_f32_16x16x32_bf16 v[144:147], v[28:31], v[72:75], v[144:147]\n" \
  "ds_read_b128 v[72:75], %[b] offset:1856\n" \
  "v_mfma_f32_16x16x32_bf16 v[152:155], v[32:35], v[76:79], v[152:155]\n" \
  "ds_read2_b32 v[32:33], %[a0] offset0:160 offset1:161\n" \
  "ds_read2_b32 v[34:35], %[a0] offset0:162 offset1:163\n" \
  "ds_read_b128 v[76:79], %[b] offset:2368\n" \
  "v_mfma_f32_16x16x32_bf16 v[148:151], v[92:95], v[128:131], v[148:151]\n" \
  "ds_read2_b32 v[92:93], %[c0] offset0:32 offset1:33\n" \
  "ds_read2_b32 v[94:95], %[c0] offset0:34 offset1:35\n" \
  "ds_read_b128 v[128:131], %[d] offset:2368\n" \
  "v_mfma_f32_16x16x32_bf16 v[152:155], v[28:31], v[80:83], v[152:155]\n" \
  "ds_read2_b32 v[28:29], %[a0] offset0:32 offset1:33\n" \
  "ds_read2_b32 v[30:31], %[a0] offset0:34 offset1:35\n" \
  "ds_read_b128 v[80:83], %[b] offset:2880\n" \
  "s_waitcnt lgkmcnt(0)\n" \
  "v_mfma_f32_16x16x32_bf16 v[152:155], v[48:51], v[60:63], v[152:155]\n" \
  "ds_read2_b32 v[48:49], %[a2] offset0:176 offset1:177\n" \
  "ds_read2_b32 v[50:51], %[a2] offset0:178 offset1:179\n" \
  "v_mfma_f32_16x16x32_bf16 v[148:151], v[108:111], v[112:115], v[148:151]\n" \
  "ds_read2_b32 v[108:109], %[c2] offset0:48 offset1:49\n" \
  "ds_read2_b32 v[110:111], %[c2] offset0:50 offset1:51\n" \
  "ds_read_b128 v[112:115], %[d] offset:256\n" \
  "v_mfma_f32_16x16x32_bf16 v[144:147], v[40:43], v[60:63], v[144:147]\n" \
  "v_mfma_f32_16x16x32_bf16 v[140:143], v[36:39], v[60:63], v[140:143]\n" \
  "v_mfma_f32_16x16x32_bf16 v[136:139], v[32:35], v[60:63], v[136:139]\n" \
  "v_mfma_f32_16x16x32_bf16 v[132:135], v[28:31], v[60:63], v[132:135]\n" \
  "ds_read_b128 v[60:63], %[b] offset:256\n" \
  "v_mfma_f32_16x16x32_bf16 v[152:155], v[44:47], v[64:67], v[152:155]\n" \
  "ds_read2_b32 v[44:45], %[a2] offset0:48 offset1:49\n" \
  "ds_read2_b32 v[46:47], %[a2] offset0:50 offset1:51\n" \
  "v_mfma_f32_16x16x32_bf16 v[148:151], v[104:107], v[116:119], v[148:151]\n" \
  "ds_read2_b32 v[104:105], %[c1] offset0:176 offset1:177\n" \
  "ds_read2_b32 v[106:107], %[c1] offset0:178 offset1:179\n" \
  "ds_read_b128 v[116:119], %[d] offset:768\n" \
  "v_mfma_f32_16x16x32_bf16 v[144:147], v[36:39], v[64:67], v[144:147]\n" \
  "v_mfma_f32_16x16x32_bf16 v[140:143], v[32:35], v[64:67], v[140:143]\n" \
  "v_mfma_f32_16x16x32_bf16 v[136:139], v[28:31], v[64:67], v[136:139]\n" \
  "ds_read_b128 v[64:67], %[b] offset:768\n" \
  "v_mfma_f32_16x16x32_bf16 v[152:155], v[40:43], v[68:71], v[152:155]\n" \
  "ds_read2_b32 v[40:41], %[a1] offset0:176 offset1:177\n" \
  "ds_read2_b32 v[42:43], %[a1] offset0:178 offset1:179\n" \
  "v_mfma_f32_16x16x32_bf16 v[148:151], v[100:103], v[120:123], v[148:151]\n" \
  "ds_read2_b32 v[100:101], %[c1] offset0:48 offset1:49\n" \
  "ds_read2_b32 v[102:103], %[c1] offset0:50 offset1:51\n" \
  "ds_read_b128 v[120:123], %[d] offset:1280\n" \
  "v_mfma_f32_16x16x32_bf16 v[144:147], v[32:35], v[68:71], v[144:147]\n" \
  "v_mfma_f32_16x16x32_bf16 v[140:143], v[28:31], v[68:71], v[140:143]\n" \
  "ds_read_b128 v[68:71], %[b] offset:1280\n" \
  "v_mfma_f32_16x16x32_bf16 v[152:155], v[36:39], v[72:75], v[152:155]\n" \
  "ds_read2_b32 v[36:37], %[a1] offset0:48 offset1:49\n" \
  "ds_read2_b32 v[38:39], %[a1] offset0:50 offset1:51\n" \
  "v_mfma_f32_16x16x32_bf16 v[148:151], v[96:99], v[124:127], v[148:151]\n" \
  "ds_read2_b32 v[96:97], %[c0] offset0:176 offset1:177\n" \
  "ds_read2_b32 v[98:99], %[c0] offset0:178 offset1:179\n" \
  "ds_read_b128 v[124:127], %[d] offset:1792\n" \
  "v_mfma_f32_16x16x32_bf16 v[144:147], v[28:31], v[72:75], v[144:147]\n" \
  "ds_read_b128 v[72:75], %[b] offset:1792\n" \
  "v_mfma_f32_16x16x32_bf16 v[152:155], v[32:35], v[76:79], v[152:155]\n" \
  "ds_read2_b32 v[32:33], %[a0] offset0:176 offset1:177\n" \
  "ds_read2_b32 v[34:35], %[a0] offset0:178 offset1:179\n" \
  "ds_read_b128 v[76:79], %[b] offset:2304\n" \
  "v_mfma_f32_16x16x32_bf16 v[148:151], v[92:95], v[128:131], v[148:151]\n" \
  "ds_read2_b32 v[92:93], %[c0] offset0:48 offset1:49\n" \
  "ds_read2_b32 v[94:95], %[c0] offset0:50 offset1:51\n" \
  "ds_read_b128 v[128:131], %[d] offset:2304\n" \
  "v_mfma_f32_16x16x32_bf16 v[152:155], v[28:31], v[80:83], v[152:155]\n" \
  "ds_read2_b32 v[28:29], %[a0] offset0:48 offset1:49\n" \
  "ds_read2_b32 v[30:31], %[a0] offset0:50 offset1:51\n" \
  "ds_read_b128 v[80:83], %[b] offset:2816\n" \
  "v_add_u32 %[a0], 128, %[a0]\n" \
  "v_add_u32 %[a1], 128, %[a1]\n" \
  "v_add_u32 %[a2], 128, %[a2]\n" \
  "v_subrev_u32 %[b], 128, %[b]\n" \
  "v_add_u32 %[c0], 128, %[c0]\n" \
  "v_add_u32 %[c1], 128, %[c1]\n" \
  "v_add_u32 %[c2], 128, %[c2]\n" \
  "v_subrev_u32 %[d], 128, %[d]\n" \
  "s_sub_u32 %[cnt], %[cnt], 1\n" \
  "s_cmp_lg_u32 %[cnt], 0\n" \
  "s_cbranch_scc1 81b\n" \
  "s_waitcnt lgkmcnt(0)\n" \
  "v_mfma_f32_16x16x32_bf16 v[152:155], v[48:51], v[60:63], v[152:155]\n" \
  "v_mfma_f32_16x16x32_bf16 v[148:151], v[108:111], v[112:115], v[148:151]\n" \
  "v_mfma_f32_16x16x32_bf16 v[144:147], v[40:43], v[60:63], v[144:147]\n" \
  "v_mfma_f32_16x16x32_bf16 v[140:143], v[36:39], v[60:63], v[140:143]\n" \
  "v_mfma_f32_16x16x32_bf16 v[136:139], v[32:35], v[60:63], v[136:139]\n" \
  "v_mfma_f32_16x16x32_bf16 v[132:135], v[28:31], v[60:63], v[132:135]\n" \
  "v_mfma_f32_16x16x32_bf16 v[152:155], v[44:47], v[64:67], v[152:155]\n" \
  "v_mfma_f32_16x16x32_bf16 v[148:151], v[104:107], v[116:119], v[148:151]\n" \
  "v_mfma_f32_16x16x32_bf16 v[144:147], v[36:39], v[64:67], v[144:147]\n" \
  "v_mfma_f32_16x16x32_bf16 v[140:143], v[32:35], v[64:67], v[140:143]\n" \
  "v_mfma_f32_16x16x32_bf16 v[136:139], v[28:31], v[64:67], v[136:139]\n" \
  "v_mfma_f32_16x16x32_bf16 v[152:155], v[40:43], v[68:71], v[152:155]\n" \
  "v_mfma_f32_16x16x32_bf16 v[148:151], v[100:103], v[120:123], v[148:151]\n" \
  "v_mfma_f32_16x16x32_bf16 v[144:147], v[32:35], v[68:71], v[144:147]\n" \
  "v_mfma_f32_16x16x32_bf16 v[140:143], v[28:31], v[68:71], v[140:143]\n" \
  "v_mfma_f32_16x16x32_bf16 v[152:155], v[36:39], v[72:75], v[152:155]\n" \
  "v_mfma_f32_16x16x32_bf16 v[148:151], v[96:99], v[124:127], v[148:151]\n" \
  "v_mfma_f32_16x16x32_bf16 v[144:147], v[28:31], v[72:75], v[144:147]\n" \
  "v_mfma_f32_16x16x32_bf16 v[152:155], v[32:35], v[76:79], v[152:155]\n" \
  "v_mfma_f32_16x16x32_bf16 v[148:151], v[92:95], v[128:131], v[148:151]\n" \
  "v_mfma_f32_16x16x32_bf16 v[152:155], v[28:31], v[80:83], v[152:155]\n" \
  "s_nop 3\n" \
  "ds_read_b64 v[28:29], %[ue] offset:0\n" \
  "ds_read_b64 v[30:31], %[ue] offset:512\n" \
  "ds_read_b64 v[32:33], %[ue] offset:1024\n" \
  "ds_read_b64 v[34:35], %[ue] offset:1536\n" \
  "ds_read_b64 v[36:37], %[ue] offset:2560\n" \
  "ds_read_b64 v[38:39], %[uf] offset:2048\n" \
  "s_waitcnt lgkmcnt(5)\n" \
  "v_and_b32 v92, 0xffff0000, v29\n" \
  "v_lshlrev_b32 v93, 16, v29\n" \
  "v_and_b32 v94, 0xffff0000, v28\n" \
  "v_lshlrev_b32 v95, 16, v28\n" \
  "v_fmac_f32 v132, %[d1], v92\n" \
  "v_fmac_f32 v133, %[d1], v93\n" \
  "v_fmac_f32 v134, %[d1], v94\n" \
  "v_fmac_f32 v135, %[d1], v95\n" \
  "v_cvt_pk_bf16_f32 v132, v132, v133\n" \
  "v_cvt_pk_bf16_f32 v133, v134, v135\n" \
  "ds_write_b64 %[zw], v[132:133]\n" \
  "ds_read_b128 v[60:63], %[zr]\n" \
  "s_waitcnt lgkmcnt(6)\n" \
  "v_and_b32 v92, 0xffff0000, v31\n" \
  "v_lshlrev_b32 v93, 16, v31\n" \
  "v_and_b32 v94, 0xffff0000, v30\n" \
  "v_lshlrev_b32 v95, 16, v30\n" \
  "v_fmac_f32 v136, %[d1], v92\n" \
  "v_fmac_f32 v137, %[d1], v93\n" \
  "v_fmac_f32 v138, %[d1], v94\n" \
  "v_fmac_f32 v139, %[d1], v95\n" \
  "v_cvt_pk_bf16_f32 v136, v136, v137\n" \
  "v_cvt_pk_bf16_f32 v137, v138, v139\n" \
  "ds_write_b64 %[zw], v[136:137]\n" \
  "ds_read_b128 v[64:67], %[zr]\n" \
  "s_waitcnt lgkmcnt(7)\n" \
  "v_and_b32 v92, 0xffff0000, v33\n" \
  "v_lshlrev_b32 v93, 16, v33\n" \
  "v_and_b32 v94, 0xffff0000, v32\n" \
  "v_lshlrev_b32 v95, 16, v32\n" \
  "v_fmac_f32 v140, %[d1], v92\n" \
  "v_fmac_f32 v141, %[d1], v93\n" \
  "v_fmac_f32 v142, %[d1], v94\n" \
  "v_fmac_f32 v143, %[d1], v95\n" \
  "v_cvt_pk_bf16_f32 v140, v140, v141\n" \
  "v_cvt_pk_bf16_f32 v141, v142, v143\n" \
  "ds_write_b64 %[zw], v[140:141]\n" \
  "ds_read_b128 v[68:71], %[zr]\n" \
  "s_waitcnt lgkmcnt(8)\n" \
  "v_and_b32 v92, 0xffff0000, v35\n" \
  "v_lshlrev_b32 v93, 16, v35\n" \
  "v_and_b32 v94, 0xffff0000, v34\n" \
  "v_lshlrev_b32 v95, 16, v34\n" \
  "v_fmac_f32 v144, %[d1], v92\n" \
  "v_fmac_f32 v145, %[d1], v93\n" \
  "v_fmac_f32 v146, %[d1], v94\n" \
  "v_fmac_f32 v147, %[d1], v95\n" \
  "v_cvt_pk_bf16_f32 v144, v144, v145\n" \
  "v_cvt_pk_bf16_f32 v145, v146, v147\n" \
  "ds_write_b64 %[zw], v[144:145]\n" \
  "ds_read_b128 v[72:75], %[zr]\n" \
  "s_waitcnt lgkmcnt(9)\n" \
  "v_and_b32 v92, 0xffff0000, v37\n" \
  "v_lshlrev_b32 v93, 16, v37\n" \
  "v_and_b32 v94, 0xffff0000, v36\n" \
  "v_lshlrev_b32 v95, 16, v36\n" \
  "v_fmac_f32 v152, %[d1], v92\n" \
  "v_fmac_f32 v153, %[d1], v93\n" \
  "v_fmac_f32 v154, %[d1], v94\n" \
  "v_fmac_f32 v155, %[d1], v95\n" \
  "v_cvt_pk_bf16_f32 v152, v152, v153\n" \
  "v_cvt_pk_bf16_f32 v153, v154, v155\n" \
  "ds_write_b64 %[zw], v[152:153]\n" \
  "ds_read_b128 v[76:79], %[zr]\n" \
  "s_waitcnt lgkmcnt(10)\n" \
  "v_and_b32 v92, 0xffff0000, v39\n" \
  "v_lshlrev_b32 v93, 16, v39\n" \
  "v_and_b32 v94, 0xffff0000, v38\n" \
  "v_lshlrev_b32 v95, 16, v38\n" \
  "v_fmac_f32 v148, %[d2], v92\n" \
  "v_fmac_f32 v149, %[d2], v93\n" \
  "v_fmac_f32 v150, %[d2], v94\n" \
  "v_fmac_f32 v151, %[d2], v95\n" \
  "v_cvt_pk_bf16_f32 v148, v148, v149\n" \
  "v_cvt_pk_bf16_f32 v149, v150, v151\n" \
  "ds_write_b64 %[zw], v[148:149]\n" \
  "ds_read_b128 v[80:83], %[zr]\n" \
  "s_mov_b32 exec_hi, 0\n" \
  "s_waitcnt lgkmcnt(10)\n" \
  "buffer_store_dwordx4 v[60:63], %[zo], %[rs], %[z1] offen offset:0 sc1\n" \
  "s_waitcnt lgkmcnt(8)\n" \
  "buffer_store_dwordx4 v[64:67], %[zo], %[rs], %[z1] offen offset:512 sc1\n" \
  "s_waitcnt lgkmcnt(6)\n" \
  "buffer_store_dwordx4 v[68:71], %[zo], %[rs], %[z1] offen offset:1024 sc1\n" \
  "s_waitcnt lgkmcnt(4)\n" \
  "buffer_store_dwordx4 v[72:75], %[zo], %[rs], %[z1] offen offset:1536 sc1\n" \
  "s_waitcnt lgkmcnt(2)\n" \
  "buffer_store_dwordx4 v[76:79], %[zo], %[rs], %[z1] offen offset:2560 sc1\n" \
  "s_waitcnt lgkmcnt(0)\n" \
  "buffer_store_dwordx4 v[80:83], %[zo], %[rs], %[z2] offen offset:2048 sc1\n" \
  "s_mov_b64 exec, -1\n" \
  "s_branch 99f\n" \
  "62:\n" \
  "ds_read2_b32 v[56:57], %[a3] offset0:128 offset1:129\n" \
  "ds_read2_b32 v[58:59], %[a3] offset0:130 offset1:131\n" \
  "ds_read_b128 v[60:63], %[b] offset:448\n" \
  "ds_read2_b32 v[52:53], %[a3] offset0:0 offset1:1\n" \
  "ds_read2_b32 v[54:55], %[a3] offset0:2 offset1:3\n" \
  "ds_read2_b32 v[40:41], %[a1] offset0:128 offset1:129\n" \
  "ds_read2_b32 v[42:43], %[a1] offset0:130 offset1:131\n" \
  "ds_read_b128 v[64:67], %[b] offset:960\n" \
  "ds_read2_b32 v[48:49], %[a2] offset0:128 offset1:129\n" \
  "ds_read2_b32 v[50:51], %[a2] offset0:130 offset1:131\n" \
  "ds_read2_b32 v[36:37], %[a1] offset0:0 offset1:1\n" \
  "ds_read2_b32 v[38:39], %[a1] offset0:2 offset1:3\n" \
  "ds_read_b128 v[68:71], %[b] offset:1472\n" \
  "ds_read2_b32 v[44:45], %[a2] offset0:0 offset1:1\n" \
  "ds_read2_b32 v[46:47], %[a2] offset0:2 offset1:3\n" \
  "ds_read2_b32 v[32:33], %[a0] offset0:128 offset1:129\n" \
  "ds_read2_b32 v[34:35], %[a0] offset0:130 offset1:131\n" \
  "ds_read_b128 v[72:75], %[b] offset:1984\n" \
  "ds_read2_b32 v[28:29], %[a0] offset0:0 offset1:1\n" \
  "ds_read2_b32 v[30:31], %[a0] offset0:2 offset1:3\n" \
  "ds_read_b128 v[76:79], %[b] offset:2496\n" \
  "ds_read_b128 v[80:83], %[b] offset:3008\n" \
  "ds_read_b128 v[84:87], %[b] offset:3520\n" \
  "ds_read_b128 v[88:91], %[b] offset:4032\n" \
  "s_waitcnt lgkmcnt(0)\n" \
  "v_mfma_f32_16x16x32_bf16 v[160:163], v[56:59], v[60:63], 0\n" \
  "ds_read2_b32 v[56:57], %[a3] offset0:144 offset1:145\n" \
  "ds_read2_b32 v[58:59], %[a3] offset0:146 offset1:147\n" \
  "v_mfma_f32_16x16x32_bf16 v[156:159], v[52:55], v[60:63], 0\n" \
  "v_mfma_f32_16x16x32_bf16 v[144:147], v[40:43], v[60:63], 0\n" \
  "ds_read_b128 v[60:63], %[b] offset:384\n" \
  "v_mfma_f32_16x16x32_bf16 v[160:163], v[52:55], v[64:67], v[160:163]\n" \
  "ds_read2_b32 v[52:53], %[a3] offset0:16 offset1:17\n" \
  "ds_read2_b32 v[54:55], %[a3] offset0:18 offset1:19\n" \
  "v_mfma_f32_16x16x32_bf16 v[156:159], v[48:51], v[64:67], v[156:159]\n" \
  "v_mfma_f32_16x16x32_bf16 v[144:147], v[36:39], v[64:67], v[144:147]\n" \
  "ds_read_b128 v[64:67], %[b] offset:896\n" \
  "v_mfma_f32_16x16x32_bf16 v[160:163], v[48:51], v[68:71], v[160:163]\n" \
  "ds_read2_b32 v[48:49], %[a2] offset0:144 offset1:145\n" \
  "ds_read2_b32 v[50:51], %[a2] offset0:146 offset1:147\n" \
  "v_mfma_f32_16x16x32_bf16 v[156:159], v[44:47], v[68:71], v[156:159]\n" \
  "v_mfma_f32_16x16x32_bf16 v[144:147], v[32:35], v[68:71], v[144:147]\n" \
  "ds_read_b128 v[68:71], %[b] offset:1408\n" \
  "v_mfma_f32_16x16x32_bf16 v[160:163], v[44:47], v[72:75], v[160:163]\n" \
  "ds_read2_b32 v[44:45], %[a2] offset0:16 offset1:17\n" \
  "ds_read2_b32 v[46:47], %[a2] offset0:18 offset1:19\n" \
  "v_mfma_f32_16x16x32_bf16 v[156:159], v[40:43], v[72:75], v[156:159]\n" \
  "v_mfma_f32_16x16x32_bf16 v[144:147], v[28:31], v[72:75], v[144:147]\n" \
  "ds_read_b128 v[72:75], %[b] offset:1920\n" \
  "v_mfma_f32_16x16x32_bf16 v[160:163], v[40:43], v[76:79], v[160:163]\n" \
  "ds_read2_b32 v[40:41], %[a1] offset0:144 offset1:145\n" \
  "ds_read2_b32 v[42:43], %[a1] offset0:146 offset1:147\n" \
  "v_mfma_f32_16x16x32_bf16 v[156:159], v[36:39], v[76:79], v[156:159]\n" \
  "ds_read_b128 v[76:79], %[b] offset:2432\n" \
  "v_mfma_f32_16x16x32_bf16 v[160:163], v[36:39], v[80:83], v[160:163]\n" \
  "ds_read2_b32 v[36:37], %[a1] offset0:16 offset1:17\n" \
  "ds_read2_b32 v[38:39], %[a1] offset0:18 offset1:19\n" \
  "v_mfma_f32_16x16x32_bf16 v[156:159], v[32:35], v[80:83], v[156:159]\n" \
  "ds_read_b128 v[80:83], %[b] offset:2944\n" \
  "v_mfma_f32_16x16x32_bf16 v[160:163], v[32:35], v[84:87], v[160:163]\n" \
  "ds_read2_b32 v[32:33], %[a0] offset0:144 offset1:145\n" \
  "ds_read2_b32 v[34:35], %[a0] offset0:146 offset1:147\n" \
  "v_mfma_f32_16x16x32_bf16 v[156:159], v[28:31], v[84:87], v[156:159]\n" \
  "ds_read_b128 v[84:87], %[b] offset:3456\n" \
  "v_mfma_f32_16x16x32_bf16 v[160:163], v[28:31], v[88:91], v[160:163]\n" \
  "ds_read2_b32 v[28:29], %[a0] offset0:16 offset1:17\n" \
  "ds_read2_b32 v[30:31], %[a0] offset0:18 offset1:19\n" \
  "ds_read_b128 v[88:91], %[b] offset:3968\n" \
  "82:\n" \
  "s_waitcnt lgkmcnt(0)\n" \
  "v_mfma_f32_16x16x32_bf16 v[160:163], v[56:59], v[60:63], v[160:163]\n" \
  "ds_read2_b32 v[56:57], %[a3] offset0:160 offset1:161\n" \
  "ds_read2_b32 v[58:59], %[a3] offset0:162 offset1:163\n" \
  "v_mfma_f32_16x16x32_bf16 v[156:159], v[52:55], v[60:63], v[156:159]\n" \
  "v_mfma_f32_16x16x32_bf16 v[144:147], v[40:43], v[60:63], v[144:147]\n" \
  "ds_read_b128 v[60:63], %[b] offset:320\n" \
  "v_mfma_f32_16x16x32_bf16 v[160:163], v[52:55], v[64:67], v[160:163]\n" \
  "ds_read2_b32 v[52:53], %[a3] offset0:32 offset1:33\n" \
  "ds_read2_b32 v[54:55], %[a3] offset0:34 offset1:35\n" \
  "v_mfma_f32_16x16x32_bf16 v[156:159], v[48:51], v[64:67], v[156:159]\n" \
  "v_mfma_f32_16x16x32_bf16 v[144:147], v[36:39], v[64:67], v[144:147]\n" \
  "ds_read_b128 v[64:67], %[b] offset:832\n" \
  "v_mfma_f32_16x16x32_bf16 v[160:163], v[48:51], v[68:71], v[160:163]\n" \
  "ds_read2_b32 v[48:49], %[a2] offset0:160 offset1:161\n" \
  "ds_read2_b32 v[50:51], %[a2] offset0:162 offset1:163\n" \
  "v_mfma_f32_16x16x32_bf16 v[156:159], v[44:47], v[68:71], v[156:159]\n" \
  "v_mfma_f32_16x16x32_bf16 v[144:147], v[32:35], v[68:71], v[144:147]\n" \
  "ds_read_b128 v[68:71], %[b] offset:1344\n" \
  "v_mfma_f32_16x16x32_bf16 v[160:163], v[44:47], v[72:75], v[160:163]\n" \
  "ds_read2_b32 v[44:45], %[a2] offset0:32 offset1:33\n" \
  "ds_read2_b32 v[46:47], %[a2] offset0:34 offset1:35\n" \
  "v_mfma_f32_16x16x32_bf16 v[156:159], v[40:43], v[72:75], v[156:159]\n" \
  "v_mfma_f32_16x16x32_bf16 v[144:147], v[28:31], v[72:75], v[144:147]\n" \
  "ds_read_b128 v[72:75], %[b] offset:1856\n" \
  "v_mfma_f32_16x16x32_bf16 v[160:163], v[40:43], v[76:79], v[160:163]\n" \
  "ds_read2_b32 v[40:41], %[a1] offset0:160 offset1:161\n" \
  "ds_read2_b32 v[42:43], %[a1] offset0:162 offset1:163\n" \
  "v_mfma_f32_16x16x32_bf16 v[156:159], v[36:39], v[76:79], v[156:159]\n" \
  "ds_read_b128 v[76:79], %[b] offset:2368\n" \
  "v_mfma_f32_16x16x32_bf16 v[160:163], v[36:39], v[80:83], v[160:163]\n" \
  "ds_read2_b32 v[36:37], %[a1] offset0:32 offset1:33\n" \
  "ds_read2_b32 v[38:39], %[a1] offset0:34 offset1:35\n" \
  "v_mfma_f32_16x16x32_bf16 v[156:159], v[32:35], v[80:83], v[156:159]\n" \
  "ds_read_b128 v[80:83], %[b] offset:2880\n" \
  "v_mfma_f32_16x16x32_bf16 v[160:163], v[32:35], v[84:87], v[160:163]\n" \
  "ds_read2_b32 v[32:33], %[a0] offset0:160 offset1:161\n" \
  "ds_read2_b32 v[34:35], %[a0] offset0:162 offset1:163\n" \
  "v_mfma_f32_16x16x32_bf16 v[156:159], v[28:31], v[84:87], v[156:159]\n" \
  "ds_read_b128 v[84:87], %[b] offset:3392\n" \
  "v_mfma_f32_16x16x32_bf16 v[160:163], v[28:31], v[88:91], v[160:163]\n" \
  "ds_read2_b32 v[28:29], %[a0] offset0:32 offset1:33\n" \
  "ds_read2_b32 v[30:31], %[a0] offset0:34 offset1:35\n" \
  "ds_read_b128 v[88:91], %[b] offset:3904\n" \
  "s_waitcnt lgkmcnt(0)\n" \
  "v_mfma_f32_16x16x32_bf16 v[160:163], v[56:59], v[60:63], v[160:163]\n" \
  "ds_read2_b32 v[56:57], %[a3] offset0:176 offset1:177\n" \
  "ds_read2_b32 v[58:59], %[a3] offset0:178 offset1:179\n" \
  "v_mfma_f32_16x16x32_bf16 v[156:159], v[52:55], v[60:63], v[156:159]\n" \
  "v_mfma_f32_16x16x32_bf16 v[144:147], v[40:43], v[60:63], v[144:147]\n" \
  "ds_read_b128 v[60:63], %[b] offset:256\n" \
  "v_mfma_f32_16x16x32_bf16 v[160:163], v[52:55], v[64:67], v[160:163]\n" \
  "ds_read2_b32 v[52:53], %[a3] offset0:48 offset1:49\n" \
  "ds_read2_b32 v[54:55], %[a3] offset0:50 offset1:51\n" \
  "v_mfma_f32_16x16x32_bf16 v[156:159], v[48:51], v[64:67], v[156:159]\n" \
  "v_mfma_f32_16x16x32_bf16 v[144:147], v[36:39], v[64:67], v[144:147]\n" \
  "ds_read_b128 v[64:67], %[b] offset:768\n" \
  "v_mfma_f32_16x16x32_bf16 v[160:163], v[48:51], v[68:71], v[160:163]\n" \
  "ds_read2_b32 v[48:49], %[a2] offset0:176 offset1:177\n" \
  "ds_read2_b32 v[50:51], %[a2] offset0:178 offset1:179\n" \
  "v_mfma_f32_16x16x32_bf16 v[156:159], v[44:47], v[68:71], v[156:159]\n" \
  "v_mfma_f32_16x16x32_bf16 v[144:147], v[32:35], v[68:71], v[144:147]\n" \
  "ds_read_b128 v[68:71], %[b] offset:1280\n" \
  "v_mfma_f32_16x16x32_bf16 v[160:163], v[44:47], v[72:75], v[160:163]\n" \
  "ds_read2_b32 v[44:45], %[a2] offset0:48 offset1:49\n" \
  "ds_read2_b32 v[46:47], %[a2] offset0:50 offset1:51\n" \
  "v_mfma_f32_16x16x32_bf16 v[156:159], v[40:43], v[72:75], v[156:159]\n" \
  "v_mfma_f32_16x16x32_bf16 v[144:147], v[28:31], v[72:75], v[144:147]\n" \
  "ds_read_b128 v[72:75], %[b] offset:1792\n" \
  "v_mfma_f32_16x16x32_bf16 v[160:163], v[40:43], v[76:79], v[160:163]\n" \
  "ds_read2_b32 v[40:41], %[a1] offset0:176 offset1:177\n" \
  "ds_read2_b32 v[42:43], %[a1] offset0:178 offset1:179\n" \
  "v_mfma_f32_16x16x32_bf16 v[156:159], v[36:39], v[76:79], v[156:159]\n" \
  "ds_read_b128 v[76:79], %[b] offset:2304\n" \
  "v_mfma_f32_16x16x32_bf16 v[160:163], v[36:39], v[80:83], v[160:163]\n" \
  "ds_read2_b32 v[36:37], %[a1] offset0:48 offset1:49\n" \
  "ds_read2_b32 v[38:39], %[a1] offset0:50 offset1:51\n" \
  "v_mfma_f32_16x16x32_bf16 v[156:159], v[32:35], v[80:83], v[156:159]\n" \
  "ds_read_b128 v[80:83], %[b] offset:2816\n" \
  "v_mfma_f32_16x16x32_bf16 v[160:163], v[32:35], v[84:87], v[160:163]\n" \
  "ds_read2_b32 v[32:33], %[a0] offset0:176 offset1:177\n" \
  "ds_read2_b32 v[34:35], %[a0] offset0:178 offset1:179\n" \
  "v_mfma_f32_16x16x32_bf16 v[156:159], v[28:31], v[84:87], v[156:159]\n" \
  "ds_read_b128 v[84:87], %[b] offset:3328\n" \
  "v_mfma_f32_16x16x32_bf16 v[160:163], v[28:31], v[88:91], v[160:163]\n" \
  "ds_read2_b32 v[28:29], %[a0] offset0:48 offset1:49\n" \
  "ds_read2_b32 v[30:31], %[a0] offset0:50 offset1:51\n" \
  "ds_read_b128 v[88:91], %[b] offset:3840\n" \
  "v_add_u32 %[a0], 128, %[a0]\n" \
  "v_add_u32 %[a1], 128, %[a1]\n" \
  "v_add_u32 %[a2], 128, %[a2]\n" \
  "v_add_u32 %[a3], 128, %[a3]\n" \
  "v_subrev_u32 %[b], 128, %[b]\n" \
  "s_sub_u32 %[cnt], %[cnt], 1\n" \
  "s_cmp_lg_u32 %[cnt], 0\n" \
  "s_cbranch_scc1 82b\n" \
  "s_waitcnt lgkmcnt(0)\n" \
  "v_mfma_f32_16x16x32_bf16 v[160:163], v[56:59], v[60:63], v[160:163]\n" \
  "v_mfma_f32_16x16x32_bf16 v[156:159], v[52:55], v[60:63], v[156:159]\n" \
  "v_mfma_f32_16x16x32_bf16 v[144:147], v[40:43], v[60:63], v[144:147]\n" \
  "v_mfma_f32_16x16x32_bf16 v[160:163], v[52:55], v[64:67], v[160:163]\n" \
  "v_mfma_f32_16x16x32_bf16 v[156:159], v[48:51], v[64:67], v[156:159]\n" \
  "v_mfma_f32_16x16x32_bf16 v[144:147], v[36:39], v[64:67], v[144:147]\n" \
  "v_mfma_f32_16x16x32_bf16 v[160:163], v[48:51], v[68:71], v[160:163]\n" \
  "v_mfma_f32_16x16x32_bf16 v[156:159], v[44:47], v[68:71], v[156:159]\n" \
  "v_mfma_f32_16x16x32_bf16 v[144:147], v[32:35], v[68:71], v[144:147]\n" \
  "v_mfma_f32_16x16x32_bf16 v[160:163], v[44:47], v[72:75], v[160:163]\n" \
  "v_mfma_f32_16x16x32_bf16 v[156:159], v[40:43], v[72:75], v[156:159]\n" \
  "v_mfma_f32_16x16x32_bf16 v[144:147], v[28:31], v[72:75], v[144:147]\n" \
  "v_mfma_f32_16x16x32_bf16 v[160:163], v[40:43], v[76:79], v[160:163]\n" \
  "v_mfma_f32_16x16x32_bf16 v[156:159], v[36:39], v[76:79], v[156:159]\n" \
  "v_mfma_f32_16x16x32_bf16 v[160:163], v[36:39], v[80:83], v[160:163]\n" \
  "v_mfma_f32_16x16x32_bf16 v[156:159], v[32:35], v[80:83], v[156:159]\n" \
  "v_mfma_f32_16x16x32_bf16 v[160:163], v[32:35], v[84:87], v[160:163]\n" \
  "v_mfma_f32_16x16x32_bf16 v[156:159], v[28:31], v[84:87], v[156:159]\n" \
  "v_mfma_f32_16x16x32_bf16 v[160:163], v[28:31], v[88:91], v[160:163]\n" \
  "s_nop 3\n" \
  "ds_read_b64 v[28:29], %[ue] offset:1536\n" \
  "ds_read_b64 v[30:31], %[ue] offset:3072\n" \
  "ds_read_b64 v[32:33], %[ue] offset:3584\n" \
  "s_waitcnt lgkmcnt(2)\n" \
  "v_and_b32 v92, 0xffff0000, v29\n" \
  "v_lshlrev_b32 v93, 16, v29\n" \
  "v_and_b32 v94, 0xffff0000, v28\n" \
  "v_lshlrev_b32 v95, 16, v28\n" \
  "v_fmac_f32 v144, %[d1], v92\n" \
  "v_fmac_f32 v145, %[d1], v93\n" \
  "v_fmac_f32 v146, %[d1], v94\n" \
  "v_fmac_f32 v147, %[d1], v95\n" \
  "v_cvt_pk_bf16_f32 v144, v144, v145\n" \
  "v_cvt_pk_bf16_f32 v145, v146, v147\n" \
  "ds_write_b64 %[zw], v[144:145]\n" \
  "ds_read_b128 v[60:63], %[zr]\n" \
  "s_waitcnt lgkmcnt(3)\n" \
  "v_and_b32 v92, 0xffff0000, v31\n" \
  "v_lshlrev_b32 v93, 16, v31\n" \
  "v_and_b32 v94, 0xffff0000, v30\n" \
  "v_lshlrev_b32 v95, 16, v30\n" \
  "v_fmac_f32 v156, %[d1], v92\n" \
  "v_fmac_f32 v157, %[d1], v93\n" \
  "v_fmac_f32 v158, %[d1], v94\n" \
  "v_fmac_f32 v159, %[d1], v95\n" \
  "v_cvt_pk_bf16_f32 v156, v156, v157\n" \
  "v_cvt_pk_bf16_f32 v157, v158, v159\n" \
  "ds_write_b64 %[zw], v[156:157]\n" \
  "ds_read_b128 v[64:67], %[zr]\n" \
  "s_waitcnt lgkmcnt(4)\n" \
  "v_and_b32 v92, 0xffff0000, v33\n" \
  "v_lshlrev_b32 v93, 16, v33\n" \
  "v_and_b32 v94, 0xffff0000, v32\n" \
  "v_lshlrev_b32 v95, 16, v32\n" \
  "v_fmac_f32 v160, %[d1], v92\n" \
  "v_fmac_f32 v161, %[d1], v93\n" \
  "v_fmac_f32 v162, %[d1], v94\n" \
  "v_fmac_f32 v163, %[d1], v95\n" \
  "v_cvt_pk_bf16_f32 v160, v160, v161\n" \
  "v_cvt_pk_bf16_f32 v161, v162, v163\n" \
  "ds_write_b64 %[zw], v[160:161]\n" \
  "ds_read_b128 v[68:71], %[zr]\n" \
  "s_mov_b32 exec_hi, 0\n" \
  "s_waitcnt lgkmcnt(4)\n" \
  "buffer_store_dwordx4 v[60:63], %[zo], %[rs], %[z1] offen offset:1536 sc1\n" \
  "s_waitcnt lgkmcnt(2)\n" \
  "buffer_store_dwordx4 v[64:67], %[zo], %[rs], %[z1] offen offset:3072 sc1\n" \
  "s_waitcnt lgkmcnt(0)\n" \
  "buffer_store_dwordx4 v[68:71], %[zo], %[rs], %[z1] offen offset:3584 sc1\n" \
  "s_mov_b64 exec, -1\n" \
  "s_branch 99f\n" \
  "63:\n" \
  "ds_read2_b32 v[48:49], %[a2] offset0:128 offset1:129\n" \
  "ds_read2_b32 v[50:51], %[a2] offset0:130 offset1:131\n" \
  "ds_read_b128 v[60:63], %[b] offset:448\n" \
  "ds_read2_b32 v[44:45], %[a2] offset0:0 offset1:1\n" \
  "ds_read2_b32 v[46:47], %[a2] offset0:2 offset1:3\n" \
  "ds_read2_b32 v[104:105], %[c1] offset0:128 offset1:129\n" \
  "ds_read2_b32 v[106:107], %[c1] offset0:130 offset1:131\n" \
  "ds_read_b128 v[112:115], %[d] offset:448\n" \
  "ds_read2_b32 v[36:37], %[a1] offset0:0 offset1:1\n" \
  "ds_read2_b32 v[38:39], %[a1] offset0:2 offset1:3\n" \
  "ds_read2_b32 v[32:33], %[a0] offset0:128 offset1:129\n" \
  "ds_read2_b32 v[34:35], %[a0] offset0:130 offset1:131\n" \
  "ds_read2_b32 v[28:29], %[a0] offset0:0 offset1:1\n" \
  "ds_read2_b32 v[30:31], %[a0] offset0:2 offset1:3\n" \
  "ds_read_b128 v[64:67], %[b] offset:960\n" \
  "ds_read2_b32 v[40:41], %[a1] offset0:128 offset1:129\n" \
  "ds_read2_b32 v[42:43], %[a1] offset0:130 offset1:131\n" \
  "ds_read2_b32 v[100:101], %[c1] offset0:0 offset1:1\n" \
  "ds_read2_b32 v[102:103], %[c1] offset0:2 offset1:3\n" \
  "ds_read_b128 v[116:119], %[d] offset:960\n" \
  "ds_read_b128 v[68:71], %[b] offset:1472\n" \
  "ds_read2_b32 v[96:97], %[c0] offset0:128 offset1:129\n" \
  "ds_read2_b32 v[98:99], %[c0] offset0:130 offset1:131\n" \
  "ds_read_b128 v[120:123], %[d] offset:1472\n" \
  "ds_read_b128 v[72:75], %[b] offset:1984\n" \
  "ds_read2_b32 v[92:93], %[c0] offset0:0 offset1:1\n" \
  "ds_read2_b32 v[94:95], %[c0] offset0:2 offset1:3\n" \
  "ds_read_b128 v[124:127], %[d] offset:1984\n" \
  "ds_read_b128 v[76:79], %[b] offset:2496\n" \
  "ds_read_b128 v[80:83], %[b] offset:3008\n" \
  "s_waitcnt lgkmcnt(0)\n" \
  "v_mfma_f32_16x16x32_bf16 v[152:155], v[48:51], v[60:63], 0\n" \
  "ds_read2_b32 v[48:49], %[a2] offset0:144 offset1:145\n" \
  "ds_read2_b32 v[50:51], %[a2] offset0:146 offset1:147\n" \
  "v_mfma_f32_16x16x32_bf16 v[148:151], v[44:47], v[60:63], 0\n" \
  "v_mfma_f32_16x16x32_bf16 v[144:147], v[104:107], v[112:115], 0\n" \
  "ds_read2_b32 v[104:105], %[c1] offset0:144 offset1:145\n" \
  "ds_read2_b32 v[106:107], %[c1] offset0:146 offset1:147\n" \
  "ds_read_b128 v[112:115], %[d] offset:384\n" \
  "v_mfma_f32_16x16x32_bf16 v[140:143], v[36:39], v[60:63], 0\n" \
  "v_mfma_f32_16x16x32_bf16 v[136:139], v[32:35], v[60:63], 0\n" \
  "v_mfma_f32_16x16x32_bf16 v[132:135], v[28:31], v[60:63], 0\n" \
  "ds_read_b128 v[60:63], %[b] offset:384\n" \
  "v_mfma_f32_16x16x32_bf16 v[152:155], v[44:47], v[64:67], v[152:155]\n" \
  "ds_read2_b32 v[44:45], %[a2] offset0:16 offset1:17\n" \
  "ds_read2_b32 v[46:47], %[a2] offset0:18 offset1:19\n" \
  "v_mfma_f32_16x16x32_bf16 v[148:151], v[40:43], v[64:67], v[148:151]\n" \
  "v_mfma_f32_16x16x32_bf16 v[144:147], v[100:103], v[116:119], v[144:147]\n" \
  "ds_read2_b32 v[100:101], %[c1] offset0:16 offset1:17\n" \
  "ds_read2_b32 v[102:103], %[c1] offset0:18 offset1:19\n" \
  "ds_read_b128 v[116:119], %[d] offset:896\n" \
  "v_mfma_f32_16x16x32_bf16 v[140:143], v[32:35], v[64:67], v[140:143]\n" \
  "v_mfma_f32_16x16x32_bf16 v[136:139], v[28:31], v[64:67], v[136:139]\n" \
  "ds_read_b128 v[64:67], %[b] offset:896\n" \
  "v_mfma_f32_16x16x32_bf16 v[152:155], v[40:43], v[68:71], v[152:155]\n" \
  "ds_read2_b32 v[40:41], %[a1] offset0:144 offset1:145\n" \
  "ds_read2_b32 v[42:43], %[a1] offset0:146 offset1:147\n" \
  "v_mfma_f32_16x16x32_bf16 v[148:151], v[36:39], v[68:71], v[148:151]\n" \
  "v_mfma_f32_16x16x32_bf16 v[144:147], v[96:99], v[120:123], v[144:147]\n" \
  "ds_read2_b32 v[96:97], %[c0] offset0:144 offset1:145\n" \
  "ds_read2_b32 v[98:99], %[c0] offset0:146 offset1:147\n" \
  "ds_read_b128 v[120:123], %[d] offset:1408\n" \
  "v_mfma_f32_16x16x32_bf16 v[140:143], v[28:31], v[68:71], v[140:143]\n" \
  "ds_read_b128 v[68:71], %[b] offset:1408\n" \
  "v_mfma_f32_16x16x32_bf16 v[152:155], v[36:39], v[72:75], v[152:155]\n" \
  "ds_read2_b32 v[36:37], %[a1] offset0:16 offset1:17\n" \
  "ds_read2_b32 v[38:39], %[a1] offset0:18 offset1:19\n" \
  "v_mfma_f32_16x16x32_bf16 v[148:151], v[32:35], v[72:75], v[148:151]\n" \
  "ds_read_b128 v[72:75], %[b] offset:1920\n" \
  "v_mfma_f32_16x16x32_bf16 v[144:147], v[92:95], v[124:127], v[144:147]\n" \
  "ds_read2_b32 v[92:93], %[c0] offset0:16 offset1:17\n" \
  "ds_read2_b32 v[94:95], %[c0] offset0:18 offset1:19\n" \
  "ds_read_b128 v[124:127], %[d] offset:1920\n" \
  "v_mfma_f32_16x16x32_bf16 v[152:155], v[32:35], v[76:79], v[152:155]\n" \
  "ds_read2_b32 v[32:33], %[a0] offset0:144 offset1:145\n" \
  "ds_read2_b32 v[34:35], %[a0] offset0:146 offset1:147\n" \
  "v_mfma_f32_16x16x32_bf16 v[148:151], v[28:31], v[76:79], v[148:151]\n" \
  "ds_read_b128 v[76:79], %[b] offset:2432\n" \
  "v_mfma_f32_16x16x32_bf16 v[152:155], v[28:31], v[80:83], v[152:155]\n" \
  "ds_read2_b32 v[28:29], %[a0] offset0:16 offset1:17\n" \
  "ds_read2_b32 v[30:31], %[a0] offset0:18 offset1:19\n" \
  "ds_read_b128 v[80:83], %[b] offset:2944\n" \
  "83:\n" \
  "s_waitcnt lgkmcnt(0)\n" \
  "v_mfma_f32_16x16x32_bf16 v[152:155], v[48:51], v[60:63], v[152:155]\n" \
  "ds_read2_b32 v[48:49], %[a2] offset0:160 offset1:161\n" \
  "ds_read2_b32 v[50:51], %[a2] offset0:162 offset1:163\n" \
  "v_mfma_f32_16x16x32_bf16 v[148:151], v[44:47], v[60:63], v[148:151]\n" \
  "v_mfma_f32_16x16x32_bf16 v[144:147], v[104:107], v[112:115], v[144:147]\n" \
  "ds_read2_b32 v[104:105], %[c1] offset0:160 offset1:161\n" \
  "ds_read2_b32 v[106:107], %[c1] offset0:162 offset1:163\n" \
  "ds_read_b128 v[112:115], %[d] offset:320\n" \
  "v_mfma_f32_16x16x32_bf16 v[140:143], v[36:39], v[60:63], v[140:143]\n" \
  "v_mfma_f32_16x16x32_bf16 v[136:139], v[32:35], v[60:63], v[136:139]\n" \
  "v_mfma_f32_16x16x32_bf16 v[132:135], v[28:31], v[60:63], v[132:135]\n" \
  "ds_read_b128 v[60:63], %[b] offset:320\n" \
  "v_mfma_f32_16x16x32_bf16 v[152:155], v[44:47], v[64:67], v[152:155]\n" \
  "ds_read2_b32 v[44:45], %[a2] offset0:32 offset1:33\n" \
  "ds_read2_b32 v[46:47], %[a2] offset0:34 offset1:35\n" \
  "v_mfma_f32_16x16x32_bf16 v[148:151], v[40:43], v[64:67], v[148:151]\n" \
  "v_mfma_f32_16x16x32_bf16 v[144:147], v[100:103], v[116:119], v[144:147]\n" \
  "ds_read2_b32 v[100:101], %[c1] offset0:32 offset1:33\n" \
  "ds_read2_b32 v[102:103], %[c1] offset0:34 offset1:35\n" \
  "ds_read_b128 v[116:119], %[d] offset:832\n" \
  "v_mfma_f32_16x16x32_bf16 v[140:143], v[32:35], v[64:67], v[140:143]\n" \
  "v_mfma_f32_16x16x32_bf16 v[136:139], v[28:31], v[64:67], v[136:139]\n" \
  "ds_read_b128 v[64:67], %[b] offset:832\n" \
  "v_mfma_f32_16x16x32_bf16 v[152:155], v[40:43], v[68:71], v[152:155]\n" \
  "ds_read2_b32 v[40:41], %[a1] offset0:160 offset1:161\n" \
  "ds_read2_b32 v[42:43], %[a1] offset0:162 offset1:163\n" \
  "v_mfma_f32_16x16x32_bf16 v[148:151], v[36:39], v[68:71], v[148:151]\n" \
  "v_mfma_f32_16x16x32_bf16 v[144:147], v[96:99], v[120:123], v[144:147]\n" \
  "ds_read2_b32 v[96:97], %[c0] offset0:160 offset1:161\n" \
  "ds_read2_b32 v[98:99], %[c0] offset0:162 offset1:163\n" \
  "ds_read_b128 v[120:123], %[d] offset:1344\n" \
  "v_mfma_f32_16x16x32_bf16 v[140:143], v[28:31], v[68:71], v[140:143]\n" \
  "ds_read_b128 v[68:71], %[b] offset:1344\n" \
  "v_mfma_f32_16x16x32_bf16 v[152:155], v[36:39], v[72:75], v[152:155]\n" \
  "ds_read2_b32 v[36:37], %[a1] offset0:32 offset1:33\n" \
  "ds_read2_b32 v[38:39], %[a1] offset0:34 offset1:35\n" \
  "v_mfma_f32_16x16x32_bf16 v[148:151], v[32:35], v[72:75], v[148:151]\n" \
  "ds_read_b128 v[72:75], %[b] offset:1856\n" \
  "v_mfma_f32_16x16x32_bf16 v[144:147], v[92:95], v[124:127], v[144:147]\n" \
  "ds_read2_b32 v[92:93], %[c0] offset0:32 offset1:33\n" \
  "ds_read2_b32 v[94:95], %[c0] offset0:34 offset1:35\n" \
  "ds_read_b128 v[124:127], %[d] offset:1856\n" \
  "v_mfma_f32_16x16x32_bf16 v[152:155], v[32:35], v[76:79], v[152:155]\n" \
  "ds_read2_b32 v[32:33], %[a0] offset0:160 offset1:161\n" \
  "ds_read2_b32 v[34:35], %[a0] offset0:162 offset1:163\n" \
  "v_mfma_f32_16x16x32_bf16 v[148:151], v[28:31], v[76:79], v[148:151]\n" \
  "ds_read_b128 v[76:79], %[b] offset:2368\n" \
  "v_mfma_f32_16x16x32_bf16 v[152:155], v[28:31], v[80:83], v[152:155]\n" \
  "ds_read2_b32 v[28:29], %[a0] offset0:32 offset1:33\n" \
  "ds_read2_b32 v[30:31], %[a0] offset0:34 offset1:35\n" \
  "ds_read_b128 v[80:83], %[b] offset:2880\n" \
  "s_waitcnt lgkmcnt(0)\n" \
  "v_mfma_f32_16x16x32_bf16 v[152:155], v[48:51], v[60:63], v[152:155]\n" \
  "ds_read2_b32 v[48:49], %[a2] offset0:176 offset1:177\n" \
  "ds_read2_b32 v[50:51], %[a2] offset0:178 offset1:179\n" \
  "v_mfma_f32_16x16x32_bf16 v[148:151], v[44:47], v[60:63], v[148:151]\n" \
  "v_mfma_f32_16x16x32_bf16 v[144:147], v[104:107], v[112:115], v[144:147]\n" \
  "ds_read2_b32 v[104:105], %[c1] offset0:176 offset1:177\n" \
  "ds_read2_b32 v[106:107], %[c1] offset0:178 offset1:179\n" \
  "ds_read_b128 v[112:115], %[d] offset:256\n" \
  "v_mfma_f32_16x16x32_bf16 v[140:143], v[36:39], v[60:63], v[140:143]\n" \
  "v_mfma_f32_16x16x32_bf16 v[136:139], v[32:35], v[60:63], v[136:139]\n" \
  "v_mfma_f32_16x16x32_bf16 v[132:135], v[28:31], v[60:63], v[132:135]\n" \
  "ds_read_b128 v[60:63], %[b] offset:256\n" \
  "v_mfma_f32_16x16x32_bf16 v[152:155], v[44:47], v[64:67], v[152:155]\n" \
  "ds_read2_b32 v[44:45], %[a2] offset0:48 offset1:49\n" \
  "ds_read2_b32 v[46:47], %[a2] offset0:50 offset1:51\n" \
  "v_mfma_f32_16x16x32_bf16 v[148:151], v[40:43], v[64:67], v[148:151]\n" \
  "v_mfma_f32_16x16x32_bf16 v[144:147], v[100:103], v[116:119], v[144:147]\n" \
  "ds_read2_b32 v[100:101], %[c1] offset0:48 offset1:49\n" \
  "ds_read2_b32 v[102:103], %[c1] offset0:50 offset1:51\n" \
  "ds_read_b128 v[116:119], %[d] offset:768\n" \
  "v_mfma_f32_16x16x32_bf16 v[140:143], v[32:35], v[64:67], v[140:143]\n" \
  "v_mfma_f32_16x16x32_bf16 v[136:139], v[28:31], v[64:67], v[136:139]\n" \
  "ds_read_b128 v[64:67], %[b] offset:768\n" \
  "v_mfma_f32_16x16x32_bf16 v[152:155], v[40:43], v[68:71], v[152:155]\n" \
  "ds_read2_b32 v[40:41], %[a1] offset0:176 offset1:177\n" \
  "ds_read2_b32 v[42:43], %[a1] offset0:178 offset1:179\n" \
  "v_mfma_f32_16x16x32_bf16 v[148:151], v[36:39], v[68:71], v[148:151]\n" \
  "v_mfma_f32_16x16x32_bf16 v[144:147], v[96:99], v[120:123], v[144:147]\n" \
  "ds_read2_b32 v[96:97], %[c0] offset0:176 offset1:177\n" \
  "ds_read2_b32 v[98:99], %[c0] offset0:178 offset1:179\n" \
  "ds_read_b128 v[120:123], %[d] offset:1280\n" \
  "v_mfma_f32_16x16x32_bf16 v[140:143], v[28:31], v[68:71], v[140:143]\n" \
  "ds_read_b128 v[68:71], %[b] offset:1280\n" \
  "v_mfma_f32_16x16x32_bf16 v[152:155], v[36:39], v[72:75], v[152:155]\n" \
  "ds_read2_b32 v[36:37], %[a1] offset0:48 offset1:49\n" \
  "ds_read2_b32 v[38:39], %[a1] offset0:50 offset1:51\n" \
  "v_mfma_f32_16x16x32_bf16 v[148:151], v[32:35], v[72:75], v[148:151]\n" \
  "ds_read_b128 v[72:75], %[b] offset:1792\n" \
  "v_mfma_f32_16x16x32_bf16 v[144:147], v[92:95], v[124:127], v[144:147]\n" \
  "ds_read2_b32 v[92:93], %[c0] offset0:48 offset1:49\n" \
  "ds_read2_b32 v[94:95], %[c0] offset0:50 offset1:51\n" \
  "ds_read_b128 v[124:127], %[d] offset:1792\n" \
  "v_mfma_f32_16x16x32_bf16 v[152:155], v[32:35], v[76:79], v[152:155]\n" \
  "ds_read2_b32 v[32:33], %[a0] offset0:176 offset1:177\n" \
  "ds_read2_b32 v[34:35], %[a0] offset0:178 offset1:179\n" \
  "v_mfma_f32_16x16x32_bf16 v[148:151], v[28:31], v[76:79], v[148:151]\n" \
  "ds_read_b128 v[76:79], %[b] offset:2304\n" \
  "v_mfma_f32_16x16x32_bf16 v[152:155], v[28:31], v[80:83], v[152:155]\n" \
  "ds_read2_b32 v[28:29], %[a0] offset0:48 offset1:49\n" \
  "ds_read2_b32 v[30:31], %[a0] offset0:50 offset1:51\n" \
  "ds_read_b128 v[80:83], %[b] offset:2816\n" \
  "v_add_u32 %[a0], 128, %[a0]\n" \
  "v_add_u32 %[a1], 128, %[a1]\n" \
  "v_add_u32 %[a2], 128, %[a2]\n" \
  "v_subrev_u32 %[b], 128, %[b]\n" \
  "v_add_u32 %[c0], 128, %[c0]\n" \
  "v_add_u32 %[c1], 128, %[c1]\n" \
  "v_subrev_u32 %[d], 128, %[d]\n" \
  "s_sub_u32 %[cnt], %[cnt], 1\n" \
  "s_cmp_lg_u32 %[cnt], 0\n" \
  "s_cbranch_scc1 83b\n" \
  "s_waitcnt lgkmcnt(0)\n" \
  "v_mfma_f32_16x16x32_bf16 v[152:155], v[48:51], v[60:63], v[152:155]\n" \
  "v_mfma_f32_16x16x32_bf16 v[148:151], v[44:47], v[60:63], v[148:151]\n" \
  "v_mfma_f32_16x16x32_bf16 v[144:147], v[104:107], v[112:115], v[144:147]\n" \
  "v_mfma_f32_16x16x32_bf16 v[140:143], v[36:39], v[60:63], v[140:143]\n" \
  "v_mfma_f32_16x16x32_bf16 v[136:139], v[32:35], v[60:63], v[136:139]\n" \
  "v_mfma_f32_16x16x32_bf16 v[132:135], v[28:31], v[60:63], v[132:135]\n" \
  "v_mfma_f32_16x16x32_bf16 v[152:155], v[44:47], v[64:67], v[152:155]\n" \
  "v_mfma_f32_16x16x32_bf16 v[148:151], v[40:43], v[64:67], v[148:151]\n" \
  "v_mfma_f32_16x16x32_bf16 v[144:147], v[100:103], v[116:119], v[144:147]\n" \
  "v_mfma_f32_16x16x32_bf16 v[140:143], v[32:35], v[64:67], v[140:143]\n" \
  "v_mfma_f32_16x16x32_bf16 v[136:139], v[28:31], v[64:67], v[136:139]\n" \
  "v_mfma_f32_16x16x32_bf16 v[152:155], v[40:43], v[68:71], v[152:155]\n" \
  "v_mfma_f32_16x16x32_bf16 v[148:151], v[36:39], v[68:71], v[148:151]\n" \
  "v_mfma_f32_16x16x32_bf16 v[144:147], v[96:99], v[120:123], v[144:147]\n" \
  "v_mfma_f32_16x16x32_bf16 v[140:143], v[28:31], v[68:71], v[140:143]\n" \
  "v_mfma_f32_16x16x32_bf16 v[152:155], v[36:39], v[72:75], v[152:155]\n" \
  "v_mfma_f32_16x16x32_bf16 v[148:151], v[32:35], v[72:75], v[148:151]\n" \
  "v_mfma_f32_16x16x32_bf16 v[144:147], v[92:95], v[124:127], v[144:147]\n" \
  "v_mfma_f32_16x16x32_bf16 v[152:155], v[32:35], v[76:79], v[152:155]\n" \
  "v_mfma_f32_16x16x32_bf16 v[148:151], v[28:31], v[76:79], v[148:151]\n" \
  "v_mfma_f32_16x16x32_bf16 v[152:155], v[28:31], v[80:83], v[152:155]\n" \
  "s_nop 3\n" \
  "ds_read_b64 v[28:29], %[ue] offset:0\n" \
  "ds_read_b64 v[30:31], %[ue] offset:512\n" \
  "ds_read_b64 v[32:33], %[ue] offset:1024\n" \
  "ds_read_b64 v[34:35], %[ue] offset:2048\n" \
  "ds_read_b64 v[36:37], %[ue] offset:2560\n" \
  "ds_read_b64 v[38:39], %[uf] offset:1536\n" \
  "s_waitcnt lgkmcnt(5)\n" \
  "v_and_b32 v92, 0xffff0000, v29\n" \
  "v_lshlrev_b32 v93, 16, v29\n" \
  "v_and_b32 v94, 0xffff0000, v28\n" \
  "v_lshlrev_b32 v95, 16, v28\n" \
  "v_fmac_f32 v132, %[d1], v92\n" \
  "v_fmac_f32 v133, %[d1], v93\n" \
  "v_fmac_f32 v134, %[d1], v94\n" \
  "v_fmac_f32 v135, %[d1], v95\n" \
  "v_cvt_pk_bf16_f32 v132, v132, v133\n" \
  "v_cvt_pk_bf16_f32 v133, v134, v135\n" \
  "ds_write_b64 %[zw], v[132:133]\n" \
  "ds_read_b128 v[60:63], %[zr]\n" \
  "s_waitcnt lgkmcnt(6)\n" \
  "v_and_b32 v92, 0xffff0000, v31\n" \
  "v_lshlrev_b32 v93, 16, v31\n" \
  "v_and_b32 v94, 0xffff0000, v30\n" \
  "v_lshlrev_b32 v95, 16, v30\n" \
  "v_fmac_f32 v136, %[d1], v92\n" \
  "v_fmac_f32 v137, %[d1], v93\n" \
  "v_fmac_f32 v138, %[d1], v94\n" \
  "v_fmac_f32 v139, %[d1], v95\n" \
  "v_cvt_pk_bf16_f32 v136, v136, v137\n" \
  "v_cvt_pk_bf16_f32 v137, v138, v139\n" \
  "ds_write_b64 %[zw], v[136:137]\n" \
  "ds_read_b128 v[64:67], %[zr]\n" \
  "s_waitcnt lgkmcnt(7)\n" \
  "v_and_b32 v92, 0xffff0000, v33\n" \
  "v_lshlrev_b32 v93, 16, v33\n" \
  "v_and_b32 v94, 0xffff0000, v32\n" \
  "v_lshlrev_b32 v95, 16, v32\n" \
  "v_fmac_f32 v140, %[d1], v92\n" \
  "v_fmac_f32 v141, %[d1], v93\n" \
  "v_fmac_f32 v142, %[d1], v94\n" \
  "v_fmac_f32 v143, %[d1], v95\n" \
  "v_cvt_pk_bf16_f32 v140, v140, v141\n" \
  "v_cvt_pk_bf16_f32 v141, v142, v143\n" \
  "ds_write_b64 %[zw], v[140:141]\n" \
  "ds_read_b128 v[68:71], %[zr]\n" \
  "s_waitcnt lgkmcnt(8)\n" \
  "v_and_b32 v92, 0xffff0000, v35\n" \
  "v_lshlrev_b32 v93, 16, v35\n" \
  "v_and_b32 v94, 0xffff0000, v34\n" \
  "v_lshlrev_b32 v95, 16, v34\n" \
  "v_fmac_f32 v148, %[d1], v92\n" \
  "v_fmac_f32 v149, %[d1], v93\n" \
  "v_fmac_f32 v150, %[d1], v94\n" \
  "v_fmac_f32 v151, %[d1], v95\n" \
  "v_cvt_pk_bf16_f32 v148, v148, v149\n" \
  "v_cvt_pk_bf16_f32 v149, v150, v151\n" \
  "ds_write_b64 %[zw], v[148:149]\n" \
  "ds_read_b128 v[72:75], %[zr]\n" \
  "s_waitcnt lgkmcnt(9)\n" \
  "v_and_b32 v92, 0xffff0000, v37\n" \
  "v_lshlrev_b32 v93, 16, v37\n" \
  "v_and_b32 v94, 0xffff0000, v36\n" \
  "v_lshlrev_b32 v95, 16, v36\n" \
  "v_fmac_f32 v152, %[d1], v92\n" \
  "v_fmac_f32 v153, %[d1], v93\n" \
  "v_fmac_f32 v154, %[d1], v94\n" \
  "v_fmac_f32 v155, %[d1], v95\n" \
  "v_cvt_pk_bf16_f32 v152, v152, v153\n" \
  "v_cvt_pk_bf16_f32 v153, v154, v155\n" \
  "ds_write_b64 %[zw], v[152:153]\n" \
  "ds_read_b128 v[76:79], %[zr]\n" \
  "s_waitcnt lgkmcnt(10)\n" \
  "v_and_b32 v92, 0xffff0000, v39\n" \
  "v_lshlrev_b32 v93, 16, v39\n" \
  "v_and_b32 v94, 0xffff0000, v38\n" \
  "v_lshlrev_b32 v95, 16, v38\n" \
  "v_fmac_f32 v144, %[d2], v92\n" \
  "v_fmac_f32 v145, %[d2], v93\n" \
  "v_fmac_f32 v146, %[d2], v94\n" \
  "v_fmac_f32 v147, %[d2], v95\n" \
  "v_cvt_pk_bf16_f32 v144, v144, v145\n" \
  "v_cvt_pk_bf16_f32 v145, v146, v147\n" \
  "ds_write_b64 %[zw], v[144:145]\n" \
  "ds_read_b128 v[80:83], %[zr]\n" \
  "s_mov_b32 exec_hi, 0\n" \
  "s_waitcnt lgkmcnt(10)\n" \
  "buffer_store_dwordx4 v[60:63], %[zo], %[rs], %[z1] offen offset:0 sc1\n" \
  "s_waitcnt lgkmcnt(8)\n" \
  "buffer_store_dwordx4 v[64:67], %[zo], %[rs], %[z1] offen offset:512 sc1\n" \
  "s_waitcnt lgkmcnt(6)\n" \
  "buffer_store_dwordx4 v[68:71], %[zo], %[rs], %[z1] offen offset:1024 sc1\n" \
  "s_waitcnt lgkmcnt(4)\n" \
  "buffer_store_dwordx4 v[72:75], %[zo], %[rs], %[z1] offen offset:2048 sc1\n" \
  "s_waitcnt lgkmcnt(2)\n" \
  "buffer_store_dwordx4 v[76:79], %[zo], %[rs], %[z1] offen offset:2560 sc1\n" \
  "s_waitcnt lgkmcnt(0)\n" \
  "buffer_store_dwordx4 v[80:83], %[zo], %[rs], %[z2] offen offset:1536 sc1\n" \
  "s_mov_b64 exec, -1\n" \
  "s_branch 99f\n" \
  "64:\n" \
  "ds_read2_b32 v[56:57], %[a3] offset0:128 offset1:129\n" \
  "ds_read2_b32 v[58:59], %[a3] offset0:130 offset1:131\n" \
  "ds_read_b128 v[60:63], %[b] offset:448\n" \
  "ds_read2_b32 v[52:53], %[a3] offset0:0 offset1:1\n" \
  "ds_read2_b32 v[54:55], %[a3] offset0:2 offset1:3\n" \
  "ds_read2_b32 v[48:49], %[a2] offset0:128 offset1:129\n" \
  "ds_read2_b32 v[50:51], %[a2] offset0:130 offset1:131\n" \
  "ds_read_b128 v[64:67], %[b] offset:960\n" \
  "ds_read2_b32 v[44:45], %[a2] offset0:0 offset1:1\n" \
  "ds_read2_b32 v[46:47], %[a2] offset0:2 offset1:3\n" \
  "ds_read_b128 v[68:71], %[b] offset:1472\n" \
  "ds_read2_b32 v[40:41], %[a1] offset0:128 offset1:129\n" \
  "ds_read2_b32 v[42:43], %[a1] offset0:130 offset1:131\n" \
  "ds_read_b128 v[72:75], %[b] offset:1984\n" \
  "ds_read2_b32 v[36:37], %[a1] offset0:0 offset1:1\n" \
  "ds_read2_b32 v[38:39], %[a1] offset0:2 offset1:3\n" \
  "ds_read_b128 v[76:79], %[b] offset:2496\n" \
  "ds_read2_b32 v[32:33], %[a0] offset0:128 offset1:129\n" \
  "ds_read2_b32 v[34:35], %[a0] offset0:130 offset1:131\n" \
  "ds_read_b128 v[80:83], %[b] offset:3008\n" \
  "ds_read2_b32 v[28:29], %[a0] offset0:0 offset1:1\n" \
  "ds_read2_b32 v[30:31], %[a0] offset0:2 offset1:3\n" \
  "ds_read_b128 v[84:87], %[b] offset:3520\n" \
  "ds_read_b128 v[88:91], %[b] offset:4032\n" \
  "s_waitcnt lgkmcnt(0)\n" \
  "v_mfma_f32_16x16x32_bf16 v[160:163], v[56:59], v[60:63], 0\n" \
  "ds_read2_b32 v[56:57], %[a3] offset0:144 offset1:145\n" \
  "ds_read2_b32 v[58:59], %[a3] offset0:146 offset1:147\n" \
  "v_mfma_f32_16x16x32_bf16 v[156:159], v[52:55], v[60:63], 0\n" \
  "v_mfma_f32_16x16x32_bf16 v[152:155], v[48:51], v[60:63], 0\n" \
  "ds_read_b128 v[60:63], %[b] offset:384\n" \
  "v_mfma_f32_16x16x32_bf16 v[160:163], v[52:55], v[64:67], v[160:163]\n" \
  "ds_read2_b32 v[52:53], %[a3] offset0:16 offset1:17\n" \
  "ds_read2_b32 v[54:55], %[a3] offset0:18 offset1:19\n" \
  "v_mfma_f32_16x16x32_bf16 v[156:159], v[48:51], v[64:67], v[156:159]\n" \
  "v_mfma_f32_16x16x32_bf16 v[152:155], v[44:47], v[64:67], v[152:155]\n" \
  "ds_read_b128 v[64:67], %[b] offset:896\n" \
  "v_mfma_f32_16x16x32_bf16 v[160:163], v[48:51], v[68:71], v[160:163]\n" \
  "ds_read2_b32 v[48:49], %[a2] offset0:144 offset1:145\n" \
  "ds_read2_b32 v[50:51], %[a2] offset0:146 offset1:147\n" \
  "v_mfma_f32_16x16x32_bf16 v[156:159], v[44:47], v[68:71], v[156:159]\n" \
  "v_mfma_f32_16x16x32_bf16 v[152:155], v[40:43], v[68:71], v[152:155]\n" \
  "ds_read_b128 v[68:71], %[b] offset:1408\n" \
  "v_mfma_f32_16x16x32_bf16 v[160:163], v[44:47], v[72:75], v[160:163]\n" \
  "ds_read2_b32 v[44:45], %[a2] offset0:16 offset1:17\n" \
  "ds_read2_b32 v[46:47], %[a2] offset0:18 offset1:19\n" \
  "v_mfma_f32_16x16x32_bf16 v[156:159], v[40:43], v[72:75], v[156:159]\n" \
  "v_mfma_f32_16x16x32_bf16 v[152:155], v[36:39], v[72:75], v[152:155]\n" \
  "ds_read_b128 v[72:75], %[b] offset:1920\n" \
  "v_mfma_f32_16x16x32_bf16 v[160:163], v[40:43], v[76:79], v[160:163]\n" \
  "ds_read2_b32 v[40:41], %[a1] offset0:144 offset1:145\n" \
  "ds_read2_b32 v[42:43], %[a1] offset0:146 offset1:147\n" \
  "v_mfma_f32_16x16x32_bf16 v[156:159], v[36:39], v[76:79], v[156:159]\n" \
  "v_mfma_f32_16x16x32_bf16 v[152:155], v[32:35], v[76:79], v[152:155]\n" \
  "ds_read_b128 v[76:79], %[b] offset:2432\n" \
  "v_mfma_f32_16x16x32_bf16 v[160:163], v[36:39], v[80:83], v[160:163]\n" \
  "ds_read2_b32 v[36:37], %[a1] offset0:16 offset1:17\n" \
  "ds_read2_b32 v[38:39], %[a1] offset0:18 offset1:19\n" \
  "v_mfma_f32_16x16x32_bf16 v[156:159], v[32:35], v[80:83], v[156:159]\n" \
  "v_mfma_f32_16x16x32_bf16 v[152:155], v[28:31], v[80:83], v[152:155]\n" \
  "ds_read_b128 v[80:83], %[b] offset:2944\n" \
  "v_mfma_f32_16x16x32_bf16 v[160:163], v[32:35], v[84:87], v[160:163]\n" \
  "ds_read2_b32 v[32:33], %[a0] offset0:144 offset1:145\n" \
  "ds_read2_b32 v[34:35], %[a0] offset0:146 offset1:147\n" \
  "v_mfma_f32_16x16x32_bf16 v[156:159], v[28:31], v[84:87], v[156:159]\n" \
  "ds_read_b128 v[84:87], %[b] offset:3456\n" \
  "v_mfma_f32_16x16x32_bf16 v[160:163], v[28:31], v[88:91], v[160:163]\n" \
  "ds_read2_b32 v[28:29], %[a0] offset0:16 offset1:17\n" \
  "ds_read2_b32 v[30:31], %[a0] offset0:18 offset1:19\n" \
  "ds_read_b128 v[88:91], %[b] offset:3968\n" \
  "84:\n" \
  "s_waitcnt lgkmcnt(0)\n" \
  "v_mfma_f32_16x16x32_bf16 v[160:163], v[56:59], v[60:63], v[160:163]\n" \
  "ds_read2_b32 v[56:57], %[a3] offset0:160 offset1:161\n" \
  "ds_read2_b32 v[58:59], %[a3] offset0:162 offset1:163\n" \
  "v_mfma_f32_16x16x32_bf16 v[156:159], v[52:55], v[60:63], v[156:159]\n" \
  "v_mfma_f32_16x16x32_bf16 v[152:155], v[48:51], v[60:63], v[152:155]\n" \
  "ds_read_b128 v[60:63], %[b] offset:320\n" \
  "v_mfma_f32_16x16x32_bf16 v[160:163], v[52:55], v[64:67], v[160:163]\n" \
  "ds_read2_b32 v[52:53], %[a3] offset0:32 offset1:33\n" \
  "ds_read2_b32 v[54:55], %[a3] offset0:34 offset1:35\n" \
  "v_mfma_f32_16x16x32_bf16 v[156:159], v[48:51], v[64:67], v[156:159]\n" \
  "v_mfma_f32_16x16x32_bf16 v[152:155], v[44:47], v[64:67], v[152:155]\n" \
  "ds_read_b128 v[64:67], %[b] offset:832\n" \
  "v_mfma_f32_16x16x32_bf16 v[160:163], v[48:51], v[68:71], v[160:163]\n" \
  "ds_read2_b32 v[48:49], %[a2] offset0:160 offset1:161\n" \
  "ds_read2_b32 v[50:51], %[a2] offset0:162 offset1:163\n" \
  "v_mfma_f32_16x16x32_bf16 v[156:159], v[44:47], v[68:71], v[156:159]\n" \
  "v_mfma_f32_16x16x32_bf16 v[152:155], v[40:43], v[68:71], v[152:155]\n" \
  "ds_read_b128 v[68:71], %[b] offset:1344\n" \
  "v_mfma_f32_16x16x32_bf16 v[160:163], v[44:47], v[72:75], v[160:163]\n" \
  "ds_read2_b32 v[44:45], %[a2] offset0:32 offset1:33\n" \
  "ds_read2_b32 v[46:47], %[a2] offset0:34 offset1:35\n" \
  "v_mfma_f32_16x16x32_bf16 v[156:159], v[40:43], v[72:75], v[156:159]\n" \
  "v_mfma_f32_16x16x32_bf16 v[152:155], v[36:39], v[72:75], v[152:155]\n" \
  "ds_read_b128 v[72:75], %[b] offset:1856\n" \
  "v_mfma_f32_16x16x32_bf16 v[160:163], v[40:43], v[76:79], v[160:163]\n" \
  "ds_read2_b32 v[40:41], %[a1] offset0:160 offset1:161\n" \
  "ds_read2_b32 v[42:43], %[a1] offset0:162 offset1:163\n" \
  "v_mfma_f32_16x16x32_bf16 v[156:159], v[36:39], v[76:79], v[156:159]\n" \
  "v_mfma_f32_16x16x32_bf16 v[152:155], v[32:35], v[76:79], v[152:155]\n" \
  "ds_read_b128 v[76:79], %[b] offset:2368\n" \
  "v_mfma_f32_16x16x32_bf16 v[160:163], v[36:39], v[80:83], v[160:163]\n" \
  "ds_read2_b32 v[36:37], %[a1] offset0:32 offset1:33\n" \
  "ds_read2_b32 v[38:39], %[a1] offset0:34 offset1:35\n" \
  "v_mfma_f32_16x16x32_bf16 v[156:159], v[32:35], v[80:83], v[156:159]\n" \
  "v_mfma_f32_16x16x32_bf16 v[152:155], v[28:31], v[80:83], v[152:155]\n" \
  "ds_read_b128 v[80:83], %[b] offset:2880\n" \
  "v_mfma_f32_16x16x32_bf16 v[160:163], v[32:35], v[84:87], v[160:163]\n" \
  "ds_read2_b32 v[32:33], %[a0] offset0:160 offset1:161\n" \
  "ds_read2_b32 v[34:35], %[a0] offset0:162 offset1:163\n" \
  "v_mfma_f32_16x16x32_bf16 v[156:159], v[28:31], v[84:87], v[156:159]\n" \
  "ds_read_b128 v[84:87], %[b] offset:3392\n" \
  "v_mfma_f32_16x16x32_bf16 v[160:163], v[28:31], v[88:91], v[160:163]\n" \
  "ds_read2_b32 v[28:29], %[a0] offset0:32 offset1:33\n" \
  "ds_read2_b32 v[30:31], %[a0] offset0:34 offset1:35\n" \
  "ds_read_b128 v[88:91], %[b] offset:3904\n" \
  "s_waitcnt lgkmcnt(0)\n" \
  "v_mfma_f32_16x16x32_bf16 v[160:163], v[56:59], v[60:63], v[160:163]\n" \
  "ds_read2_b32 v[56:57], %[a3] offset0:176 offset1:177\n" \
  "ds_read2_b32 v[58:59], %[a3] offset0:178 offset1:179\n" \
  "v_mfma_f32_16x16x32_bf16 v[156:159], v[52:55], v[60:63], v[156:159]\n" \
  "v_mfma_f32_16x16x32_bf16 v[152:155], v[48:51], v[60:63], v[152:155]\n" \
  "ds_read_b128 v[60:63], %[b] offset:256\n" \
  "v_mfma_f32_16x16x32_bf16 v[160:163], v[52:55], v[64:67], v[160:163]\n" \
  "ds_read2_b32 v[52:53], %[a3] offset0:48 offset1:49\n" \
  "ds_read2_b32 v[54:55], %[a3] offset0:50 offset1:51\n" \
  "v_mfma_f32_16x16x32_bf16 v[156:159], v[48:51], v[64:67], v[156:159]\n" \
  "v_mfma_f32_16x16x32_bf16 v[152:155], v[44:47], v[64:67], v[152:155]\n" \
  "ds_read_b128 v[64:67], %[b] offset:768\n" \
  "v_mfma_f32_16x16x32_bf16 v[160:163], v[48:51], v[68:71], v[160:163]\n" \
  "ds_read2_b32 v[48:49], %[a2] offset0:176 offset1:177\n" \
  "ds_read2_b32 v[50:51], %[a2] offset0:178 offset1:179\n" \
  "v_mfma_f32_16x16x32_bf16 v[156:159], v[44:47], v[68:71], v[156:159]\n" \
  "v_mfma_f32_16x16x32_bf16 v[152:155], v[40:43], v[68:71], v[152:155]\n" \
  "ds_read_b128 v[68:71], %[b] offset:1280\n" \
  "v_mfma_f32_16x16x32_bf16 v[160:163], v[44:47], v[72:75], v[160:163]\n" \
  "ds_read2_b32 v[44:45], %[a2] offset0:48 offset1:49\n" \
  "ds_read2_b32 v[46:47], %[a2] offset0:50 offset1:51\n" \
  "v_mfma_f32_16x16x32_bf16 v[156:159], v[40:43], v[72:75], v[156:159]\n" \
  "v_mfma_f32_16x16x32_bf16 v[152:155], v[36:39], v[72:75], v[152:155]\n" \
  "ds_read_b128 v[72:75], %[b] offset:1792\n" \
  "v_mfma_f32_16x16x32_bf16 v[160:163], v[40:43], v[76:79], v[160:163]\n" \
  "ds_read2_b32 v[40:41], %[a1] offset0:176 offset1:177\n" \
  "ds_read2_b32 v[42:43], %[a1] offset0:178 offset1:179\n" \
  "v_mfma_f32_16x16x32_bf16 v[156:159], v[36:39], v[76:79], v[156:159]\n" \
  "v_mfma_f32_16x16x32_bf16 v[152:155], v[32:35], v[76:79], v[152:155]\n" \
  "ds_read_b128 v[76:79], %[b] offset:2304\n" \
  "v_mfma_f32_16x16x32_bf16 v[160:163], v[36:39], v[80:83], v[160:163]\n" \
  "ds_read2_b32 v[36:37], %[a1] offset0:48 offset1:49\n" \
  "ds_read2_b32 v[38:39], %[a1] offset0:50 offset1:51\n" \
  "v_mfma_f32_16x16x32_bf16 v[156:159], v[32:35], v[80:83], v[156:159]\n" \
  "v_mfma_f32_16x16x32_bf16 v[152:155], v[28:31], v[80:83], v[152:155]\n" \
  "ds_read_b128 v[80:83], %[b] offset:2816\n" \
  "v_mfma_f32_16x16x32_bf16 v[160:163], v[32:35], v[84:87], v[160:163]\n" \
  "ds_read2_b32 v[32:33], %[a0] offset0:176 offset1:177\n" \
  "ds_read2_b32 v[34:35], %[a0] offset0:178 offset1:179\n" \
  "v_mfma_f32_16x16x32_bf16 v[156:159], v[28:31], v[84:87], v[156:159]\n" \
  "ds_read_b128 v[84:87], %[b] offset:3328\n" \
  "v_mfma_f32_16x16x32_bf16 v[160:163], v[28:31], v[88:91], v[160:163]\n" \
  "ds_read2_b32 v[28:29], %[a0] offset0:48 offset1:49\n" \
  "ds_read2_b32 v[30:31], %[a0] offset0:50 offset1:51\n" \
  "ds_read_b128 v[88:91], %[b] offset:3840\n" \
  "v_add_u32 %[a0], 128, %[a0]\n" \
  "v_add_u32 %[a1], 128, %[a1]\n" \
  "v_add_u32 %[a2], 128, %[a2]\n" \
  "v_add_u32 %[a3], 128, %[a3]\n" \
  "v_subrev_u32 %[b], 128, %[b]\n" \
  "s_sub_u32 %[cnt], %[cnt], 1\n" \
  "s_cmp_lg_u32 %[cnt], 0\n" \
  "s_cbranch_scc1 84b\n" \
  "s_waitcnt lgkmcnt(0)\n" \
  "v_mfma_f32_16x16x32_bf16 v[160:163], v[56:59], v[60:63], v[160:163]\n" \
  "v_mfma_f32_16x16x32_bf16 v[156:159], v[52:55], v[60:63], v[156:159]\n" \
  "v_mfma_f32_16x16x32_bf16 v[152:155], v[48:51], v[60:63], v[152:155]\n" \
  "v_mfma_f32_16x16x32_bf16 v[160:163], v[52:55], v[64:67], v[160:163]\n" \
  "v_mfma_f32_16x16x32_bf16 v[156:159], v[48:51], v[64:67], v[156:159]\n" \
  "v_mfma_f32_16x16x32_bf16 v[152:155], v[44:47], v[64:67], v[152:155]\n" \
  "v_mfma_f32_16x16x32_bf16 v[160:163], v[48:51], v[68:71], v[160:163]\n" \
  "v_mfma_f32_16x16x32_bf16 v[156:159], v[44:47], v[68:71], v[156:159]\n" \
  "v_mfma_f32_16x16x32_bf16 v[152:155], v[40:43], v[68:71], v[152:155]\n" \
  "v_mfma_f32_16x16x32_bf16 v[160:163], v[44:47], v[72:75], v[160:163]\n" \
  "v_mfma_f32_16x16x32_bf16 v[156:159], v[40:43], v[72:75], v[156:159]\n" \
  "v_mfma_f32_16x16x32_bf16 v[152:155], v[36:39], v[72:75], v[152:155]\n" \
  "v_mfma_f32_16x16x32_bf16 v[160:163], v[40:43], v[76:79], v[160:163]\n" \
  "v_mfma_f32_16x16x32_bf16 v[156:159], v[36:39], v[76:79], v[156:159]\n" \
  "v_mfma_f32_16x16x32_bf16 v[152:155], v[32:35], v[76:79], v[152:155]\n" \
  "v_mfma_f32_16x16x32_bf16 v[160:163], v[36:39], v[80:83], v[160:163]\n" \
  "v_mfma_f32_16x16x32_bf16 v[156:159], v[32:35], v[80:83], v[156:159]\n" \
  "v_mfma_f32_16x16x32_bf16 v[152:155], v[28:31], v[80:83], v[152:155]\n" \
  "v_mfma_f32_16x16x32_bf16 v[160:163], v[32:35], v[84:87], v[160:163]\n" \
  "v_mfma_f32_16x16x32_bf16 v[156:159], v[28:31], v[84:87], v[156:159]\n" \
  "v_mfma_f32_16x16x32_bf16 v[160:163], v[28:31], v[88:91], v[160:163]\n" \
  "s_nop 3\n" \
  "ds_read_b64 v[28:29], %[ue] offset:2560\n" \
  "ds_read_b64 v[30:31], %[ue] offset:3072\n" \
  "ds_read_b64 v[32:33], %[ue] offset:3584\n" \
  "s_waitcnt lgkmcnt(2)\n" \
  "v_and_b32 v92, 0xffff0000, v29\n" \
  "v_lshlrev_b32 v93, 16, v29\n" \
  "v_and_b32 v94, 0xffff0000, v28\n" \
  "v_lshlrev_b32 v95, 16, v28\n" \
  "v_fmac_f32 v152, %[d1], v92\n" \
  "v_fmac_f32 v153, %[d1], v93\n" \
  "v_fmac_f32 v154, %[d1], v94\n" \
  "v_fmac_f32 v155, %[d1], v95\n" \
  "v_cvt_pk_bf16_f32 v152, v152, v153\n" \
  "v_cvt_pk_bf16_f32 v153, v154, v155\n" \
  "ds_write_b64 %[zw], v[152:153]\n" \
  "ds_read_b128 v[60:63], %[zr]\n" \
  "s_waitcnt lgkmcnt(3)\n" \
  "v_and_b32 v92, 0xffff0000, v31\n" \
  "v_lshlrev_b32 v93, 16, v31\n" \
  "v_and_b32 v94, 0xffff0000, v30\n" \
  "v_lshlrev_b32 v95, 16, v30\n" \
  "v_fmac_f32 v156, %[d1], v92\n" \
  "v_fmac_f32 v157, %[d1], v93\n" \
  "v_fmac_f32 v158, %[d1], v94\n" \
  "v_fmac_f32 v159, %[d1], v95\n" \
  "v_cvt_pk_bf16_f32 v156, v156, v157\n" \
  "v_cvt_pk_bf16_f32 v157, v158, v159\n" \
  "ds_write_b64 %[zw], v[156:157]\n" \
  "ds_read_b128 v[64:67], %[zr]\n" \
  "s_waitcnt lgkmcnt(4)\n" \
  "v_and_b32 v92, 0xffff0000, v33\n" \
  "v_lshlrev_b32 v93, 16, v33\n" \
  "v_and_b32 v94, 0xffff0000, v32\n" \
  "v_lshlrev_b32 v95, 16, v32\n" \
  "v_fmac_f32 v160, %[d1], v92\n" \
  "v_fmac_f32 v161, %[d1], v93\n" \
  "v_fmac_f32 v162, %[d1], v94\n" \
  "v_fmac_f32 v163, %[d1], v95\n" \
  "v_cvt_pk_bf16_f32 v160, v160, v161\n" \
  "v_cvt_pk_bf16_f32 v161, v162, v163\n" \
  "ds_write_b64 %[zw], v[160:161]\n" \
  "ds_read_b128 v[68:71], %[zr]\n" \
  "s_mov_b32 exec_hi, 0\n" \
  "s_waitcnt lgkmcnt(4)\n" \
  "buffer_store_dwordx4 v[60:63], %[zo], %[rs], %[z1] offen offset:2560 sc1\n" \
  "s_waitcnt lgkmcnt(2)\n" \
  "buffer_store_dwordx4 v[64:67], %[zo], %[rs], %[z1] offen offset:3072 sc1\n" \
  "s_waitcnt lgkmcnt(0)\n" \
  "buffer_store_dwordx4 v[68:71], %[zo], %[rs], %[z1] offen offset:3584 sc1\n" \
  "s_mov_b64 exec, -1\n" \
  "s_branch 99f\n" \
  "65:\n" \
  "ds_read2_b32 v[56:57], %[a3] offset0:128 offset1:129\n" \
  "ds_read2_b32 v[58:59], %[a3] offset0:130 offset1:131\n" \
  "ds_read_b128 v[60:63], %[b] offset:448\n" \
  "ds_read2_b32 v[48:49], %[a2] offset0:128 offset1:129\n" \
  "ds_read2_b32 v[50:51], %[a2] offset0:130 offset1:131\n" \
  "ds_read2_b32 v[100:101], %[c1] offset0:0 offset1:1\n" \
  "ds_read2_b32 v[102:103], %[c1] offset0:2 offset1:3\n" \
  "ds_read_b128 v[112:115], %[d] offset:448\n" \
  "ds_read2_b32 v[96:97], %[c0] offset0:128 offset1:129\n" \
  "ds_read2_b32 v[98:99], %[c0] offset0:130 offset1:131\n" \
  "ds_read2_b32 v[92:93], %[c0] offset0:0 offset1:1\n" \
  "ds_read2_b32 v[94:95], %[c0] offset0:2 offset1:3\n" \
  "ds_read2_b32 v[52:53], %[a3] offset0:0 offset1:1\n" \
  "ds_read2_b32 v[54:55], %[a3] offset0:2 offset1:3\n" \
  "ds_read_b128 v[64:67], %[b] offset:960\n" \
  "ds_read2_b32 v[44:45], %[a2] offset0:0 offset1:1\n" \
  "ds_read2_b32 v[46:47], %[a2] offset0:2 offset1:3\n" \
  "ds_read_b128 v[116:119], %[d] offset:960\n" \
  "ds_read_b128 v[68:71], %[b] offset:1472\n" \
  "ds_read2_b32 v[40:41], %[a1] offset0:128 offset1:129\n" \
  "ds_read2_b32 v[42:43], %[a1] offset0:130 offset1:131\n" \
  "ds_read_b128 v[120:123], %[d] offset:1472\n" \
  "ds_read_b128 v[72:75], %[b] offset:1984\n" \
  "ds_read2_b32 v[36:37], %[a1] offset0:0 offset1:1\n" \
  "ds_read2_b32 v[38:39], %[a1] offset0:2 offset1:3\n" \
  "ds_read_b128 v[76:79], %[b] offset:2496\n" \
  "ds_read2_b32 v[32:33], %[a0] offset0:128 offset1:129\n" \
  "ds_read2_b32 v[34:35], %[a0] offset0:130 offset1:131\n" \
  "ds_read_b128 v[80:83], %[b] offset:3008\n" \
  "ds_read2_b32 v[28:29], %[a0] offset0:0 offset1:1\n" \
  "ds_read2_b32 v[30:31], %[a0] offset0:2 offset1:3\n" \
  "ds_read_b128 v[84:87], %[b] offset:3520\n" \
  "ds_read_b128 v[88:91], %[b] offset:4032\n" \
  "s_waitcnt lgkmcnt(0)\n" \
  "v_mfma_f32_16x16x32_bf16 v[160:163], v[56:59], v[60:63], 0\n" \
  "ds_read2_b32 v[56:57], %[a3] offset0:144 offset1:145\n" \
  "ds_read2_b32 v[58:59], %[a3] offset0:146 offset1:147\n" \
  "v_mfma_f32_16x16x32_bf16 v[152:155], v[48:51], v[60:63], 0\n" \
  "ds_read_b128 v[60:63], %[b] offset:384\n" \
  "v_mfma_f32_16x16x32_bf16 v[140:143], v[100:103], v[112:115], 0\n" \
  "ds_read2_b32 v[100:101], %[c1] offset0:16 offset1:17\n" \
  "ds_read2_b32 v[102:103], %[c1] offset0:18 offset1:19\n" \
  "v_mfma_f32_16x16x32_bf16 v[136:139], v[96:99], v[112:115], 0\n" \
  "v_mfma_f32_16x16x32_bf16 v[132:135], v[92:95], v[112:115], 0\n" \
  "ds_read_b128 v[112:115], %[d] offset:384\n" \
  "v_mfma_f32_16x16x32_bf16 v[160:163], v[52:55], v[64:67], v[160:163]\n" \
  "ds_read2_b32 v[52:53], %[a3] offset0:16 offset1:17\n" \
  "ds_read2_b32 v[54:55], %[a3] offset0:18 offset1:19\n" \
  "v_mfma_f32_16x16x32_bf16 v[152:155], v[44:47], v[64:67], v[152:155]\n" \
  "ds_read_b128 v[64:67], %[b] offset:896\n" \
  "v_mfma_f32_16x16x32_bf16 v[140:143], v[96:99], v[116:119], v[140:143]\n" \
  "ds_read2_b32 v[96:97], %[c0] offset0:144 offset1:145\n" \
  "ds_read2_b32 v[98:99], %[c0] offset0:146 offset1:147\n" \
  "v_mfma_f32_16x16x32_bf16 v[136:139], v[92:95], v[116:119], v[136:139]\n" \
  "ds_read_b128 v[116:119], %[d] offset:896\n" \
  "v_mfma_f32_16x16x32_bf16 v[160:163], v[48:51], v[68:71], v[160:163]\n" \
  "ds_read2_b32 v[48:49], %[a2] offset0:144 offset1:145\n" \
  "ds_read2_b32 v[50:51], %[a2] offset0:146 offset1:147\n" \
  "v_mfma_f32_16x16x32_bf16 v[152:155], v[40:43], v[68:71], v[152:155]\n" \
  "ds_read_b128 v[68:71], %[b] offset:1408\n" \
  "v_mfma_f32_16x16x32_bf16 v[140:143], v[92:95], v[120:123], v[140:143]\n" \
  "ds_read2_b32 v[92:93], %[c0] offset0:16 offset1:17\n" \
  "ds_read2_b32 v[94:95], %[c0] offset0:18 offset1:19\n" \
  "ds_read_b128 v[120:123], %[d] offset:1408\n" \
  "v_mfma_f32_16x16x32_bf16 v[160:163], v[44:47], v[72:75], v[160:163]\n" \
  "ds_read2_b32 v[44:45], %[a2] offset0:16 offset1:17\n" \
  "ds_read2_b32 v[46:47], %[a2] offset0:18 offset1:19\n" \
  "v_mfma_f32_16x16x32_bf16 v[152:155], v[36:39], v[72:75], v[152:155]\n" \
  "ds_read_b128 v[72:75], %[b] offset:1920\n" \
  "v_mfma_f32_16x16x32_bf16 v[160:163], v[40:43], v[76:79], v[160:163]\n" \
  "ds_read2_b32 v[40:41], %[a1] offset0:144 offset1:145\n" \
  "ds_read2_b32 v[42:43], %[a1] offset0:146 offset1:147\n" \
  "v_mfma_f32_16x16x32_bf16 v[152:155], v[32:35], v[76:79], v[152:155]\n" \
  "ds_read_b128 v[76:79], %[b] offset:2432\n" \
  "v_mfma_f32_16x16x32_bf16 v[160:163], v[36:39], v[80:83], v[160:163]\n" \
  "ds_read2_b32 v[36:37], %[a1] offset0:16 offset1:17\n" \
  "ds_read2_b32 v[38:39], %[a1] offset0:18 offset1:19\n" \
  "v_mfma_f32_16x16x32_bf16 v[152:155], v[28:31], v[80:83], v[152:155]\n" \
  "ds_read_b128 v[80:83], %[b] offset:2944\n" \
  "v_mfma_f32_16x16x32_bf16 v[160:163], v[32:35], v[84:87], v[160:163]\n" \
  "ds_read2_b32 v[32:33], %[a0] offset0:144 offset1:145\n" \
  "ds_read2_b32 v[34:35], %[a0] offset0:146 offset1:147\n" \
  "ds_read_b128 v[84:87], %[b] offset:3456\n" \
  "v_mfma_f32_16x16x32_bf16 v[160:163], v[28:31], v[88:91], v[160:163]\n" \
  "ds_read2_b32 v[28:29], %[a0] offset0:16 offset1:17\n" \
  "ds_read2_b32 v[30:31], %[a0] offset0:18 offset1:19\n" \
  "ds_read_b128 v[88:91], %[b] offset:3968\n" \
  "85:\n" \
  "s_waitcnt lgkmcnt(0)\n" \
  "v_mfma_f32_16x16x32_bf16 v[160:163], v[56:59], v[60:63], v[160:163]\n" \
  "ds_read2_b32 v[56:57], %[a3] offset0:160 offset1:161\n" \
  "ds_read2_b32 v[58:59], %[a3] offset0:162 offset1:163\n" \
  "v_mfma_f32_16x16x32_bf16 v[152:155], v[48:51], v[60:63], v[152:155]\n" \
  "ds_read_b128 v[60:63], %[b] offset:320\n" \
  "v_mfma_f32_16x16x32_bf16 v[140:143], v[100:103], v[112:115], v[140:143]\n" \
  "ds_read2_b32 v[100:101], %[c1] offset0:32 offset1:33\n" \
  "ds_read2_b32 v[102:103], %[c1] offset0:34 offset1:35\n" \
  "v_mfma_f32_16x16x32_bf16 v[136:139], v[96:99], v[112:115], v[136:139]\n" \
  "v_mfma_f32_16x16x32_bf16 v[132:135], v[92:95], v[112:115], v[132:135]\n" \
  "ds_read_b128 v[112:115], %[d] offset:320\n" \
  "v_mfma_f32_16x16x32_bf16 v[160:163], v[52:55], v[64:67], v[160:163]\n" \
  "ds_read2_b32 v[52:53], %[a3] offset0:32 offset1:33\n" \
  "ds_read2_b32 v[54:55], %[a3] offset0:34 offset1:35\n" \
  "v_mfma_f32_16x16x32_bf16 v[152:155], v[44:47], v[64:67], v[152:155]\n" \
  "ds_read_b128 v[64:67], %[b] offset:832\n" \
  "v_mfma_f32_16x16x32_bf16 v[140:143], v[96:99], v[116:119], v[140:143]\n" \
  "ds_read2_b32 v[96:97], %[c0] offset0:160 offset1:161\n" \
  "ds_read2_b32 v[98:99], %[c0] offset0:162 offset1:163\n" \
  "v_mfma_f32_16x16x32_bf16 v[136:139], v[92:95], v[116:119], v[136:139]\n" \
  "ds_read_b128 v[116:119], %[d] offset:832\n" \
  "v_mfma_f32_16x16x32_bf16 v[160:163], v[48:51], v[68:71], v[160:163]\n" \
  "ds_read2_b32 v[48:49], %[a2] offset0:160 offset1:161\n" \
  "ds_read2_b32 v[50:51], %[a2] offset0:162 offset1:163\n" \
  "v_mfma_f32_16x16x32_bf16 v[152:155], v[40:43], v[68:71], v[152:155]\n" \
  "ds_read_b128 v[68:71], %[b] offset:1344\n" \
  "v_mfma_f32_16x16x32_bf16 v[140:143], v[92:95], v[120:123], v[140:143]\n" \
  "ds_read2_b32 v[92:93], %[c0] offset0:32 offset1:33\n" \
  "ds_read2_b32 v[94:95], %[c0] offset0:34 offset1:35\n" \
  "ds_read_b128 v[120:123], %[d] offset:1344\n" \
  "v_mfma_f32_16x16x32_bf16 v[160:163], v[44:47], v[72:75], v[160:163]\n" \
  "ds_read2_b32 v[44:45], %[a2] offset0:32 offset1:33\n" \
  "ds_read2_b32 v[46:47], %[a2] offset0:34 offset1:35\n" \
  "v_mfma_f32_16x16x32_bf16 v[152:155], v[36:39], v[72:75], v[152:155]\n" \
  "ds_read_b128 v[72:75], %[b] offset:1856\n" \
  "v_mfma_f32_16x16x32_bf16 v[160:163], v[40:43], v[76:79], v[160:163]\n" \
  "ds_read2_b32 v[40:41], %[a1] offset0:160 offset1:161\n" \
  "ds_read2_b32 v[42:43], %[a1] offset0:162 offset1:163\n" \
  "v_mfma_f32_16x16x32_bf16 v[152:155], v[32:35], v[76:79], v[152:155]\n" \
  "ds_read_b128 v[76:79], %[b] offset:2368\n" \
  "v_mfma_f32_16x16x32_bf16 v[160:163], v[36:39], v[80:83], v[160:163]\n" \
  "ds_read2_b32 v[36:37], %[a1] offset0:32 offset1:33\n" \
  "ds_read2_b32 v[38:39], %[a1] offset0:34 offset1:35\n" \
  "v_mfma_f32_16x16x32_bf16 v[152:155], v[28:31], v[80:83], v[152:155]\n" \
  "ds_read_b128 v[80:83], %[b] offset:2880\n" \
  "v_mfma_f32_16x16x32_bf16 v[160:163], v[32:35], v[84:87], v[160:163]\n" \
  "ds_read2_b32 v[32:33], %[a0] offset0:160 offset1:161\n" \
  "ds_read2_b32 v[34:35], %[a0] offset0:162 offset1:163\n" \
  "ds_read_b128 v[84:87], %[b] offset:3392\n" \
  "v_mfma_f32_16x16x32_bf16 v[160:163], v[28:31], v[88:91], v[160:163]\n" \
  "ds_read2_b32 v[28:29], %[a0] offset0:32 offset1:33\n" \
  "ds_read2_b32 v[30:31], %[a0] offset0:34 offset1:35\n" \
  "ds_read_b128 v[88:91], %[b] offset:3904\n" \
  "s_waitcnt lgkmcnt(0)\n" \
  "v_mfma_f32_16x16x32_bf16 v[160:163], v[56:59], v[60:63], v[160:163]\n" \
  "ds_read2_b32 v[56:57], %[a3] offset0:176 offset1:177\n" \
  "ds_read2_b32 v[58:59], %[a3] offset0:178 offset1:179\n" \
  "v_mfma_f32_16x16x32_bf16 v[152:155], v[48:51], v[60:63], v[152:155]\n" \
  "ds_read_b128 v[60:63], %[b] offset:256\n" \
  "v_mfma_f32_16x16x32_bf16 v[140:143], v[100:103], v[112:115], v[140:143]\n" \
  "ds_read2_b32 v[100:101], %[c1] offset0:48 offset1:49\n" \
  "ds_read2_b32 v[102:103], %[c1] offset0:50 offset1:51\n" \
  "v_mfma_f32_16x16x32_bf16 v[136:139], v[96:99], v[112:115], v[136:139]\n" \
  "v_mfma_f32_16x16x32_bf16 v[132:135], v[92:95], v[112:115], v[132:135]\n" \
  "ds_read_b128 v[112:115], %[d] offset:256\n" \
  "v_mfma_f32_16x16x32_bf16 v[160:163], v[52:55], v[64:67], v[160:163]\n" \
  "ds_read2_b32 v[52:53], %[a3] offset0:48 offset1:49\n" \
  "ds_read2_b32 v[54:55], %[a3] offset0:50 offset1:51\n" \
  "v_mfma_f32_16x16x32_bf16 v[152:155], v[44:47], v[64:67], v[152:155]\n" \
  "ds_read_b128 v[64:67], %[b] offset:768\n" \
  "v_mfma_f32_16x16x32_bf16 v[140:143], v[96:99], v[116:119], v[140:143]\n" \
  "ds_read2_b32 v[96:97], %[c0] offset0:176 offset1:177\n" \
  "ds_read2_b32 v[98:99], %[c0] offset0:178 offset1:179\n" \
  "v_mfma_f32_16x16x32_bf16 v[136:139], v[92:95], v[116:119], v[136:139]\n" \
  "ds_read_b128 v[116:119], %[d] offset:768\n" \
  "v_mfma_f32_16x16x32_bf16 v[160:163], v[48:51], v[68:71], v[160:163]\n" \
  "ds_read2_b32 v[48:49], %[a2] offset0:176 offset1:177\n" \
  "ds_read2_b32 v[50:51], %[a2] offset0:178 offset1:179\n" \
  "v_mfma_f32_16x16x32_bf16 v[152:155], v[40:43], v[68:71], v[152:155]\n" \
  "ds_read_b128 v[68:71], %[b] offset:1280\n" \
  "v_mfma_f32_16x16x32_bf16 v[140:143], v[92:95], v[120:123], v[140:143]\n" \
  "ds_read2_b32 v[92:93], %[c0] offset0:48 offset1:49\n" \
  "ds_read2_b32 v[94:95], %[c0] offset0:50 offset1:51\n" \
  "ds_read_b128 v[120:123], %[d] offset:1280\n" \
  "v_mfma_f32_16x16x32_bf16 v[160:163], v[44:47], v[72:75], v[160:163]\n" \
  "ds_read2_b32 v[44:45], %[a2] offset0:48 offset1:49\n" \
  "ds_read2_b32 v[46:47], %[a2] offset0:50 offset1:51\n" \
  "v_mfma_f32_16x16x32_bf16 v[152:155], v[36:39], v[72:75], v[152:155]\n" \
  "ds_read_b128 v[72:75], %[b] offset:1792\n" \
  "v_mfma_f32_16x16x32_bf16 v[160:163], v[40:43], v[76:79], v[160:163]\n" \
  "ds_read2_b32 v[40:41], %[a1] offset0:176 offset1:177\n" \
  "ds_read2_b32 v[42:43], %[a1] offset0:178 offset1:179\n" \
  "v_mfma_f32_16x16x32_bf16 v[152:155], v[32:35], v[76:79], v[152:155]\n" \
  "ds_read_b128 v[76:79], %[b] offset:2304\n" \
  "v_mfma_f32_16x16x32_bf16 v[160:163], v[36:39], v[80:83], v[160:163]\n" \
  "ds_read2_b32 v[36:37], %[a1] offset0:48 offset1:49\n" \
  "ds_read2_b32 v[38:39], %[a1] offset0:50 offset1:51\n" \
  "v_mfma_f32_16x16x32_bf16 v[152:155], v[28:31], v[80:83], v[152:155]\n" \
  "ds_read_b128 v[80:83], %[b] offset:2816\n" \
  "v_mfma_f32_16x16x32_bf16 v[160:163], v[32:35], v[84:87], v[160:163]\n" \
  "ds_read2_b32 v[32:33], %[a0] offset0:176 offset1:177\n" \
  "ds_read2_b32 v[34:35], %[a0] offset0:178 offset1:179\n" \
  "ds_read_b128 v[84:87], %[b] offset:3328\n" \
  "v_mfma_f32_16x16x32_bf16 v[160:163], v[28:31], v[88:91], v[160:163]\n" \
  "ds_read2_b32 v[28:29], %[a0] offset0:48 offset1:49\n" \
  "ds_read2_b32 v[30:31], %[a0] offset0:50 offset1:51\n" \
  "ds_read_b128 v[88:91], %[b] offset:3840\n" \
  "v_add_u32 %[a0], 128, %[a0]\n" \
  "v_add_u32 %[a1], 128, %[a1]\n" \
  "v_add_u32 %[a2], 128, %[a2]\n" \
  "v_add_u32 %[a3], 128, %[a3]\n" \
  "v_subrev_u32 %[b], 128, %[b]\n" \
  "v_add_u32 %[c0], 128, %[c0]\n" \
  "v_add_u32 %[c1], 128, %[c1]\n" \
  "v_subrev_u32 %[d], 128, %[d]\n" \
  "s_sub_u32 %[cnt], %[cnt], 1\n" \
  "s_cmp_lg_u32 %[cnt], 0\n" \
  "s_cbranch_scc1 85b\n" \
  "s_waitcnt lgkmcnt(0)\n" \
  "v_mfma_f32_16x16x32_bf16 v[160:163], v[56:59], v[60:63], v[160:163]\n" \
  "v_mfma_f32_16x16x32_bf16 v[152:155], v[48:51], v[60:63], v[152:155]\n" \
  "v_mfma_f32_16x16x32_bf16 v[140:143], v[100:103], v[112:115], v[140:143]\n" \
  "v_mfma_f32_16x16x32_bf16 v[136:139], v[96:99], v[112:115], v[136:139]\n" \
  "v_mfma_f32_16x16x32_bf16 v[132:135], v[92:95], v[112:115], v[132:135]\n" \
  "v_mfma_f32_16x16x32_bf16 v[160:163], v[52:55], v[64:67], v[160:163]\n" \
  "v_mfma_f32_16x16x32_bf16 v[152:155], v[44:47], v[64:67], v[152:155]\n" \
  "v_mfma_f32_16x16x32_bf16 v[140:143], v[96:99], v[116:119], v[140:143]\n" \
  "v_mfma_f32_16x16x32_bf16 v[136:139], v[92:95], v[116:119], v[136:139]\n" \
  "v_mfma_f32_16x16x32_bf16 v[160:163], v[48:51], v[68:71], v[160:163]\n" \
  "v_mfma_f32_16x16x32_bf16 v[152:155], v[40:43], v[68:71], v[152:155]\n" \
  "v_mfma_f32_16x16x32_bf16 v[140:143], v[92:95], v[120:123], v[140:143]\n" \
  "v_mfma_f32_16x16x32_bf16 v[160:163], v[44:47], v[72:75], v[160:163]\n" \
  "v_mfma_f32_16x16x32_bf16 v[152:155], v[36:39], v[72:75], v[152:155]\n" \
  "v_mfma_f32_16x16x32_bf16 v[160:163], v[40:43], v[76:79], v[160:163]\n" \
  "v_mfma_f32_16x16x32_bf16 v[152:155], v[32:35], v[76:79], v[152:155]\n" \
  "v_mfma_f32_16x16x32_bf16 v[160:163], v[36:39], v[80:83], v[160:163]\n" \
  "v_mfma_f32_16x16x32_bf16 v[152:155], v[28:31], v[80:83], v[152:155]\n" \
  "v_mfma_f32_16x16x32_bf16 v[160:163], v[32:35], v[84:87], v[160:163]\n" \
  "v_mfma_f32_16x16x32_bf16 v[160:163], v[28:31], v[88:91], v[160:163]\n" \
  "s_nop 3\n" \
  "ds_read_b64 v[28:29], %[ue] offset:2560\n" \
  "ds_read_b64 v[30:31], %[ue] offset:3584\n" \
  "ds_read_b64 v[32:33], %[uf] offset:0\n" \
  "ds_read_b64 v[34:35], %[uf] offset:512\n" \
  "ds_read_b64 v[36:37], %[uf] offset:1024\n" \
  "s_waitcnt lgkmcnt(4)\n" \
  "v_and_b32 v92, 0xffff0000, v29\n" \
  "v_lshlrev_b32 v93, 16, v29\n" \
  "v_and_b32 v94, 0xffff0000, v28\n" \
  "v_lshlrev_b32 v95, 16, v28\n" \
  "v_fmac_f32 v152, %[d1], v92\n" \
  "v_fmac_f32 v153, %[d1], v93\n" \
  "v_fmac_f32 v154, %[d1], v94\n" \
  "v_fmac_f32 v155, %[d1], v95\n" \
  "v_cvt_pk_bf16_f32 v152, v152, v153\n" \
  "v_cvt_pk_bf16_f32 v153, v154, v155\n" \
  "ds_write_b64 %[zw], v[152:153]\n" \
  "ds_read_b128 v[60:63], %[zr]\n" \
  "s_waitcnt lgkmcnt(5)\n" \
  "v_and_b32 v92, 0xffff0000, v31\n" \
  "v_lshlrev_b32 v93, 16, v31\n" \
  "v_and_b32 v94, 0xffff0000, v30\n" \
  "v_lshlrev_b32 v95, 16, v30\n" \
  "v_fmac_f32 v160, %[d1], v92\n" \
  "v_fmac_f32 v161, %[d1], v93\n" \
  "v_fmac_f32 v162, %[d1], v94\n" \
  "v_fmac_f32 v163, %[d1], v95\n" \
  "v_cvt_pk_bf16_f32 v160, v160, v161\n" \
  "v_cvt_pk_bf16_f32 v161, v162, v163\n" \
  "ds_write_b64 %[zw], v[160:161]\n" \
  "ds_read_b128 v[64:67], %[zr]\n" \
  "s_waitcnt lgkmcnt(6)\n" \
  "v_and_b32 v92, 0xffff0000, v33\n" \
  "v_lshlrev_b32 v93, 16, v33\n" \
  "v_and_b32 v94, 0xffff0000, v32\n" \
  "v_lshlrev_b32 v95, 16, v32\n" \
  "v_fmac_f32 v132, %[d2], v92\n" \
  "v_fmac_f32 v133, %[d2], v93\n" \
  "v_fmac_f32 v134, %[d2], v94\n" \
  "v_fmac_f32 v135, %[d2], v95\n" \
  "v_cvt_pk_bf16_f32 v132, v132, v133\n" \
  "v_cvt_pk_bf16_f32 v133, v134, v135\n" \
  "ds_write_b64 %[zw], v[132:133]\n" \
  "ds_read_b128 v[68:71], %[zr]\n" \
  "s_waitcnt lgkmcnt(7)\n" \
  "v_and_b32 v92, 0xffff0000, v35\n" \
  "v_lshlrev_b32 v93, 16, v35\n" \
  "v_and_b32 v94, 0xffff0000, v34\n" \
  "v_lshlrev_b32 v95, 16, v34\n" \
  "v_fmac_f32 v136, %[d2], v92\n" \
  "v_fmac_f32 v137, %[d2], v93\n" \
  "v_fmac_f32 v138, %[d2], v94\n" \
  "v_fmac_f32 v139, %[d2], v95\n" \
  "v_cvt_pk_bf16_f32 v136, v136, v137\n" \
  "v_cvt_pk_bf16_f32 v137, v138, v139\n" \
  "ds_write_b64 %[zw], v[136:137]\n" \
  "ds_read_b128 v[72:75], %[zr]\n" \
  "s_waitcnt lgkmcnt(8)\n" \
  "v_and_b32 v92, 0xffff0000, v37\n" \
  "v_lshlrev_b32 v93, 16, v37\n" \
  "v_and_b32 v94, 0xffff0000, v36\n" \
  "v_lshlrev_b32 v95, 16, v36\n" \
  "v_fmac_f32 v140, %[d2], v92\n" \
  "v_fmac_f32 v141, %[d2], v93\n" \
  "v_fmac_f32 v142, %[d2], v94\n" \
  "v_fmac_f32 v143, %[d2], v95\n" \
  "v_cvt_pk_bf16_f32 v140, v140, v141\n" \
  "v_cvt_pk_bf16_f32 v141, v142, v143\n" \
  "ds_write_b64 %[zw], v[140:141]\n" \
  "ds_read_b128 v[76:79], %[zr]\n" \
  "s_mov_b32 exec_hi, 0\n" \
  "s_waitcnt lgkmcnt(8)\n" \
  "buffer_store_dwordx4 v[60:63], %[zo], %[rs], %[z1] offen offset:2560 sc1\n" \
  "s_waitcnt lgkmcnt(6)\n" \
  "buffer_store_dwordx4 v[64:67], %[zo], %[rs], %[z1] offen offset:3584 sc1\n" \
  "s_waitcnt lgkmcnt(4)\n" \
  "buffer_store_dwordx4 v[68:71], %[zo], %[rs], %[z2] offen offset:0 sc1\n" \
  "s_waitcnt lgkmcnt(2)\n" \
  "buffer_store_dwordx4 v[72:75], %[zo], %[rs], %[z2] offen offset:512 sc1\n" \
  "s_waitcnt lgkmcnt(0)\n" \
  "buffer_store_dwordx4 v[76:79], %[zo], %[rs], %[z2] offen offset:1024 sc1\n" \
  "s_mov_b64 exec, -1\n" \
  "s_branch 99f\n" \
  "66:\n" \
  "ds_read2_b32 v[52:53], %[a3] offset0:0 offset1:1\n" \
  "ds_read2_b32 v[54:55], %[a3] offset0:2 offset1:3\n" \
  "ds_read_b128 v[60:63], %[b] offset:448\n" \
  "ds_read2_b32 v[44:45], %[a2] offset0:0 offset1:1\n" \
  "ds_read2_b32 v[46:47], %[a2] offset0:2 offset1:3\n" \
  "ds_read2_b32 v[40:41], %[a1] offset0:128 offset1:129\n" \
  "ds_read2_b32 v[42:43], %[a1] offset0:130 offset1:131\n" \
  "ds_read2_b32 v[36:37], %[a1] offset0:0 offset1:1\n" \
  "ds_read2_b32 v[38:39], %[a1] offset0:2 offset1:3\n" \
  "ds_read2_b32 v[28:29], %[a0] offset0:0 offset1:1\n" \
  "ds_read2_b32 v[30:31], %[a0] offset0:2 offset1:3\n" \
  "ds_read2_b32 v[48:49], %[a2] offset0:128 offset1:129\n" \
  "ds_read2_b32 v[50:51], %[a2] offset0:130 offset1:131\n" \
  "ds_read_b128 v[64:67], %[b] offset:960\n" \
  "ds_read2_b32 v[32:33], %[a0] offset0:128 offset1:129\n" \
  "ds_read2_b32 v[34:35], %[a0] offset0:130 offset1:131\n" \
  "ds_read_b128 v[68:71], %[b] offset:1472\n" \
  "ds_read_b128 v[72:75], %[b] offset:1984\n" \
  "ds_read_b128 v[76:79], %[b] offset:2496\n" \
  "ds_read_b128 v[80:83], %[b] offset:3008\n" \
  "ds_read_b128 v[84:87], %[b] offset:3520\n" \
  "s_waitcnt lgkmcnt(0)\n" \
  "v_mfma_f32_16x16x32_bf16 v[156:159], v[52:55], v[60:63], 0\n" \
  "ds_read2_b32 v[52:53], %[a3] offset0:16 offset1:17\n" \
  "ds_read2_b32 v[54:55], %[a3] offset0:18 offset1:19\n" \
  "v_mfma_f32_16x16x32_bf16 v[148:151], v[44:47], v[60:63], 0\n" \
  "v_mfma_f32_16x16x32_bf16 v[144:147], v[40:43], v[60:63], 0\n" \
  "v_mfma_f32_16x16x32_bf16 v[140:143], v[36:39], v[60:63], 0\n" \
  "v_mfma_f32_16x16x32_bf16 v[132:135], v[28:31], v[60:63], 0\n" \
  "ds_read_b128 v[60:63], %[b] offset:384\n" \
  "v_mfma_f32_16x16x32_bf16 v[156:159], v[48:51], v[64:67], v[156:159]\n" \
  "ds_read2_b32 v[48:49], %[a2] offset0:144 offset1:145\n" \
  "ds_read2_b32 v[50:51], %[a2] offset0:146 offset1:147\n" \
  "v_mfma_f32_16x16x32_bf16 v[148:151], v[40:43], v[64:67], v[148:151]\n" \
  "v_mfma_f32_16x16x32_bf16 v[144:147], v[36:39], v[64:67], v[144:147]\n" \
  "v_mfma_f32_16x16x32_bf16 v[140:143], v[32:35], v[64:67], v[140:143]\n" \
  "ds_read_b128 v[64:67], %[b] offset:896\n" \
  "v_mfma_f32_16x16x32_bf16 v[156:159], v[44:47], v[68:71], v[156:159]\n" \
  "ds_read2_b32 v[44:45], %[a2] offset0:16 offset1:17\n" \
  "ds_read2_b32 v[46:47], %[a2] offset0:18 offset1:19\n" \
  "v_mfma_f32_16x16x32_bf16 v[148:151], v[36:39], v[68:71], v[148:151]\n" \
  "v_mfma_f32_16x16x32_bf16 v[144:147], v[32:35], v[68:71], v[144:147]\n" \
  "v_mfma_f32_16x16x32_bf16 v[140:143], v[28:31], v[68:71], v[140:143]\n" \
  "ds_read_b128 v[68:71], %[b] offset:1408\n" \
  "v_mfma_f32_16x16x32_bf16 v[156:159], v[40:43], v[72:75], v[156:159]\n" \
  "ds_read2_b32 v[40:41], %[a1] offset0:144 offset1:145\n" \
  "ds_read2_b32 v[42:43], %[a1] offset0:146 offset1:147\n" \
  "v_mfma_f32_16x16x32_bf16 v[148:151], v[32:35], v[72:75], v[148:151]\n" \
  "v_mfma_f32_16x16x32_bf16 v[144:147], v[28:31], v[72:75], v[144:147]\n" \
  "ds_read_b128 v[72:75], %[b] offset:1920\n" \
  "v_mfma_f32_16x16x32_bf16 v[156:159], v[36:39], v[76:79], v[156:159]\n" \
  "ds_read2_b32 v[36:37], %[a1] offset0:16 offset1:17\n" \
  "ds_read2_b32 v[38:39], %[a1] offset0:18 offset1:19\n" \
  "v_mfma_f32_16x16x32_bf16 v[148:151], v[28:31], v[76:79], v[148:151]\n" \
  "ds_read_b128 v[76:79], %[b] offset:2432\n" \
  "v_mfma_f32_16x16x32_bf16 v[156:159], v[32:35], v[80:83], v[156:159]\n" \
  "ds_read2_b32 v[32:33], %[a0] offset0:144 offset1:145\n" \
  "ds_read2_b32 v[34:35], %[a0] offset0:146 offset1:147\n" \
  "ds_read_b128 v[80:83], %[b] offset:2944\n" \
  "v_mfma_f32_16x16x32_bf16 v[156:159], v[28:31], v[84:87], v[156:159]\n" \
  "ds_read2_b32 v[28:29], %[a0] offset0:16 offset1:17\n" \
  "ds_read2_b32 v[30:31], %[a0] offset0:18 offset1:19\n" \
  "ds_read_b128 v[84:87], %[b] offset:3456\n" \
  "86:\n" \
  "s_waitcnt lgkmcnt(0)\n" \
  "v_mfma_f32_16x16x32_bf16 v[156:159], v[52:55], v[60:63], v[156:159]\n" \
  "ds_read2_b32 v[52:53], %[a3] offset0:32 offset1:33\n" \
  "ds_read2_b32 v[54:55], %[a3] offset0:34 offset1:35\n" \
  "v_mfma_f32_16x16x32_bf16 v[148:151], v[44:47], v[60:63], v[148:151]\n" \
  "v_mfma_f32_16x16x32_bf16 v[144:147], v[40:43], v[60:63], v[144:147]\n" \
  "v_mfma_f32_16x16x32_bf16 v[140:143], v[36:39], v[60:63], v[140:143]\n" \
  "v_mfma_f32_16x16x32_bf16 v[132:135], v[28:31], v[60:63], v[132:135]\n" \
  "ds_read_b128 v[60:63], %[b] offset:320\n" \
  "v_mfma_f32_16x16x32_bf16 v[156:159], v[48:51], v[64:67], v[156:159]\n" \
  "ds_read2_b32 v[48:49], %[a2] offset0:160 offset1:161\n" \
  "ds_read2_b32 v[50:51], %[a2] offset0:162 offset1:163\n" \
  "v_mfma_f32_16x16x32_bf16 v[148:151], v[40:43], v[64:67], v[148:151]\n" \
  "v_mfma_f32_16x16x32_bf16 v[144:147], v[36:39], v[64:67], v[144:147]\n" \
  "v_mfma_f32_16x16x32_bf16 v[140:143], v[32:35], v[64:67], v[140:143]\n" \
  "ds_read_b128 v[64:67], %[b] offset:832\n" \
  "v_mfma_f32_16x16x32_bf16 v[156:159], v[44:47], v[68:71], v[156:159]\n" \
  "ds_read2_b32 v[44:45], %[a2] offset0:32 offset1:33\n" \
  "ds_read2_b32 v[46:47], %[a2] offset0:34 offset1:35\n" \
  "v_mfma_f32_16x16x32_bf16 v[148:151], v[36:39], v[68:71], v[148:151]\n" \
  "v_mfma_f32_16x16x32_bf16 v[144:147], v[32:35], v[68:71], v[144:147]\n" \
  "v_mfma_f32_16x16x32_bf16 v[140:143], v[28:31], v[68:71], v[140:143]\n" \
  "ds_read_b128 v[68:71], %[b] offset:1344\n" \
  "v_mfma_f32_16x16x32_bf16 v[156:159], v[40:43], v[72:75], v[156:159]\n" \
  "ds_read2_b32 v[40:41], %[a1] offset0:160 offset1:161\n" \
  "ds_read2_b32 v[42:43], %[a1] offset0:162 offset1:163\n" \
  "v_mfma_f32_16x16x32_bf16 v[148:151], v[32:35], v[72:75], v[148:151]\n" \
  "v_mfma_f32_16x16x32_bf16 v[144:147], v[28:31], v[72:75], v[144:147]\n" \
  "ds_read_b128 v[72:75], %[b] offset:1856\n" \
  "v_mfma_f32_16x16x32_bf16 v[156:159], v[36:39], v[76:79], v[156:159]\n" \
  "ds_read2_b32 v[36:37], %[a1] offset0:32 offset1:33\n" \
  "ds_read2_b32 v[38:39], %[a1] offset0:34 offset1:35\n" \
  "v_mfma_f32_16x16x32_bf16 v[148:151], v[28:31], v[76:79], v[148:151]\n" \
  "ds_read_b128 v[76:79], %[b] offset:2368\n" \
  "v_mfma_f32_16x16x32_bf16 v[156:159], v[32:35], v[80:83], v[156:159]\n" \
  "ds_read2_b32 v[32:33], %[a0] offset0:160 offset1:161\n" \
  "ds_read2_b32 v[34:35], %[a0] offset0:162 offset1:163\n" \
  "ds_read_b128 v[80:83], %[b] offset:2880\n" \
  "v_mfma_f32_16x16x32_bf16 v[156:159], v[28:31], v[84:87], v[156:159]\n" \
  "ds_read2_b32 v[28:29], %[a0] offset0:32 offset1:33\n" \
  "ds_read2_b32 v[30:31], %[a0] offset0:34 offset1:35\n" \
  "ds_read_b128 v[84:87], %[b] offset:3392\n" \
  "s_waitcnt lgkmcnt(0)\n" \
  "v_mfma_f32_16x16x32_bf16 v[156:159], v[52:55], v[60:63], v[156:159]\n" \
  "ds_read2_b32 v[52:53], %[a3] offset0:48 offset1:49\n" \
  "ds_read2_b32 v[54:55], %[a3] offset0:50 offset1:51\n" \
  "v_mfma_f32_16x16x32_bf16 v[148:151], v[44:47], v[60:63], v[148:151]\n" \
  "v_mfma_f32_16x16x32_bf16 v[144:147], v[40:43], v[60:63], v[144:147]\n" \
  "v_mfma_f32_16x16x32_bf16 v[140:143], v[36:39], v[60:63], v[140:143]\n" \
  "v_mfma_f32_16x16x32_bf16 v[132:135], v[28:31], v[60:63], v[132:135]\n" \
  "ds_read_b128 v[60:63], %[b] offset:256\n" \
  "v_mfma_f32_16x16x32_bf16 v[156:159], v[48:51], v[64:67], v[156:159]\n" \
  "ds_read2_b32 v[48:49], %[a2] offset0:176 offset1:177\n" \
  "ds_read2_b32 v[50:51], %[a2] offset0:178 offset1:179\n" \
  "v_mfma_f32_16x16x32_bf16 v[148:151], v[40:43], v[64:67], v[148:151]\n" \
  "v_mfma_f32_16x16x32_bf16 v[144:147], v[36:39], v[64:67], v[144:147]\n" \
  "v_mfma_f32_16x16x32_bf16 v[140:143], v[32:35], v[64:67], v[140:143]\n" \
  "ds_read_b128 v[64:67], %[b] offset:768\n" \
  "v_mfma_f32_16x16x32_bf16 v[156:159], v[44:47], v[68:71], v[156:159]\n" \
  "ds_read2_b32 v[44:45], %[a2] offset0:48 offset1:49\n" \
  "ds_read2_b32 v[46:47], %[a2] offset0:50 offset1:51\n" \
  "v_mfma_f32_16x16x32_bf16 v[148:151], v[36:39], v[68:71], v[148:151]\n" \
  "v_mfma_f32_16x16x32_bf16 v[144:147], v[32:35], v[68:71], v[144:147]\n" \
  "v_mfma_f32_16x16x32_bf16 v[140:143], v[28:31], v[68:71], v[140:143]\n" \
  "ds_read_b128 v[68:71], %[b] offset:1280\n" \
  "v_mfma_f32_16x16x32_bf16 v[156:159], v[40:43], v[72:75], v[156:159]\n" \
  "ds_read2_b32 v[40:41], %[a1] offset0:176 offset1:177\n" \
  "ds_read2_b32 v[42:43], %[a1] offset0:178 offset1:179\n" \
  "v_mfma_f32_16x16x32_bf16 v[148:151], v[32:35], v[72:75], v[148:151]\n" \
  "v_mfma_f32_16x16x32_bf16 v[144:147], v[28:31], v[72:75], v[144:147]\n" \
  "ds_read_b128 v[72:75], %[b] offset:1792\n" \
  "v_mfma_f32_16x16x32_bf16 v[156:159], v[36:39], v[76:79], v[156:159]\n" \
  "ds_read2_b32 v[36:37], %[a1] offset0:48 offset1:49\n" \
  "ds_read2_b32 v[38:39], %[a1] offset0:50 offset1:51\n" \
  "v_mfma_f32_16x16x32_bf16 v[148:151], v[28:31], v[76:79], v[148:151]\n" \
  "ds_read_b128 v[76:79], %[b] offset:2304\n" \
  "v_mfma_f32_16x16x32_bf16 v[156:159], v[32:35], v[80:83], v[156:159]\n" \
  "ds_read2_b32 v[32:33], %[a0] offset0:176 offset1:177\n" \
  "ds_read2_b32 v[34:35], %[a0] offset0:178 offset1:179\n" \
  "ds_read_b128 v[80:83], %[b] offset:2816\n" \
  "v_mfma_f32_16x16x32_bf16 v[156:159], v[28:31], v[84:87], v[156:159]\n" \
  "ds_read2_b32 v[28:29], %[a0] offset0:48 offset1:49\n" \
  "ds_read2_b32 v[30:31], %[a0] offset0:50 offset1:51\n" \
  "ds_read_b128 v[84:87], %[b] offset:3328\n" \
  "v_add_u32 %[a0], 128, %[a0]\n" \
  "v_add_u32 %[a1], 128, %[a1]\n" \
  "v_add_u32 %[a2], 128, %[a2]\n" \
  "v_add_u32 %[a3], 128, %[a3]\n" \
  "v_subrev_u32 %[b], 128, %[b]\n" \
  "s_sub_u32 %[cnt], %[cnt], 1\n" \
  "s_cmp_lg_u32 %[cnt], 0\n" \
  "s_cbranch_scc1 86b\n" \
  "s_waitcnt lgkmcnt(0)\n" \
  "v_mfma_f32_16x16x32_bf16 v[156:159], v[52:55], v[60:63], v[156:159]\n" \
  "v_mfma_f32_16x16x32_bf16 v[148:151], v[44:47], v[60:63], v[148:151]\n" \
  "v_mfma_f32_16x16x32_bf16 v[144:147], v[40:43], v[60:63], v[144:147]\n" \
  "v_mfma_f32_16x16x32_bf16 v[140:143], v[36:39], v[60:63], v[140:143]\n" \
  "v_mfma_f32_16x16x32_bf16 v[132:135], v[28:31], v[60:63], v[132:135]\n" \
  "v_mfma_f32_16x16x32_bf16 v[156:159], v[48:51], v[64:67], v[156:159]\n" \
  "v_mfma_f32_16x16x32_bf16 v[148:151], v[40:43], v[64:67], v[148:151]\n" \
  "v_mfma_f32_16x16x32_bf16 v[144:147], v[36:39], v[64:67], v[144:147]\n" \
  "v_mfma_f32_16x16x32_bf16 v[140:143], v[32:35], v[64:67], v[140:143]\n" \
  "v_mfma_f32_16x16x32_bf16 v[156:159], v[44:47], v[68:71], v[156:159]\n" \
  "v_mfma_f32_16x16x32_bf16 v[148:151], v[36:39], v[68:71], v[148:151]\n" \
  "v_mfma_f32_16x16x32_bf16 v[144:147], v[32:35], v[68:71], v[144:147]\n" \
  "v_mfma_f32_16x16x32_bf16 v[140:143], v[28:31], v[68:71], v[140:143]\n" \
  "v_mfma_f32_16x16x32_bf16 v[156:159], v[40:43], v[72:75], v[156:159]\n" \
  "v_mfma_f32_16x16x32_bf16 v[148:151], v[32:35], v[72:75], v[148:151]\n" \
  "v_mfma_f32_16x16x32_bf16 v[144:147], v[28:31], v[72:75], v[144:147]\n" \
  "v_mfma_f32_16x16x32_bf16 v[156:159], v[36:39], v[76:79], v[156:159]\n" \
  "v_mfma_f32_16x16x32_bf16 v[148:151], v[28:31], v[76:79], v[148:151]\n" \
  "v_mfma_f32_16x16x32_bf16 v[156:159], v[32:35], v[80:83], v[156:159]\n" \
  "v_mfma_f32_16x16x32_bf16 v[156:159], v[28:31], v[84:87], v[156:159]\n" \
  "s_nop 3\n" \
  "ds_read_b64 v[28:29], %[ue] offset:0\n" \
  "ds_read_b64 v[30:31], %[ue] offset:1024\n" \
  "ds_read_b64 v[32:33], %[ue] offset:1536\n" \
  "ds_read_b64 v[34:35], %[ue] offset:2048\n" \
  "ds_read_b64 v[36:37], %[ue] offset:3072\n" \
  "s_waitcnt lgkmcnt(4)\n" \
  "v_and_b32 v92, 0xffff0000, v29\n" \
  "v_lshlrev_b32 v93, 16, v29\n" \
  "v_and_b32 v94, 0xffff0000, v28\n" \
  "v_lshlrev_b32 v95, 16, v28\n" \
  "v_fmac_f32 v132, %[d1], v92\n" \
  "v_fmac_f32 v133, %[d1], v93\n" \
  "v_fmac_f32 v134, %[d1], v94\n" \
  "v_fmac_f32 v135, %[d1], v95\n" \
  "v_cvt_pk_bf16_f32 v132, v132, v133\n" \
  "v_cvt_pk_bf16_f32 v133, v134, v135\n" \
  "ds_write_b64 %[zw], v[132:133]\n" \
  "ds_read_b128 v[60:63], %[zr]\n" \
  "s_waitcnt lgkmcnt(5)\n" \
  "v_and_b32 v92, 0xffff0000, v31\n" \
  "v_lshlrev_b32 v93, 16, v31\n" \
  "v_and_b32 v94, 0xffff0000, v30\n" \
  "v_lshlrev_b32 v95, 16, v30\n" \
  "v_fmac_f32 v140, %[d1], v92\n" \
  "v_fmac_f32 v141, %[d1], v93\n" \
  "v_fmac_f32 v142, %[d1], v94\n" \
  "v_fmac_f32 v143, %[d1], v95\n" \
  "v_cvt_pk_bf16_f32 v140, v140, v141\n" \
  "v_cvt_pk_bf16_f32 v141, v142, v143\n" \
  "ds_write_b64 %[zw], v[140:141]\n" \
  "ds_read_b128 v[64:67], %[zr]\n" \
  "s_waitcnt lgkmcnt(6)\n" \
  "v_and_b32 v92, 0xffff0000, v33\n" \
  "v_lshlrev_b32 v93, 16, v33\n" \
  "v_and_b32 v94, 0xffff0000, v32\n" \
  "v_lshlrev_b32 v95, 16, v32\n" \
  "v_fmac_f32 v144, %[d1], v92\n" \
  "v_fmac_f32 v145, %[d1], v93\n" \
  "v_fmac_f32 v146, %[d1], v94\n" \
  "v_fmac_f32 v147, %[d1], v95\n" \
  "v_cvt_pk_bf16_f32 v144, v144, v145\n" \
  "v_cvt_pk_bf16_f32 v145, v146, v147\n" \
  "ds_write_b64 %[zw], v[144:145]\n" \
  "ds_read_b128 v[68:71], %[zr]\n" \
  "s_waitcnt lgkmcnt(7)\n" \
  "v_and_b32 v92, 0xffff0000, v35\n" \
  "v_lshlrev_b32 v93, 16, v35\n" \
  "v_and_b32 v94, 0xffff0000, v34\n" \
  "v_lshlrev_b32 v95, 16, v34\n" \
  "v_fmac_f32 v148, %[d1], v92\n" \
  "v_fmac_f32 v149, %[d1], v93\n" \
  "v_fmac_f32 v150, %[d1], v94\n" \
  "v_fmac_f32 v151, %[d1], v95\n" \
  "v_cvt_pk_bf16_f32 v148, v148, v149\n" \
  "v_cvt_pk_bf16_f32 v149, v150, v151\n" \
  "ds_write_b64 %[zw], v[148:149]\n" \
  "ds_read_b128 v[72:75], %[zr]\n" \
  "s_waitcnt lgkmcnt(8)\n" \
  "v_and_b32 v92, 0xffff0000, v37\n" \
  "v_lshlrev_b32 v93, 16, v37\n" \
  "v_and_b32 v94, 0xffff0000, v36\n" \
  "v_lshlrev_b32 v95, 16, v36\n" \
  "v_fmac_f32 v156, %[d1], v92\n" \
  "v_fmac_f32 v157, %[d1], v93\n" \
  "v_fmac_f32 v158, %[d1], v94\n" \
  "v_fmac_f32 v159, %[d1], v95\n" \
  "v_cvt_pk_bf16_f32 v156, v156, v157\n" \
  "v_cvt_pk_bf16_f32 v157, v158, v159\n" \
  "ds_write_b64 %[zw], v[156:157]\n" \
  "ds_read_b128 v[76:79], %[zr]\n" \
  "s_mov_b32 exec_hi, 0\n" \
  "s_waitcnt lgkmcnt(8)\n" \
  "buffer_store_dwordx4 v[60:63], %[zo], %[rs], %[z1] offen offset:0 sc1\n" \
  "s_waitcnt lgkmcnt(6)\n" \
  "buffer_store_dwordx4 v[64:67], %[zo], %[rs], %[z1] offen offset:1024 sc1\n" \
  "s_waitcnt lgkmcnt(4)\n" \
  "buffer_store_dwordx4 v[68:71], %[zo], %[rs], %[z1] offen offset:1536 sc1\n" \
  "s_waitcnt lgkmcnt(2)\n" \
  "buffer_store_dwordx4 v[72:75], %[zo], %[rs], %[z1] offen offset:2048 sc1\n" \
  "s_waitcnt lgkmcnt(0)\n" \
  "buffer_store_dwordx4 v[76:79], %[zo], %[rs], %[z1] offen offset:3072 sc1\n" \
  "s_mov_b64 exec, -1\n" \
  "s_branch 99f\n" \
  "67:\n" \
  "ds_read2_b32 v[56:57], %[a3] offset0:128 offset1:129\n" \
  "ds_read2_b32 v[58:59], %[a3] offset0:130 offset1:131\n" \
  "ds_read_b128 v[60:63], %[b] offset:448\n" \
  "ds_read2_b32 v[52:53], %[a3] offset0:0 offset1:1\n" \
  "ds_read2_b32 v[54:55], %[a3] offset0:2 offset1:3\n" \
  "ds_read2_b32 v[36:37], %[a1] offset0:0 offset1:1\n" \
  "ds_read2_b32 v[38:39], %[a1] offset0:2 offset1:3\n" \
  "ds_read2_b32 v[96:97], %[c0] offset0:128 offset1:129\n" \
  "ds_read2_b32 v[98:99], %[c0] offset0:130 offset1:131\n" \
  "ds_read_b128 v[112:115], %[d] offset:448\n" \
  "ds_read_b128 v[64:67], %[b] offset:960\n" \
  "ds_read2_b32 v[48:49], %[a2] offset0:128 offset1:129\n" \
  "ds_read2_b32 v[50:51], %[a2] offset0:130 offset1:131\n" \
  "ds_read2_b32 v[32:33], %[a0] offset0:128 offset1:129\n" \
  "ds_read2_b32 v[34:35], %[a0] offset0:130 offset1:131\n" \
  "ds_read2_b32 v[92:93], %[c0] offset0:0 offset1:1\n" \
  "ds_read2_b32 v[94:95], %[c0] offset0:2 offset1:3\n" \
  "ds_read_b128 v[116:119], %[d] offset:960\n" \
  "ds_read_b128 v[68:71], %[b] offset:1472\n" \
  "ds_read2_b32 v[44:45], %[a2] offset0:0 offset1:1\n" \
  "ds_read2_b32 v[46:47], %[a2] offset0:2 offset1:3\n" \
  "ds_read2_b32 v[28:29], %[a0] offset0:0 offset1:1\n" \
  "ds_read2_b32 v[30:31], %[a0] offset0:2 offset1:3\n" \
  "ds_read_b128 v[72:75], %[b] offset:1984\n" \
  "ds_read2_b32 v[40:41], %[a1] offset0:128 offset1:129\n" \
  "ds_read2_b32 v[42:43], %[a1] offset0:130 offset1:131\n" \
  "ds_read_b128 v[76:79], %[b] offset:2496\n" \
  "ds_read_b128 v[80:83], %[b] offset:3008\n" \
  "ds_read_b128 v[84:87], %[b] offset:3520\n" \
  "ds_read_b128 v[88:91], %[b] offset:4032\n" \
  "s_waitcnt lgkmcnt(0)\n" \
  "v_mfma_f32_16x16x32_bf16 v[160:163], v[56:59], v[60:63], 0\n" \
  "ds_read2_b32 v[56:57], %[a3] offset0:144 offset1:145\n" \
  "ds_read2_b32 v[58:59], %[a3] offset0:146 offset1:147\n" \
  "v_mfma_f32_16x16x32_bf16 v[156:159], v[52:55], v[60:63], 0\n" \
  "v_mfma_f32_16x16x32_bf16 v[140:143], v[36:39], v[60:63], 0\n" \
  "ds_read_b128 v[60:63], %[b] offset:384\n" \
  "v_mfma_f32_16x16x32_bf16 v[136:139], v[96:99], v[112:115], 0\n" \
  "ds_read2_b32 v[96:97], %[c0] offset0:144 offset1:145\n" \
  "ds_read2_b32 v[98:99], %[c0] offset0:146 offset1:147\n" \
  "ds_read_b128 v[112:115], %[d] offset:384\n" \
  "v_mfma_f32_16x16x32_bf16 v[160:163], v[52:55], v[64:67], v[160:163]\n" \
  "ds_read2_b32 v[52:53], %[a3] offset0:16 offset1:17\n" \
  "ds_read2_b32 v[54:55], %[a3] offset0:18 offset1:19\n" \
  "v_mfma_f32_16x16x32_bf16 v[156:159], v[48:51], v[64:67], v[156:159]\n" \
  "v_mfma_f32_16x16x32_bf16 v[140:143], v[32:35], v[64:67], v[140:143]\n" \
  "ds_read_b128 v[64:67], %[b] offset:896\n" \
  "v_mfma_f32_16x16x32_bf16 v[136:139], v[92:95], v[116:119], v[136:139]\n" \
  "ds_read2_b32 v[92:93], %[c0] offset0:16 offset1:17\n" \
  "ds_read2_b32 v[94:95], %[c0] offset0:18 offset1:19\n" \
  "ds_read_b128 v[116:119], %[d] offset:896\n" \
  "v_mfma_f32_16x16x32_bf16 v[160:163], v[48:51], v[68:71], v[160:163]\n" \
  "ds_read2_b32 v[48:49], %[a2] offset0:144 offset1:145\n" \
  "ds_read2_b32 v[50:51], %[a2] offset0:146 offset1:147\n" \
  "v_mfma_f32_16x16x32_bf16 v[156:159], v[44:47], v[68:71], v[156:159]\n" \
  "v_mfma_f32_16x16x32_bf16 v[140:143], v[28:31], v[68:71], v[140:143]\n" \
  "ds_read_b128 v[68:71], %[b] offset:1408\n" \
  "v_mfma_f32_16x16x32_bf16 v[160:163], v[44:47], v[72:75], v[160:163]\n" \
  "ds_read2_b32 v[44:45], %[a2] offset0:16 offset1:17\n" \
  "ds_read2_b32 v[46:47], %[a2] offset0:18 offset1:19\n" \
  "v_mfma_f32_16x16x32_bf16 v[156:159], v[40:43], v[72:75], v[156:159]\n" \
  "ds_read_b128 v[72:75], %[b] offset:1920\n" \
  "v_mfma_f32_16x16x32_bf16 v[160:163], v[40:43], v[76:79], v[160:163]\n" \
  "ds_read2_b32 v[40:41], %[a1] offset0:144 offset1:145\n" \
  "ds_read2_b32 v[42:43], %[a1] offset0:146 offset1:147\n" \
  "v_mfma_f32_16x16x32_bf16 v[156:159], v[36:39], v[76:79], v[156:159]\n" \
  "ds_read_b128 v[76:79], %[b] offset:2432\n" \
  "v_mfma_f32_16x16x32_bf16 v[160:163], v[36:39], v[80:83], v[160:163]\n" \
  "ds_read2_b32 v[36:37], %[a1] offset0:16 offset1:17\n" \
  "ds_read2_b32 v[38:39], %[a1] offset0:18 offset1:19\n" \
  "v_mfma_f32_16x16x32_bf16 v[156:159], v[32:35], v[80:83], v[156:159]\n" \
  "ds_read_b128 v[80:83], %[b] offset:2944\n" \
  "v_mfma_f32_16x16x32_bf16 v[160:163], v[32:35], v[84:87], v[160:163]\n" \
  "ds_read2_b32 v[32:33], %[a0] offset0:144 offset1:145\n" \
  "ds_read2_b32 v[34:35], %[a0] offset0:146 offset1:147\n" \
  "v_mfma_f32_16x16x32_bf16 v[156:159], v[28:31], v[84:87], v[156:159]\n" \
  "ds_read_b128 v[84:87], %[b] offset:3456\n" \
  "v_mfma_f32_16x16x32_bf16 v[160:163], v[28:31], v[88:91], v[160:163]\n" \
  "ds_read2_b32 v[28:29], %[a0] offset0:16 offset1:17\n" \
  "ds_read2_b32 v[30:31], %[a0] offset0:18 offset1:19\n" \
  "ds_read_b128 v[88:91], %[b] offset:3968\n" \
  "87:\n" \
  "s_waitcnt lgkmcnt(0)\n" \
  "v_mfma_f32_16x16x32_bf16 v[160:163], v[56:59], v[60:63], v[160:163]\n" \
  "ds_read2_b32 v[56:57], %[a3] offset0:160 offset1:161\n" \
  "ds_read2_b32 v[58:59], %[a3] offset0:162 offset1:163\n" \
  "v_mfma_f32_16x16x32_bf16 v[156:159], v[52:55], v[60:63], v[156:159]\n" \
  "v_mfma_f32_16x16x32_bf16 v[140:143], v[36:39], v[60:63], v[140:143]\n" \
  "ds_read_b128 v[60:63], %[b] offset:320\n" \
  "v_mfma_f32_16x16x32_bf16 v[136:139], v[96:99], v[112:115], v[136:139]\n" \
  "ds_read2_b32 v[96:97], %[c0] offset0:160 offset1:161\n" \
  "ds_read2_b32 v[98:99], %[c0] offset0:162 offset1:163\n" \
  "ds_read_b128 v[112:115], %[d] offset:320\n" \
  "v_mfma_f32_16x16x32_bf16 v[160:163], v[52:55], v[64:67], v[160:163]\n" \
  "ds_read2_b32 v[52:53], %[a3] offset0:32 offset1:33\n" \
  "ds_read2_b32 v[54:55], %[a3] offset0:34 offset1:35\n" \
  "v_mfma_f32_16x16x32_bf16 v[156:159], v[48:51], v[64:67], v[156:159]\n" \
  "v_mfma_f32_16x16x32_bf16 v[140:143], v[32:35], v[64:67], v[140:143]\n" \
  "ds_read_b128 v[64:67], %[b] offset:832\n" \
  "v_mfma_f32_16x16x32_bf16 v[136:139], v[92:95], v[116:119], v[136:139]\n" \
  "ds_read2_b32 v[92:93], %[c0] offset0:32 offset1:33\n" \
  "ds_read2_b32 v[94:95], %[c0] offset0:34 offset1:35\n" \
  "ds_read_b128 v[116:119], %[d] offset:832\n" \
  "v_mfma_f32_16x16x32_bf16 v[160:163], v[48:51], v[68:71], v[160:163]\n" \
  "ds_read2_b32 v[48:49], %[a2] offset0:160 offset1:161\n" \
  "ds_read2_b32 v[50:51], %[a2] offset0:162 offset1:163\n" \
  "v_mfma_f32_16x16x32_bf16 v[156:159], v[44:47], v[68:71], v[156:159]\n" \
  "v_mfma_f32_16x16x32_bf16 v[140:143], v[28:31], v[68:71], v[140:143]\n" \
  "ds_read_b128 v[68:71], %[b] offset:1344\n" \
  "v_mfma_f32_16x16x32_bf16 v[160:163], v[44:47], v[72:75], v[160:163]\n" \
  "ds_read2_b32 v[44:45], %[a2] offset0:32 offset1:33\n" \
  "ds_read2_b32 v[46:47], %[a2] offset0:34 offset1:35\n" \
  "v_mfma_f32_16x16x32_bf16 v[156:159], v[40:43], v[72:75], v[156:159]\n" \
  "ds_read_b128 v[72:75], %[b] offset:1856\n" \
  "v_mfma_f32_16x16x32_bf16 v[160:163], v[40:43], v[76:79], v[160:163]\n" \
  "ds_read2_b32 v[40:41], %[a1] offset0:160 offset1:161\n" \
  "ds_read2_b32 v[42:43], %[a1] offset0:162 offset1:163\n" \
  "v_mfma_f32_16x16x32_bf16 v[156:159], v[36:39], v[76:79], v[156:159]\n" \
  "ds_read_b128 v[76:79], %[b] offset:2368\n" \
  "v_mfma_f32_16x16x32_bf16 v[160:163], v[36:39], v[80:83], v[160:163]\n" \
  "ds_read2_b32 v[36:37], %[a1] offset0:32 offset1:33\n" \
  "ds_read2_b32 v[38:39], %[a1] offset0:34 offset1:35\n" \
  "v_mfma_f32_16x16x32_bf16 v[156:159], v[32:35], v[80:83], v[156:159]\n" \
  "ds_read_b128 v[80:83], %[b] offset:2880\n" \
  "v_mfma_f32_16x16x32_bf16 v[160:163], v[32:35], v[84:87], v[160:163]\n" \
  "ds_read2_b32 v[32:33], %[a0] offset0:160 offset1:161\n" \
  "ds_read2_b32 v[34:35], %[a0] offset0:162 offset1:163\n" \
  "v_mfma_f32_16x16x32_bf16 v[156:159], v[28:31], v[84:87], v[156:159]\n" \
  "ds_read_b128 v[84:87], %[b] offset:3392\n" \
  "v_mfma_f32_16x16x32_bf16 v[160:163], v[28:31], v[88:91], v[160:163]\n" \
  "ds_read2_b32 v[28:29], %[a0] offset0:32 offset1:33\n" \
  "ds_read2_b32 v[30:31], %[a0] offset0:34 offset1:35\n" \
  "ds_read_b128 v[88:91], %[b] offset:3904\n" \
  "s_waitcnt lgkmcnt(0)\n" \
  "v_mfma_f32_16x16x32_bf16 v[160:163], v[56:59], v[60:63], v[160:163]\n" \
  "ds_read2_b32 v[56:57], %[a3] offset0:176 offset1:177\n" \
  "ds_read2_b32 v[58:59], %[a3] offset0:178 offset1:179\n" \
  "v_mfma_f32_16x16x32_bf16 v[156:159], v[52:55], v[60:63], v[156:159]\n" \
  "v_mfma_f32_16x16x32_bf16 v[140:143], v[36:39], v[60:63], v[140:143]\n" \
  "ds_read_b128 v[60:63], %[b] offset:256\n" \
  "v_mfma_f32_16x16x32_bf16 v[136:139], v[96:99], v[112:115], v[136:139]\n" \
  "ds_read2_b32 v[96:97], %[c0] offset0:176 offset1:177\n" \
  "ds_read2_b32 v[98:99], %[c0] offset0:178 offset1:179\n" \
  "ds_read_b128 v[112:115], %[d] offset:256\n" \
  "v_mfma_f32_16x16x32_bf16 v[160:163], v[52:55], v[64:67], v[160:163]\n" \
  "ds_read2_b32 v[52:53], %[a3] offset0:48 offset1:49\n" \
  "ds_read2_b32 v[54:55], %[a3] offset0:50 offset1:51\n" \
  "v_mfma_f32_16x16x32_bf16 v[156:159], v[48:51], v[64:67], v[156:159]\n" \
  "v_mfma_f32_16x16x32_bf16 v[140:143], v[32:35], v[64:67], v[140:143]\n" \
  "ds_read_b128 v[64:67], %[b] offset:768\n" \
  "v_mfma_f32_16x16x32_bf16 v[136:139], v[92:95], v[116:119], v[136:139]\n" \
  "ds_read2_b32 v[92:93], %[c0] offset0:48 offset1:49\n" \
  "ds_read2_b32 v[94:95], %[c0] offset0:50 offset1:51\n" \
  "ds_read_b128 v[116:119], %[d] offset:768\n" \
  "v_mfma_f32_16x16x32_bf16 v[160:163], v[48:51], v[68:71], v[160:163]\n" \
  "ds_read2_b32 v[48:49], %[a2] offset0:176 offset1:177\n" \
  "ds_read2_b32 v[50:51], %[a2] offset0:178 offset1:179\n" \
  "v_mfma_f32_16x16x32_bf16 v[156:159], v[44:47], v[68:71], v[156:159]\n" \
  "v_mfma_f32_16x16x32_bf16 v[140:143], v[28:31], v[68:71], v[140:143]\n" \
  "ds_read_b128 v[68:71], %[b] offset:1280\n" \
  "v_mfma_f32_16x16x32_bf16 v[160:163], v[44:47], v[72:75], v[160:163]\n" \
  "ds_read2_b32 v[44:45], %[a2] offset0:48 offset1:49\n" \
  "ds_read2_b32 v[46:47], %[a2] offset0:50 offset1:51\n" \
  "v_mfma_f32_16x16x32_bf16 v[156:159], v[40:43], v[72:75], v[156:159]\n" \
  "ds_read_b128 v[72:75], %[b] offset:1792\n" \
  "v_mfma_f32_16x16x32_bf16 v[160:163], v[40:43], v[76:79], v[160:163]\n" \
  "ds_read2_b32 v[40:41], %[a1] offset0:176 offset1:177\n" \
  "ds_read2_b32 v[42:43], %[a1] offset0:178 offset1:179\n" \
  "v_mfma_f32_16x16x32_bf16 v[156:159], v[36:39], v[76:79], v[156:159]\n" \
  "ds_read_b128 v[76:79], %[b] offset:2304\n" \
  "v_mfma_f32_16x16x32_bf16 v[160:163], v[36:39], v[80:83], v[160:163]\n" \
  "ds_read2_b32 v[36:37], %[a1] offset0:48 offset1:49\n" \
  "ds_read2_b32 v[38:39], %[a1] offset0:50 offset1:51\n" \
  "v_mfma_f32_16x16x32_bf16 v[156:159], v[32:35], v[80:83], v[156:159]\n" \
  "ds_read_b128 v[80:83], %[b] offset:2816\n" \
  "v_mfma_f32_16x16x32_bf16 v[160:163], v[32:35], v[84:87], v[160:163]\n" \
  "ds_read2_b32 v[32:33], %[a0] offset0:176 offset1:177\n" \
  "ds_read2_b32 v[34:35], %[a0] offset0:178 offset1:179\n" \
  "v_mfma_f32_16x16x32_bf16 v[156:159], v[28:31], v[84:87], v[156:159]\n" \
  "ds_read_b128 v[84:87], %[b] offset:3328\n" \
  "v_mfma_f32_16x16x32_bf16 v[160:163], v[28:31], v[88:91], v[160:163]\n" \
  "ds_read2_b32 v[28:29], %[a0] offset0:48 offset1:49\n" \
  "ds_read2_b32 v[30:31], %[a0] offset0:50 offset1:51\n" \
  "ds_read_b128 v[88:91], %[b] offset:3840\n" \
  "v_add_u32 %[a0], 128, %[a0]\n" \
  "v_add_u32 %[a1], 128, %[a1]\n" \
  "v_add_u32 %[a2], 128, %[a2]\n" \
  "v_add_u32 %[a3], 128, %[a3]\n" \
  "v_subrev_u32 %[b], 128, %[b]\n" \
  "v_add_u32 %[c0], 128, %[c0]\n" \
  "v_subrev_u32 %[d], 128, %[d]\n" \
  "s_sub_u32 %[cnt], %[cnt], 1\n" \
  "s_cmp_lg_u32 %[cnt], 0\n" \
  "s_cbranch_scc1 87b\n" \
  "s_waitcnt lgkmcnt(0)\n" \
  "v_mfma_f32_16x16x32_bf16 v[160:163], v[56:59], v[60:63], v[160:163]\n" \
  "v_mfma_f32_16x16x32_bf16 v[156:159], v[52:55], v[60:63], v[156:159]\n" \
  "v_mfma_f32_16x16x32_bf16 v[140:143], v[36:39], v[60:63], v[140:143]\n" \
  "v_mfma_f32_16x16x32_bf16 v[136:139], v[96:99], v[112:115], v[136:139]\n" \
  "v_mfma_f32_16x16x32_bf16 v[160:163], v[52:55], v[64:67], v[160:163]\n" \
  "v_mfma_f32_16x16x32_bf16 v[156:159], v[48:51], v[64:67], v[156:159]\n" \
  "v_mfma_f32_16x16x32_bf16 v[140:143], v[32:35], v[64:67], v[140:143]\n" \
  "v_mfma_f32_16x16x32_bf16 v[136:139], v[92:95], v[116:119], v[136:139]\n" \
  "v_mfma_f32_16x16x32_bf16 v[160:163], v[48:51], v[68:71], v[160:163]\n" \
  "v_mfma_f32_16x16x32_bf16 v[156:159], v[44:47], v[68:71], v[156:159]\n" \
  "v_mfma_f32_16x16x32_bf16 v[140:143], v[28:31], v[68:71], v[140:143]\n" \
  "v_mfma_f32_16x16x32_bf16 v[160:163], v[44:47], v[72:75], v[160:163]\n" \
  "v_mfma_f32_16x16x32_bf16 v[156:159], v[40:43], v[72:75], v[156:159]\n" \
  "v_mfma_f32_16x16x32_bf16 v[160:163], v[40:43], v[76:79], v[160:163]\n" \
  "v_mfma_f32_16x16x32_bf16 v[156:159], v[36:39], v[76:79], v[156:159]\n" \
  "v_mfma_f32_16x16x32_bf16 v[160:163], v[36:39], v[80:83], v[160:163]\n" \
  "v_mfma_f32_16x16x32_bf16 v[156:159], v[32:35], v[80:83], v[156:159]\n" \
  "v_mfma_f32_16x16x32_bf16 v[160:163], v[32:35], v[84:87], v[160:163]\n" \
  "v_mfma_f32_16x16x32_bf16 v[156:159], v[28:31], v[84:87], v[156:159]\n" \
  "v_mfma_f32_16x16x32_bf16 v[160:163], v[28:31], v[88:91], v[160:163]\n" \
  "s_nop 3\n" \
  "ds_read_b64 v[28:29], %[ue] offset:1024\n" \
  "ds_read_b64 v[30:31], %[ue] offset:3072\n" \
  "ds_read_b64 v[32:33], %[ue] offset:3584\n" \
  "ds_read_b64 v[34:35], %[uf] offset:512\n" \
  "s_waitcnt lgkmcnt(3)\n" \
  "v_and_b32 v92, 0xffff0000, v29\n" \
  "v_lshlrev_b32 v93, 16, v29\n" \
  "v_and_b32 v94, 0xffff0000, v28\n" \
  "v_lshlrev_b32 v95, 16, v28\n" \
  "v_fmac_f32 v140, %[d1], v92\n" \
  "v_fmac_f32 v141, %[d1], v93\n" \
  "v_fmac_f32 v142, %[d1], v94\n" \
  "v_fmac_f32 v143, %[d1], v95\n" \
  "v_cvt_pk_bf16_f32 v140, v140, v141\n" \
  "v_cvt_pk_bf16_f32 v141, v142, v143\n" \
  "ds_write_b64 %[zw], v[140:141]\n" \
  "ds_read_b128 v[60:63], %[zr]\n" \
  "s_waitcnt lgkmcnt(4)\n" \
  "v_and_b32 v92, 0xffff0000, v31\n" \
  "v_lshlrev_b32 v93, 16, v31\n" \
  "v_and_b32 v94, 0xffff0000, v30\n" \
  "v_lshlrev_b32 v95, 16, v30\n" \
  "v_fmac_f32 v156, %[d1], v92\n" \
  "v_fmac_f32 v157, %[d1], v93\n" \
  "v_fmac_f32 v158, %[d1], v94\n" \
  "v_fmac_f32 v159, %[d1], v95\n" \
  "v_cvt_pk_bf16_f32 v156, v156, v157\n" \
  "v_cvt_pk_bf16_f32 v157, v158, v159\n" \
  "ds_write_b64 %[zw], v[156:157]\n" \
  "ds_read_b128 v[64:67], %[zr]\n" \
  "s_waitcnt lgkmcnt(5)\n" \
  "v_and_b32 v92, 0xffff0000, v33\n" \
  "v_lshlrev_b32 v93, 16, v33\n" \
  "v_and_b32 v94, 0xffff0000, v32\n" \
  "v_lshlrev_b32 v95, 16, v32\n" \
  "v_fmac_f32 v160, %[d1], v92\n" \
  "v_fmac_f32 v161, %[d1], v93\n" \
  "v_fmac_f32 v162, %[d1], v94\n" \
  "v_fmac_f32 v163, %[d1], v95\n" \
  "v_cvt_pk_bf16_f32 v160, v160, v161\n" \
  "v_cvt_pk_bf16_f32 v161, v162, v163\n" \
  "ds_write_b64 %[zw], v[160:161]\n" \
  "ds_read_b128 v[68:71], %[zr]\n" \
  "s_waitcnt lgkmcnt(6)\n" \
  "v_and_b32 v92, 0xffff0000, v35\n" \
  "v_lshlrev_b32 v93, 16, v35\n" \
  "v_and_b32 v94, 0xffff0000, v34\n" \
  "v_lshlrev_b32 v95, 16, v34\n" \
  "v_fmac_f32 v136, %[d2], v92\n" \
  "v_fmac_f32 v137, %[d2], v93\n" \
  "v_fmac_f32 v138, %[d2], v94\n" \
  "v_fmac_f32 v139, %[d2], v95\n" \
  "v_cvt_pk_bf16_f32 v136, v136, v137\n" \
  "v_cvt_pk_bf16_f32 v137, v138, v139\n" \
  "ds_write_b64 %[zw], v[136:137]\n" \
  "ds_read_b128 v[72:75], %[zr]\n" \
  "s_mov_b32 exec_hi, 0\n" \
  "s_waitcnt lgkmcnt(6)\n" \
  "buffer_store_dwordx4 v[60:63], %[zo], %[rs], %[z1] offen offset:1024 sc1\n" \
  "s_waitcnt lgkmcnt(4)\n" \
  "buffer_store_dwordx4 v[64:67], %[zo], %[rs], %[z1] offen offset:3072 sc1\n" \
  "s_waitcnt lgkmcnt(2)\n" \
  "buffer_store_dwordx4 v[68:71], %[zo], %[rs], %[z1] offen offset:3584 sc1\n" \
  "s_waitcnt lgkmcnt(0)\n" \
  "buffer_store_dwordx4 v[72:75], %[zo], %[rs], %[z2] offen offset:512 sc1\n" \
  "s_mov_b64 exec, -1\n" \
  "s_branch 99f\n" \
  "68:\n" \
  "ds_read2_b32 v[52:53], %[a3] offset0:0 offset1:1\n" \
  "ds_read2_b32 v[54:55], %[a3] offset0:2 offset1:3\n" \
  "ds_read_b128 v[60:63], %[b] offset:448\n" \
  "ds_read2_b32 v[44:45], %[a2] offset0:0 offset1:1\n" \
  "ds_read2_b32 v[46:47], %[a2] offset0:2 offset1:3\n" \
  "ds_read2_b32 v[40:41], %[a1] offset0:128 offset1:129\n" \
  "ds_read2_b32 v[42:43], %[a1] offset0:130 offset1:131\n" \
  "ds_read2_b32 v[32:33], %[a0] offset0:128 offset1:129\n" \
  "ds_read2_b32 v[34:35], %[a0] offset0:130 offset1:131\n" \
  "ds_read2_b32 v[28:29], %[a0] offset0:0 offset1:1\n" \
  "ds_read2_b32 v[30:31], %[a0] offset0:2 offset1:3\n" \
  "ds_read2_b32 v[48:49], %[a2] offset0:128 offset1:129\n" \
  "ds_read2_b32 v[50:51], %[a2] offset0:130 offset1:131\n" \
  "ds_read_b128 v[64:67], %[b] offset:960\n" \
  "ds_read2_b32 v[36:37], %[a1] offset0:0 offset1:1\n" \
  "ds_read2_b32 v[38:39], %[a1] offset0:2 offset1:3\n" \
  "ds_read_b128 v[68:71], %[b] offset:1472\n" \
  "ds_read_b128 v[72:75], %[b] offset:1984\n" \
  "ds_read_b128 v[76:79], %[b] offset:2496\n" \
  "ds_read_b128 v[80:83], %[b] offset:3008\n" \
  "ds_read_b128 v[84:87], %[b] offset:3520\n" \
  "s_waitcnt lgkmcnt(0)\n" \
  "v_mfma_f32_16x16x32_bf16 v[156:159], v[52:55], v[60:63], 0\n" \
  "ds_read2_b32 v[52:53], %[a3] offset0:16 offset1:17\n" \
  "ds_read2_b32 v[54:55], %[a3] offset0:18 offset1:19\n" \
  "v_mfma_f32_16x16x32_bf16 v[148:151], v[44:47], v[60:63], 0\n" \
  "v_mfma_f32_16x16x32_bf16 v[144:147], v[40:43], v[60:63], 0\n" \
  "v_mfma_f32_16x16x32_bf16 v[136:139], v[32:35], v[60:63], 0\n" \
  "v_mfma_f32_16x16x32_bf16 v[132:135], v[28:31], v[60:63], 0\n" \
  "ds_read_b128 v[60:63], %[b] offset:384\n" \
  "v_mfma_f32_16x16x32_bf16 v[156:159], v[48:51], v[64:67], v[156:159]\n" \
  "ds_read2_b32 v[48:49], %[a2] offset0:144 offset1:145\n" \
  "ds_read2_b32 v[50:51], %[a2] offset0:146 offset1:147\n" \
  "v_mfma_f32_16x16x32_bf16 v[148:151], v[40:43], v[64:67], v[148:151]\n" \
  "v_mfma_f32_16x16x32_bf16 v[144:147], v[36:39], v[64:67], v[144:147]\n" \
  "v_mfma_f32_16x16x32_bf16 v[136:139], v[28:31], v[64:67], v[136:139]\n" \
  "ds_read_b128 v[64:67], %[b] offset:896\n" \
  "v_mfma_f32_16x16x32_bf16 v[156:159], v[44:47], v[68:71], v[156:159]\n" \
  "ds_read2_b32 v[44:45], %[a2] offset0:16 offset1:17\n" \
  "ds_read2_b32 v[46:47], %[a2] offset0:18 offset1:19\n" \
  "v_mfma_f32_16x16x32_bf16 v[148:151], v[36:39], v[68:71], v[148:151]\n" \
  "v_mfma_f32_16x16x32_bf16 v[144:147], v[32:35], v[68:71], v[144:147]\n" \
  "ds_read_b128 v[68:71], %[b] offset:1408\n" \
  "v_mfma_f32_16x16x32_bf16 v[156:159], v[40:43], v[72:75], v[156:159]\n" \
  "ds_read2_b32 v[40:41], %[a1] offset0:144 offset1:145\n" \
  "ds_read2_b32 v[42:43], %[a1] offset0:146 offset1:147\n" \
  "v_mfma_f32_16x16x32_bf16 v[148:151], v[32:35], v[72:75], v[148:151]\n" \
  "v_mfma_f32_16x16x32_bf16 v[144:147], v[28:31], v[72:75], v[144:147]\n" \
  "ds_read_b128 v[72:75], %[b] offset:1920\n" \
  "v_mfma_f32_16x16x32_bf16 v[156:159], v[36:39], v[76:79], v[156:159]\n" \
  "ds_read2_b32 v[36:37], %[a1] offset0:16 offset1:17\n" \
  "ds_read2_b32 v[38:39], %[a1] offset0:18 offset1:19\n" \
  "v_mfma_f32_16x16x32_bf16 v[148:151], v[28:31], v[76:79], v[148:151]\n" \
  "ds_read_b128 v[76:79], %[b] offset:2432\n" \
  "v_mfma_f32_16x16x32_bf16 v[156:159], v[32:35], v[80:83], v[156:159]\n" \
  "ds_read2_b32 v[32:33], %[a0] offset0:144 offset1:145\n" \
  "ds_read2_b32 v[34:35], %[a0] offset0:146 offset1:147\n" \
  "ds_read_b128 v[80:83], %[b] offset:2944\n" \
  "v_mfma_f32_16x16x32_bf16 v[156:159], v[28:31], v[84:87], v[156:159]\n" \
  "ds_read2_b32 v[28:29], %[a0] offset0:16 offset1:17\n" \
  "ds_read2_b32 v[30:31], %[a0] offset0:18 offset1:19\n" \
  "ds_read_b128 v[84:87], %[b] offset:3456\n" \
  "88:\n" \
  "s_waitcnt lgkmcnt(0)\n" \
  "v_mfma_f32_16x16x32_bf16 v[156:159], v[52:55], v[60:63], v[156:159]\n" \
  "ds_read2_b32 v[52:53], %[a3] offset0:32 offset1:33\n" \
  "ds_read2_b32 v[54:55], %[a3] offset0:34 offset1:35\n" \
  "v_mfma_f32_16x16x32_bf16 v[148:151], v[44:47], v[60:63], v[148:151]\n" \
  "v_mfma_f32_16x16x32_bf16 v[144:147], v[40:43], v[60:63], v[144:147]\n" \
  "v_mfma_f32_16x16x32_bf16 v[136:139], v[32:35], v[60:63], v[136:139]\n" \
  "v_mfma_f32_16x16x32_bf16 v[132:135], v[28:31], v[60:63], v[132:135]\n" \
  "ds_read_b128 v[60:63], %[b] offset:320\n" \
  "v_mfma_f32_16x16x32_bf16 v[156:159], v[48:51], v[64:67], v[156:159]\n" \
  "ds_read2_b32 v[48:49], %[a2] offset0:160 offset1:161\n" \
  "ds_read2_b32 v[50:51], %[a2] offset0:162 offset1:163\n" \
  "v_mfma_f32_16x16x32_bf16 v[148:151], v[40:43], v[64:67], v[148:151]\n" \
  "v_mfma_f32_16x16x32_bf16 v[144:147], v[36:39], v[64:67], v[144:147]\n" \
  "v_mfma_f32_16x16x32_bf16 v[136:139], v[28:31], v[64:67], v[136:139]\n" \
  "ds_read_b128 v[64:67], %[b] offset:832\n" \
  "v_mfma_f32_16x16x32_bf16 v[156:159], v[44:47], v[68:71], v[156:159]\n" \
  "ds_read2_b32 v[44:45], %[a2] offset0:32 offset1:33\n" \
  "ds_read2_b32 v[46:47], %[a2] offset0:34 offset1:35\n" \
  "v_mfma_f32_16x16x32_bf16 v[148:151], v[36:39], v[68:71], v[148:151]\n" \
  "v_mfma_f32_16x16x32_bf16 v[144:147], v[32:35], v[68:71], v[144:147]\n" \
  "ds_read_b128 v[68:71], %[b] offset:1344\n" \
  "v_mfma_f32_16x16x32_bf16 v[156:159], v[40:43], v[72:75], v[156:159]\n" \
  "ds_read2_b32 v[40:41], %[a1] offset0:160 offset1:161\n" \
  "ds_read2_b32 v[42:43], %[a1] offset0:162 offset1:163\n" \
  "v_mfma_f32_16x16x32_bf16 v[148:151], v[32:35], v[72:75], v[148:151]\n" \
  "v_mfma_f32_16x16x32_bf16 v[144:147], v[28:31], v[72:75], v[144:147]\n" \
  "ds_read_b128 v[72:75], %[b] offset:1856\n" \
  "v_mfma_f32_16x16x32_bf16 v[156:159], v[36:39], v[76:79], v[156:159]\n" \
  "ds_read2_b32 v[36:37], %[a1] offset0:32 offset1:33\n" \
  "ds_read2_b32 v[38:39], %[a1] offset0:34 offset1:35\n" \
  "v_mfma_f32_16x16x32_bf16 v[148:151], v[28:31], v[76:79], v[148:151]\n" \
  "ds_read_b128 v[76:79], %[b] offset:2368\n" \
  "v_mfma_f32_16x16x32_bf16 v[156:159], v[32:35], v[80:83], v[156:159]\n" \
  "ds_read2_b32 v[32:33], %[a0] offset0:160 offset1:161\n" \
  "ds_read2_b32 v[34:35], %[a0] offset0:162 offset1:163\n" \
  "ds_read_b128 v[80:83], %[b] offset:2880\n" \
  "v_mfma_f32_16x16x32_bf16 v[156:159], v[28:31], v[84:87], v[156:159]\n" \
  "ds_read2_b32 v[28:29], %[a0] offset0:32 offset1:33\n" \
  "ds_read2_b32 v[30:31], %[a0] offset0:34 offset1:35\n" \
  "ds_read_b128 v[84:87], %[b] offset:3392\n" \
  "s_waitcnt lgkmcnt(0)\n" \
  "v_mfma_f32_16x16x32_bf16 v[156:159], v[52:55], v[60:63], v[156:159]\n" \
  "ds_read2_b32 v[52:53], %[a3] offset0:48 offset1:49\n" \
  "ds_read2_b32 v[54:55], %[a3] offset0:50 offset1:51\n" \
  "v_mfma_f32_16x16x32_bf16 v[148:151], v[44:47], v[60:63], v[148:151]\n" \
  "v_mfma_f32_16x16x32_bf16 v[144:147], v[40:43], v[60:63], v[144:147]\n" \
  "v_mfma_f32_16x16x32_bf16 v[136:139], v[32:35], v[60:63], v[136:139]\n" \
  "v_mfma_f32_16x16x32_bf16 v[132:135], v[28:31], v[60:63], v[132:135]\n" \
  "ds_read_b128 v[60:63], %[b] offset:256\n" \
  "v_mfma_f32_16x16x32_bf16 v[156:159], v[48:51], v[64:67], v[156:159]\n" \
  "ds_read2_b32 v[48:49], %[a2] offset0:176 offset1:177\n" \
  "ds_read2_b32 v[50:51], %[a2] offset0:178 offset1:179\n" \
  "v_mfma_f32_16x16x32_bf16 v[148:151], v[40:43], v[64:67], v[148:151]\n" \
  "v_mfma_f32_16x16x32_bf16 v[144:147], v[36:39], v[64:67], v[144:147]\n" \
  "v_mfma_f32_16x16x32_bf16 v[136:139], v[28:31], v[64:67], v[136:139]\n" \
  "ds_read_b128 v[64:67], %[b] offset:768\n" \
  "v_mfma_f32_16x16x32_bf16 v[156:159], v[44:47], v[68:71], v[156:159]\n" \
  "ds_read2_b32 v[44:45], %[a2] offset0:48 offset1:49\n" \
  "ds_read2_b32 v[46:47], %[a2] offset0:50 offset1:51\n" \
  "v_mfma_f32_16x16x32_bf16 v[148:151], v[36:39], v[68:71], v[148:151]\n" \
  "v_mfma_f32_16x16x32_bf16 v[144:147], v[32:35], v[68:71], v[144:147]\n" \
  "ds_read_b128 v[68:71], %[b] offset:1280\n" \
  "v_mfma_f32_16x16x32_bf16 v[156:159], v[40:43], v[72:75], v[156:159]\n" \
  "ds_read2_b32 v[40:41], %[a1] offset0:176 offset1:177\n" \
  "ds_read2_b32 v[42:43], %[a1] offset0:178 offset1:179\n" \
  "v_mfma_f32_16x16x32_bf16 v[148:151], v[32:35], v[72:75], v[148:151]\n" \
  "v_mfma_f32_16x16x32_bf16 v[144:147], v[28:31], v[72:75], v[144:147]\n" \
  "ds_read_b128 v[72:75], %[b] offset:1792\n" \
  "v_mfma_f32_16x16x32_bf16 v[156:159], v[36:39], v[76:79], v[156:159]\n" \
  "ds_read2_b32 v[36:37], %[a1] offset0:48 offset1:49\n" \
  "ds_read2_b32 v[38:39], %[a1] offset0:50 offset1:51\n" \
  "v_mfma_f32_16x16x32_bf16 v[148:151], v[28:31], v[76:79], v[148:151]\n" \
  "ds_read_b128 v[76:79], %[b] offset:2304\n" \
  "v_mfma_f32_16x16x32_bf16 v[156:159], v[32:35], v[80:83], v[156:159]\n" \
  "ds_read2_b32 v[32:33], %[a0] offset0:176 offset1:177\n" \
  "ds_read2_b32 v[34:35], %[a0] offset0:178 offset1:179\n" \
  "ds_read_b128 v[80:83], %[b] offset:2816\n" \
  "v_mfma_f32_16x16x32_bf16 v[156:159], v[28:31], v[84:87], v[156:159]\n" \
  "ds_read2_b32 v[28:29], %[a0] offset0:48 offset1:49\n" \
  "ds_read2_b32 v[30:31], %[a0] offset0:50 offset1:51\n" \
  "ds_read_b128 v[84:87], %[b] offset:3328\n" \
  "v_add_u32 %[a0], 128, %[a0]\n" \
  "v_add_u32 %[a1], 128, %[a1]\n" \
  "v_add_u32 %[a2], 128, %[a2]\n" \
  "v_add_u32 %[a3], 128, %[a3]\n" \
  "v_subrev_u32 %[b], 128, %[b]\n" \
  "s_sub_u32 %[cnt], %[cnt], 1\n" \
  "s_cmp_lg_u32 %[cnt], 0\n" \
  "s_cbranch_scc1 88b\n" \
  "s_waitcnt lgkmcnt(0)\n" \
  "v_mfma_f32_16x16x32_bf16 v[156:159], v[52:55], v[60:63], v[156:159]\n" \
  "v_mfma_f32_16x16x32_bf16 v[148:151], v[44:47], v[60:63], v[148:151]\n" \
  "v_mfma_f32_16x16x32_bf16 v[144:147], v[40:43], v[60:63], v[144:147]\n" \
  "v_mfma_f32_16x16x32_bf16 v[136:139], v[32:35], v[60:63], v[136:139]\n" \
  "v_mfma_f32_16x16x32_bf16 v[132:135], v[28:31], v[60:63], v[132:135]\n" \
  "v_mfma_f32_16x16x32_bf16 v[156:159], v[48:51], v[64:67], v[156:159]\n" \
  "v_mfma_f32_16x16x32_bf16 v[148:151], v[40:43], v[64:67], v[148:151]\n" \
  "v_mfma_f32_16x16x32_bf16 v[144:147], v[36:39], v[64:67], v[144:147]\n" \
  "v_mfma_f32_16x16x32_bf16 v[136:139], v[28:31], v[64:67], v[136:139]\n" \
  "v_mfma_f32_16x16x32_bf16 v[156:159], v[44:47], v[68:71], v[156:159]\n" \
  "v_mfma_f32_16x16x32_bf16 v[148:151], v[36:39], v[68:71], v[148:151]\n" \
  "v_mfma_f32_16x16x32_bf16 v[144:147], v[32:35], v[68:71], v[144:147]\n" \
  "v_mfma_f32_16x16x32_bf16 v[156:159], v[40:43], v[72:75], v[156:159]\n" \
  "v_mfma_f32_16x16x32_bf16 v[148:151], v[32:35], v[72:75], v[148:151]\n" \
  "v_mfma_f32_16x16x32_bf16 v[144:147], v[28:31], v[72:75], v[144:147]\n" \
  "v_mfma_f32_16x16x32_bf16 v[156:159], v[36:39], v[76:79], v[156:159]\n" \
  "v_mfma_f32_16x16x32_bf16 v[148:151], v[28:31], v[76:79], v[148:151]\n" \
  "v_mfma_f32_16x16x32_bf16 v[156:159], v[32:35], v[80:83], v[156:159]\n" \
  "v_mfma_f32_16x16x32_bf16 v[156:159], v[28:31], v[84:87], v[156:159]\n" \
  "s_nop 3\n" \
  "ds_read_b64 v[28:29], %[ue] offset:0\n" \
  "ds_read_b64 v[30:31], %[ue] offset:512\n" \
  "ds_read_b64 v[32:33], %[ue] offset:1536\n" \
  "ds_read_b64 v[34:35], %[ue] offset:2048\n" \
  "ds_read_b64 v[36:37], %[ue] offset:3072\n" \
  "s_waitcnt lgkmcnt(4)\n" \
  "v_and_b32 v92, 0xffff0000, v29\n" \
  "v_lshlrev_b32 v93, 16, v29\n" \
  "v_and_b32 v94, 0xffff0000, v28\n" \
  "v_lshlrev_b32 v95, 16, v28\n" \
  "v_fmac_f32 v132, %[d1], v92\n" \
  "v_fmac_f32 v133, %[d1], v93\n" \
  "v_fmac_f32 v134, %[d1], v94\n" \
  "v_fmac_f32 v135, %[d1], v95\n" \
  "v_cvt_pk_bf16_f32 v132, v132, v133\n" \
  "v_cvt_pk_bf16_f32 v133, v134, v135\n" \
  "ds_write_b64 %[zw], v[132:133]\n" \
  "ds_read_b128 v[60:63], %[zr]\n" \
  "s_waitcnt lgkmcnt(5)\n" \
  "v_and_b32 v92, 0xffff0000, v31\n" \
  "v_lshlrev_b32 v93, 16, v31\n" \
  "v_and_b32 v94, 0xffff0000, v30\n" \
  "v_lshlrev_b32 v95, 16, v30\n" \
  "v_fmac_f32 v136, %[d1], v92\n" \
  "v_fmac_f32 v137, %[d1], v93\n" \
  "v_fmac_f32 v138, %[d1], v94\n" \
  "v_fmac_f32 v139, %[d1], v95\n" \
  "v_cvt_pk_bf16_f32 v136, v136, v137\n" \
  "v_cvt_pk_bf16_f32 v137, v138, v139\n" \
  "ds_write_b64 %[zw], v[136:137]\n" \
  "ds_read_b128 v[64:67], %[zr]\n" \
  "s_waitcnt lgkmcnt(6)\n" \
  "v_and_b32 v92, 0xffff0000, v33\n" \
  "v_lshlrev_b32 v93, 16, v33\n" \
  "v_and_b32 v94, 0xffff0000, v32\n" \
  "v_lshlrev_b32 v95, 16, v32\n" \
  "v_fmac_f32 v144, %[d1], v92\n" \
  "v_fmac_f32 v145, %[d1], v93\n" \
  "v_fmac_f32 v146, %[d1], v94\n" \
  "v_fmac_f32 v147, %[d1], v95\n" \
  "v_cvt_pk_bf16_f32 v144, v144, v145\n" \
  "v_cvt_pk_bf16_f32 v145, v146, v147\n" \
  "ds_write_b64 %[zw], v[144:145]\n" \
  "ds_read_b128 v[68:71], %[zr]\n" \
  "s_waitcnt lgkmcnt(7)\n" \
  "v_and_b32 v92, 0xffff0000, v35\n" \
  "v_lshlrev_b32 v93, 16, v35\n" \
  "v_and_b32 v94, 0xffff0000, v34\n" \
  "v_lshlrev_b32 v95, 16, v34\n" \
  "v_fmac_f32 v148, %[d1], v92\n" \
  "v_fmac_f32 v149, %[d1], v93\n" \
  "v_fmac_f32 v150, %[d1], v94\n" \
  "v_fmac_f32 v151, %[d1], v95\n" \
  "v_cvt_pk_bf16_f32 v148, v148, v149\n" \
  "v_cvt_pk_bf16_f32 v149, v150, v151\n" \
  "ds_write_b64 %[zw], v[148:149]\n" \
  "ds_read_b128 v[72:75], %[zr]\n" \
  "s_waitcnt lgkmcnt(8)\n" \
  "v_and_b32 v92, 0xffff0000, v37\n" \
  "v_lshlrev_b32 v93, 16, v37\n" \
  "v_and_b32 v94, 0xffff0000, v36\n" \
  "v_lshlrev_b32 v95, 16, v36\n" \
  "v_fmac_f32 v156, %[d1], v92\n" \
  "v_fmac_f32 v157, %[d1], v93\n" \
  "v_fmac_f32 v158, %[d1], v94\n" \
  "v_fmac_f32 v159, %[d1], v95\n" \
  "v_cvt_pk_bf16_f32 v156, v156, v157\n" \
  "v_cvt_pk_bf16_f32 v157, v158, v159\n" \
  "ds_write_b64 %[zw], v[156:157]\n" \
  "ds_read_b128 v[76:79], %[zr]\n" \
  "s_mov_b32 exec_hi, 0\n" \
  "s_waitcnt lgkmcnt(8)\n" \
  "buffer_store_dwordx4 v[60:63], %[zo], %[rs], %[z1] offen offset:0 sc1\n" \
  "s_waitcnt lgkmcnt(6)\n" \
  "buffer_store_dwordx4 v[64:67], %[zo], %[rs], %[z1] offen offset:512 sc1\n" \
  "s_waitcnt lgkmcnt(4)\n" \
  "buffer_store_dwordx4 v[68:71], %[zo], %[rs], %[z1] offen offset:1536 sc1\n" \
  "s_waitcnt lgkmcnt(2)\n" \
  "buffer_store_dwordx4 v[72:75], %[zo], %[rs], %[z1] offen offset:2048 sc1\n" \
  "s_waitcnt lgkmcnt(0)\n" \
  "buffer_store_dwordx4 v[76:79], %[zo], %[rs], %[z1] offen offset:3072 sc1\n" \
  "s_mov_b64 exec, -1\n" \
  "s_branch 99f\n" \
  "69:\n" \
  "ds_read2_b32 v[48:49], %[a2] offset0:128 offset1:129\n" \
  "ds_read2_b32 v[50:51], %[a2] offset0:130 offset1:131\n" \
  "ds_read_b128 v[60:63], %[b] offset:448\n" \
  "ds_read2_b32 v[44:45], %[a2] offset0:0 offset1:1\n" \
  "ds_read2_b32 v[46:47], %[a2] offset0:2 offset1:3\n" \
  "ds_read2_b32 v[40:41], %[a1] offset0:128 offset1:129\n" \
  "ds_read2_b32 v[42:43], %[a1] offset0:130 offset1:131\n" \
  "ds_read2_b32 v[100:101], %[c1] offset0:0 offset1:1\n" \
  "ds_read2_b32 v[102:103], %[c1] offset0:2 offset1:3\n" \
  "ds_read_b128 v[112:115], %[d] offset:448\n" \
  "ds_read2_b32 v[32:33], %[a0] offset0:128 offset1:129\n" \
  "ds_read2_b32 v[34:35], %[a0] offset0:130 offset1:131\n" \
  "ds_read2_b32 v[28:29], %[a0] offset0:0 offset1:1\n" \
  "ds_read2_b32 v[30:31], %[a0] offset0:2 offset1:3\n" \
  "ds_read_b128 v[64:67], %[b] offset:960\n" \
  "ds_read2_b32 v[36:37], %[a1] offset0:0 offset1:1\n" \
  "ds_read2_b32 v[38:39], %[a1] offset0:2 offset1:3\n" \
  "ds_read2_b32 v[96:97], %[c0] offset0:128 offset1:129\n" \
  "ds_read2_b32 v[98:99], %[c0] offset0:130 offset1:131\n" \
  "ds_read_b128 v[116:119], %[d] offset:960\n" \
  "ds_read_b128 v[68:71], %[b] offset:1472\n" \
  "ds_read2_b32 v[92:93], %[c0] offset0:0 offset1:1\n" \
  "ds_read2_b32 v[94:95], %[c0] offset0:2 offset1:3\n" \
  "ds_read_b128 v[120:123], %[d] offset:1472\n" \
  "ds_read_b128 v[72:75], %[b] offset:1984\n" \
  "ds_read_b128 v[76:79], %[b] offset:2496\n" \
  "ds_read_b128 v[80:83], %[b] offset:3008\n" \
  "s_waitcnt lgkmcnt(0)\n" \
  "v_mfma_f32_16x16x32_bf16 v[152:155], v[48:51], v[60:63], 0\n" \
  "ds_read2_b32 v[48:49], %[a2] offset0:144 offset1:145\n" \
  "ds_read2_b32 v[50:51], %[a2] offset0:146 offset1:147\n" \
  "v_mfma_f32_16x16x32_bf16 v[148:151], v[44:47], v[60:63], 0\n" \
  "v_mfma_f32_16x16x32_bf16 v[144:147], v[40:43], v[60:63], 0\n" \
  "v_mfma_f32_16x16x32_bf16 v[140:143], v[100:103], v[112:115], 0\n" \
  "ds_read2_b32 v[100:101], %[c1] offset0:16 offset1:17\n" \
  "ds_read2_b32 v[102:103], %[c1] offset0:18 offset1:19\n" \
  "ds_read_b128 v[112:115], %[d] offset:384\n" \
  "v_mfma_f32_16x16x32_bf16 v[136:139], v[32:35], v[60:63], 0\n" \
  "v_mfma_f32_16x16x32_bf16 v[132:135], v[28:31], v[60:63], 0\n" \
  "ds_read_b128 v[60:63], %[b] offset:384\n" \
  "v_mfma_f32_16x16x32_bf16 v[152:155], v[44:47], v[64:67], v[152:155]\n" \
  "ds_read2_b32 v[44:45], %[a2] offset0:16 offset1:17\n" \
  "ds_read2_b32 v[46:47], %[a2] offset0:18 offset1:19\n" \
  "v_mfma_f32_16x16x32_bf16 v[148:151], v[40:43], v[64:67], v[148:151]\n" \
  "v_mfma_f32_16x16x32_bf16 v[144:147], v[36:39], v[64:67], v[144:147]\n" \
  "v_mfma_f32_16x16x32_bf16 v[140:143], v[96:99], v[116:119], v[140:143]\n" \
  "ds_read2_b32 v[96:97], %[c0] offset0:144 offset1:145\n" \
  "ds_read2_b32 v[98:99], %[c0] offset0:146 offset1:147\n" \
  "ds_read_b128 v[116:119], %[d] offset:896\n" \
  "v_mfma_f32_16x16x32_bf16 v[136:139], v[28:31], v[64:67], v[136:139]\n" \
  "ds_read_b128 v[64:67], %[b] offset:896\n" \
  "v_mfma_f32_16x16x32_bf16 v[152:155], v[40:43], v[68:71], v[152:155]\n" \
  "ds_read2_b32 v[40:41], %[a1] offset0:144 offset1:145\n" \
  "ds_read2_b32 v[42:43], %[a1] offset0:146 offset1:147\n" \
  "v_mfma_f32_16x16x32_bf16 v[148:151], v[36:39], v[68:71], v[148:151]\n" \
  "v_mfma_f32_16x16x32_bf16 v[144:147], v[32:35], v[68:71], v[144:147]\n" \
  "ds_read_b128 v[68:71], %[b] offset:1408\n" \
  "v_mfma_f32_16x16x32_bf16 v[140:143], v[92:95], v[120:123], v[140:143]\n" \
  "ds_read2_b32 v[92:93], %[c0] offset0:16 offset1:17\n" \
  "ds_read2_b32 v[94:95], %[c0] offset0:18 offset1:19\n" \
  "ds_read_b128 v[120:123], %[d] offset:1408\n" \
  "v_mfma_f32_16x16x32_bf16 v[152:155], v[36:39], v[72:75], v[152:155]\n" \
  "ds_read2_b32 v[36:37], %[a1] offset0:16 offset1:17\n" \
  "ds_read2_b32 v[38:39], %[a1] offset0:18 offset1:19\n" \
  "v_mfma_f32_16x16x32_bf16 v[148:151], v[32:35], v[72:75], v[148:151]\n" \
  "v_mfma_f32_16x16x32_bf16 v[144:147], v[28:31], v[72:75], v[144:147]\n" \
  "ds_read_b128 v[72:75], %[b] offset:1920\n" \
  "v_mfma_f32_16x16x32_bf16 v[152:155], v[32:35], v[76:79], v[152:155]\n" \
  "ds_read2_b32 v[32:33], %[a0] offset0:144 offset1:145\n" \
  "ds_read2_b32 v[34:35], %[a0] offset0:146 offset1:147\n" \
  "v_mfma_f32_16x16x32_bf16 v[148:151], v[28:31], v[76:79], v[148:151]\n" \
  "ds_read_b128 v[76:79], %[b] offset:2432\n" \
  "v_mfma_f32_16x16x32_bf16 v[152:155], v[28:31], v[80:83], v[152:155]\n" \
  "ds_read2_b32 v[28:29], %[a0] offset0:16 offset1:17\n" \
  "ds_read2_b32 v[30:31], %[a0] offset0:18 offset1:19\n" \
  "ds_read_b128 v[80:83], %[b] offset:2944\n" \
  "89:\n" \
  "s_waitcnt lgkmcnt(0)\n" \
  "v_mfma_f32_16x16x32_bf16 v[152:155], v[48:51], v[60:63], v[152:155]\n" \
  "ds_read2_b32 v[48:49], %[a2] offset0:160 offset1:161\n" \
  "ds_read2_b32 v[50:51], %[a2] offset0:162 offset1:163\n" \
  "v_mfma_f32_16x16x32_bf16 v[148:151], v[44:47], v[60:63], v[148:151]\n" \
  "v_mfma_f32_16x16x32_bf16 v[144:147], v[40:43], v[60:63], v[144:147]\n" \
  "v_mfma_f32_16x16x32_bf16 v[140:143], v[100:103], v[112:115], v[140:143]\n" \
  "ds_read2_b32 v[100:101], %[c1] offset0:32 offset1:33\n" \
  "ds_read2_b32 v[102:103], %[c1] offset0:34 offset1:35\n" \
  "ds_read_b128 v[112:115], %[d] offset:320\n" \
  "v_mfma_f32_16x16x32_bf16 v[136:139], v[32:35], v[60:63], v[136:139]\n" \
  "v_mfma_f32_16x16x32_bf16 v[132:135], v[28:31], v[60:63], v[132:135]\n" \
  "ds_read_b128 v[60:63], %[b] offset:320\n" \
  "v_mfma_f32_16x16x32_bf16 v[152:155], v[44:47], v[64:67], v[152:155]\n" \
  "ds_read2_b32 v[44:45], %[a2] offset0:32 offset1:33\n" \
  "ds_read2_b32 v[46:47], %[a2] offset0:34 offset1:35\n" \
  "v_mfma_f32_16x16x32_bf16 v[148:151], v[40:43], v[64:67], v[148:151]\n" \
  "v_mfma_f32_16x16x32_bf16 v[144:147], v[36:39], v[64:67], v[144:147]\n" \
  "v_mfma_f32_16x16x32_bf16 v[140:143], v[96:99], v[116:119], v[140:143]\n" \
  "ds_read2_b32 v[96:97], %[c0] offset0:160 offset1:161\n" \
  "ds_read2_b32 v[98:99], %[c0] offset0:162 offset1:163\n" \
  "ds_read_b128 v[116:119], %[d] offset:832\n" \
  "v_mfma_f32_16x16x32_bf16 v[136:139], v[28:31], v[64:67], v[136:139]\n" \
  "ds_read_b128 v[64:67], %[b] offset:832\n" \
  "v_mfma_f32_16x16x32_bf16 v[152:155], v[40:43], v[68:71], v[152:155]\n" \
  "ds_read2_b32 v[40:41], %[a1] offset0:160 offset1:161\n" \
  "ds_read2_b32 v[42:43], %[a1] offset0:162 offset1:163\n" \
  "v_mfma_f32_16x16x32_bf16 v[148:151], v[36:39], v[68:71], v[148:151]\n" \
  "v_mfma_f32_16x16x32_bf16 v[144:147], v[32:35], v[68:71], v[144:147]\n" \
  "ds_read_b128 v[68:71], %[b] offset:1344\n" \
  "v_mfma_f32_16x16x32_bf16 v[140:143], v[92:95], v[120:123], v[140:143]\n" \
  "ds_read2_b32 v[92:93], %[c0] offset0:32 offset1:33\n" \
  "ds_read2_b32 v[94:95], %[c0] offset0:34 offset1:35\n" \
  "ds_read_b128 v[120:123], %[d] offset:1344\n" \
  "v_mfma_f32_16x16x32_bf16 v[152:155], v[36:39], v[72:75], v[152:155]\n" \
  "ds_read2_b32 v[36:37], %[a1] offset0:32 offset1:33\n" \
  "ds_read2_b32 v[38:39], %[a1] offset0:34 offset1:35\n" \
  "v_mfma_f32_16x16x32_bf16 v[148:151], v[32:35], v[72:75], v[148:151]\n" \
  "v_mfma_f32_16x16x32_bf16 v[144:147], v[28:31], v[72:75], v[144:147]\n" \
  "ds_read_b128 v[72:75], %[b] offset:1856\n" \
  "v_mfma_f32_16x16x32_bf16 v[152:155], v[32:35], v[76:79], v[152:155]\n" \
  "ds_read2_b32 v[32:33], %[a0] offset0:160 offset1:161\n" \
  "ds_read2_b32 v[34:35], %[a0] offset0:162 offset1:163\n" \
  "v_mfma_f32_16x16x32_bf16 v[148:151], v[28:31], v[76:79], v[148:151]\n" \
  "ds_read_b128 v[76:79], %[b] offset:2368\n" \
  "v_mfma_f32_16x16x32_bf16 v[152:155], v[28:31], v[80:83], v[152:155]\n" \
  "ds_read2_b32 v[28:29], %[a0] offset0:32 offset1:33\n" \
  "ds_read2_b32 v[30:31], %[a0] offset0:34 offset1:35\n" \
  "ds_read_b128 v[80:83], %[b] offset:2880\n" \
  "s_waitcnt lgkmcnt(0)\n" \
  "v_mfma_f32_16x16x32_bf16 v[152:155], v[48:51], v[60:63], v[152:155]\n" \
  "ds_read2_b32 v[48:49], %[a2] offset0:176 offset1:177\n" \
  "ds_read2_b32 v[50:51], %[a2] offset0:178 offset1:179\n" \
  "v_mfma_f32_16x16x32_bf16 v[148:151], v[44:47], v[60:63], v[148:151]\n" \
  "v_mfma_f32_16x16x32_bf16 v[144:147], v[40:43], v[60:63], v[144:147]\n" \
  "v_mfma_f32_16x16x32_bf16 v[140:143], v[100:103], v[112:115], v[140:143]\n" \
  "ds_read2_b32 v[100:101], %[c1] offset0:48 offset1:49\n" \
  "ds_read2_b32 v[102:103], %[c1] offset0:50 offset1:51\n" \
  "ds_read_b128 v[112:115], %[d] offset:256\n" \
  "v_mfma_f32_16x16x32_bf16 v[136:139], v[32:35], v[60:63], v[136:139]\n" \
  "v_mfma_f32_16x16x32_bf16 v[132:135], v[28:31], v[60:63], v[132:135]\n" \
  "ds_read_b128 v[60:63], %[b] offset:256\n" \
  "v_mfma_f32_16x16x32_bf16 v[152:155], v[44:47], v[64:67], v[152:155]\n" \
  "ds_read2_b32 v[44:45], %[a2] offset0:48 offset1:49\n" \
  "ds_read2_b32 v[46:47], %[a2] offset0:50 offset1:51\n" \
  "v_mfma_f32_16x16x32_bf16 v[148:151], v[40:43], v[64:67], v[148:151]\n" \
  "v_mfma_f32_16x16x32_bf16 v[144:147], v[36:39], v[64:67], v[144:147]\n" \
  "v_mfma_f32_16x16x32_bf16 v[140:143], v[96:99], v[116:119], v[140:143]\n" \
  "ds_read2_b32 v[96:97], %[c0] offset0:176 offset1:177\n" \
  "ds_read2_b32 v[98:99], %[c0] offset0:178 offset1:179\n" \
  "ds_read_b128 v[116:119], %[d] offset:768\n" \
  "v_mfma_f32_16x16x32_bf16 v[136:139], v[28:31], v[64:67], v[136:139]\n" \
  "ds_read_b128 v[64:67], %[b] offset:768\n" \
  "v_mfma_f32_16x16x32_bf16 v[152:155], v[40:43], v[68:71], v[152:155]\n" \
  "ds_read2_b32 v[40:41], %[a1] offset0:176 offset1:177\n" \
  "ds_read2_b32 v[42:43], %[a1] offset0:178 offset1:179\n" \
  "v_mfma_f32_16x16x32_bf16 v[148:151], v[36:39], v[68:71], v[148:151]\n" \
  "v_mfma_f32_16x16x32_bf16 v[144:147], v[32:35], v[68:71], v[144:147]\n" \
  "ds_read_b128 v[68:71], %[b] offset:1280\n" \
  "v_mfma_f32_16x16x32_bf16 v[140:143], v[92:95], v[120:123], v[140:143]\n" \
  "ds_read2_b32 v[92:93], %[c0] offset0:48 offset1:49\n" \
  "ds_read2_b32 v[94:95], %[c0] offset0:50 offset1:51\n" \
  "ds_read_b128 v[120:123], %[d] offset:1280\n" \
  "v_mfma_f32_16x16x32_bf16 v[152:155], v[36:39], v[72:75], v[152:155]\n" \
  "ds_read2_b32 v[36:37], %[a1] offset0:48 offset1:49\n" \
  "ds_read2_b32 v[38:39], %[a1] offset0:50 offset1:51\n" \
  "v_mfma_f32_16x16x32_bf16 v[148:151], v[32:35], v[72:75], v[148:151]\n" \
  "v_mfma_f32_16x16x32_bf16 v[144:147], v[28:31], v[72:75], v[144:147]\n" \
  "ds_read_b128 v[72:75], %[b] offset:1792\n" \
  "v_mfma_f32_16x16x32_bf16 v[152:155], v[32:35], v[76:79], v[152:155]\n" \
  "ds_read2_b32 v[32:33], %[a0] offset0:176 offset1:177\n" \
  "ds_read2_b32 v[34:35], %[a0] offset0:178 offset1:179\n" \
  "v_mfma_f32_16x16x32_bf16 v[148:151], v[28:31], v[76:79], v[148:151]\n" \
  "ds_read_b128 v[76:79], %[b] offset:2304\n" \
  "v_mfma_f32_16x16x32_bf16 v[152:155], v[28:31], v[80:83], v[152:155]\n" \
  "ds_read2_b32 v[28:29], %[a0] offset0:48 offset1:49\n" \
  "ds_read2_b32 v[30:31], %[a0] offset0:50 offset1:51\n" \
  "ds_read_b128 v[80:83], %[b] offset:2816\n" \
  "v_add_u32 %[a0], 128, %[a0]\n" \
  "v_add_u32 %[a1], 128, %[a1]\n" \
  "v_add_u32 %[a2], 128, %[a2]\n" \
  "v_subrev_u32 %[b], 128, %[b]\n" \
  "v_add_u32 %[c0], 128, %[c0]\n" \
  "v_add_u32 %[c1], 128, %[c1]\n" \
  "v_subrev_u32 %[d], 128, %[d]\n" \
  "s_sub_u32 %[cnt], %[cnt], 1\n" \
  "s_cmp_lg_u32 %[cnt], 0\n" \
  "s_cbranch_scc1 89b\n" \
  "s_waitcnt lgkmcnt(0)\n" \
  "v_mfma_f32_16x16x32_bf16 v[152:155], v[48:51], v[60:63], v[152:155]\n" \
  "v_mfma_f32_16x16x32_bf16 v[148:151], v[44:47], v[60:63], v[148:151]\n" \
  "v_mfma_f32_16x16x32_bf16 v[144:147], v[40:43], v[60:63], v[144:147]\n" \
  "v_mfma_f32_16x16x32_bf16 v[140:143], v[100:103], v[112:115], v[140:143]\n" \
  "v_mfma_f32_16x16x32_bf16 v[136:139], v[32:35], v[60:63], v[136:139]\n" \
  "v_mfma_f32_16x16x32_bf16 v[132:135], v[28:31], v[60:63], v[132:135]\n" \
  "v_mfma_f32_16x16x32_bf16 v[152:155], v[44:47], v[64:67], v[152:155]\n" \
  "v_mfma_f32_16x16x32_bf16 v[148:151], v[40:43], v[64:67], v[148:151]\n" \
  "v_mfma_f32_16x16x32_bf16 v[144:147], v[36:39], v[64:67], v[144:147]\n" \
  "v_mfma_f32_16x16x32_bf16 v[140:143], v[96:99], v[116:119], v[140:143]\n" \
  "v_mfma_f32_16x16x32_bf16 v[136:139], v[28:31], v[64:67], v[136:139]\n" \
  "v_mfma_f32_16x16x32_bf16 v[152:155], v[40:43], v[68:71], v[152:155]\n" \
  "v_mfma_f32_16x16x32_bf16 v[148:151], v[36:39], v[68:71], v[148:151]\n" \
  "v_mfma_f32_16x16x32_bf16 v[144:147], v[32:35], v[68:71], v[144:147]\n" \
  "v_mfma_f32_16x16x32_bf16 v[140:143], v[92:95], v[120:123], v[140:143]\n" \
  "v_mfma_f32_16x16x32_bf16 v[152:155], v[36:39], v[72:75], v[152:155]\n" \
  "v_mfma_f32_16x16x32_bf16 v[148:151], v[32:35], v[72:75], v[148:151]\n" \
  "v_mfma_f32_16x16x32_bf16 v[144:147], v[28:31], v[72:75], v[144:147]\n" \
  "v_mfma_f32_16x16x32_bf16 v[152:155], v[32:35], v[76:79], v[152:155]\n" \
  "v_mfma_f32_16x16x32_bf16 v[148:151], v[28:31], v[76:79], v[148:151]\n" \
  "v_mfma_f32_16x16x32_bf16 v[152:155], v[28:31], v[80:83], v[152:155]\n" \
  "s_nop 3\n" \
  "ds_read_b64 v[28:29], %[ue] offset:0\n" \
  "ds_read_b64 v[30:31], %[ue] offset:512\n" \
  "ds_read_b64 v[32:33], %[ue] offset:1536\n" \
  "ds_read_b64 v[34:35], %[ue] offset:2048\n" \
  "ds_read_b64 v[36:37], %[ue] offset:2560\n" \
  "ds_read_b64 v[38:39], %[uf] offset:1024\n" \
  "s_waitcnt lgkmcnt(5)\n" \
  "v_and_b32 v92, 0xffff0000, v29\n" \
  "v_lshlrev_b32 v93, 16, v29\n" \
  "v_and_b32 v94, 0xffff0000, v28\n" \
  "v_lshlrev_b32 v95, 16, v28\n" \
  "v_fmac_f32 v132, %[d1], v92\n" \
  "v_fmac_f32 v133, %[d1], v93\n" \
  "v_fmac_f32 v134, %[d1], v94\n" \
  "v_fmac_f32 v135, %[d1], v95\n" \
  "v_cvt_pk_bf16_f32 v132, v132, v133\n" \
  "v_cvt_pk_bf16_f32 v133, v134, v135\n" \
  "ds_write_b64 %[zw], v[132:133]\n" \
  "ds_read_b128 v[60:63], %[zr]\n" \
  "s_waitcnt lgkmcnt(6)\n" \
  "v_and_b32 v92, 0xffff0000, v31\n" \
  "v_lshlrev_b32 v93, 16, v31\n" \
  "v_and_b32 v94, 0xffff0000, v30\n" \
  "v_lshlrev_b32 v95, 16, v30\n" \
  "v_fmac_f32 v136, %[d1], v92\n" \
  "v_fmac_f32 v137, %[d1], v93\n" \
  "v_fmac_f32 v138, %[d1], v94\n" \
  "v_fmac_f32 v139, %[d1], v95\n" \
  "v_cvt_pk_bf16_f32 v136, v136, v137\n" \
  "v_cvt_pk_bf16_f32 v137, v138, v139\n" \
  "ds_write_b64 %[zw], v[136:137]\n" \
  "ds_read_b128 v[64:67], %[zr]\n" \
  "s_waitcnt lgkmcnt(7)\n" \
  "v_and_b32 v92, 0xffff0000, v33\n" \
  "v_lshlrev_b32 v93, 16, v33\n" \
  "v_and_b32 v94, 0xffff0000, v32\n" \
  "v_lshlrev_b32 v95, 16, v32\n" \
  "v_fmac_f32 v144, %[d1], v92\n" \
  "v_fmac_f32 v145, %[d1], v93\n" \
  "v_fmac_f32 v146, %[d1], v94\n" \
  "v_fmac_f32 v147, %[d1], v95\n" \
  "v_cvt_pk_bf16_f32 v144, v144, v145\n" \
  "v_cvt_pk_bf16_f32 v145, v146, v147\n" \
  "ds_write_b64 %[zw], v[144:145]\n" \
  "ds_read_b128 v[68:71], %[zr]\n" \
  "s_waitcnt lgkmcnt(8)\n" \
  "v_and_b32 v92, 0xffff0000, v35\n" \
  "v_lshlrev_b32 v93, 16, v35\n" \
  "v_and_b32 v94, 0xffff0000, v34\n" \
  "v_lshlrev_b32 v95, 16, v34\n" \
  "v_fmac_f32 v148, %[d1], v92\n" \
  "v_fmac_f32 v149, %[d1], v93\n" \
  "v_fmac_f32 v150, %[d1], v94\n" \
  "v_fmac_f32 v151, %[d1], v95\n" \
  "v_cvt_pk_bf16_f32 v148, v148, v149\n" \
  "v_cvt_pk_bf16_f32 v149, v150, v151\n" \
  "ds_write_b64 %[zw], v[148:149]\n" \
  "ds_read_b128 v[72:75], %[zr]\n" \
  "s_waitcnt lgkmcnt(9)\n" \
  "v_and_b32 v92, 0xffff0000, v37\n" \
  "v_lshlrev_b32 v93, 16, v37\n" \
  "v_and_b32 v94, 0xffff0000, v36\n" \
  "v_lshlrev_b32 v95, 16, v36\n" \
  "v_fmac_f32 v152, %[d1], v92\n" \
  "v_fmac_f32 v153, %[d1], v93\n" \
  "v_fmac_f32 v154, %[d1], v94\n" \
  "v_fmac_f32 v155, %[d1], v95\n" \
  "v_cvt_pk_bf16_f32 v152, v152, v153\n" \
  "v_cvt_pk_bf16_f32 v153, v154, v155\n" \
  "ds_write_b64 %[zw], v[152:153]\n" \
  "ds_read_b128 v[76:79], %[zr]\n" \
  "s_waitcnt lgkmcnt(10)\n" \
  "v_and_b32 v92, 0xffff0000, v39\n" \
  "v_lshlrev_b32 v93, 16, v39\n" \
  "v_and_b32 v94, 0xffff0000, v38\n" \
  "v_lshlrev_b32 v95, 16, v38\n" \
  "v_fmac_f32 v140, %[d2], v92\n" \
  "v_fmac_f32 v141, %[d2], v93\n" \
  "v_fmac_f32 v142, %[d2], v94\n" \
  "v_fmac_f32 v143, %[d2], v95\n" \
  "v_cvt_pk_bf16_f32 v140, v140, v141\n" \
  "v_cvt_pk_bf16_f32 v141, v142, v143\n" \
  "ds_write_b64 %[zw], v[140:141]\n" \
  "ds_read_b128 v[80:83], %[zr]\n" \
  "s_mov_b32 exec_hi, 0\n" \
  "s_waitcnt lgkmcnt(10)\n" \
  "buffer_store_dwordx4 v[60:63], %[zo], %[rs], %[z1] offen offset:0 sc1\n" \
  "s_waitcnt lgkmcnt(8)\n" \
  "buffer_store_dwordx4 v[64:67], %[zo], %[rs], %[z1] offen offset:512 sc1\n" \
  "s_waitcnt lgkmcnt(6)\n" \
  "buffer_store_dwordx4 v[68:71], %[zo], %[rs], %[z1] offen offset:1536 sc1\n" \
  "s_waitcnt lgkmcnt(4)\n" \
  "buffer_store_dwordx4 v[72:75], %[zo], %[rs], %[z1] offen offset:2048 sc1\n" \
  "s_waitcnt lgkmcnt(2)\n" \
  "buffer_store_dwordx4 v[76:79], %[zo], %[rs], %[z1] offen offset:2560 sc1\n" \
  "s_waitcnt lgkmcnt(0)\n" \
  "buffer_store_dwordx4 v[80:83], %[zo], %[rs], %[z2] offen offset:1024 sc1\n" \
  "s_mov_b64 exec, -1\n" \
  "s_branch 99f\n" \
  "99:\n" \
  "s_nop 1\n"
#define CONV_CLOB "v28","v29","v30","v31","v32","v33","v34","v35","v36","v37","v38","v39","v40","v41","v42","v43","v44","v45","v46","v47","v48","v49","v50","v51","v52","v53","v54","v55","v56","v57","v58","v59","v60","v61","v62","v63","v64","v65","v66","v67","v68","v69","v70","v71","v72","v73","v74","v75","v76","v77","v78","v79","v80","v81","v82","v83","v84","v85","v86","v87","v88","v89","v90","v91","v92","v93","v94","v95","v96","v97","v98","v99","v100","v101","v102","v103","v104","v105","v106","v107","v108","v109","v110","v111","v112","v113","v114","v115","v116","v117","v118","v119","v120","v121","v122","v123","v124","v125","v126","v127","v128","v129","v130","v131","v132","v133","v134","v135","v136","v137","v138","v139","v140","v141","v142","v143","v144","v145","v146","v147","v148","v149","v150","v151","v152","v153","v154","v155","v156","v157","v158","v159","v160","v161","v162","v163"
#define QFIRST \
  "s_mov_b32 m0, %[xm]\n" \
  "s_setprio 2\n" \
  "buffer_load_dwordx4 %[xv], %[xs], %[xo] offen lds\n" \
  "s_add_u32 m0, m0, 8192\n" \
  "s_add_u32 %[xo], %[xo], 128\n" \
  "s_cmp_eq_u32 %[xo], 1152\n" \
  "s_cselect_b32 %[xo], 0, %[xo]\n" \
  "buffer_load_dwordx4 %[xv], %[xs], %[xo] offen lds\n" \
  "s_add_u32 m0, m0, 8192\n" \
  "s_add_u32 %[xo], %[xo], 128\n" \
  "s_cmp_eq_u32 %[xo], 1152\n" \
  "s_cselect_b32 %[xo], 0, %[xo]\n" \
  "buffer_load_dwordx4 %[xv], %[xs], %[xo] offen lds\n" \
  "s_add_u32 m0, m0, 8192\n" \
  "s_add_u32 %[xo], %[xo], 128\n" \
  "s_cmp_eq_u32 %[xo], 1152\n" \
  "s_cselect_b32 %[xo], 0, %[xo]\n" \
  "buffer_load_dwordx4 v[40:43], %[wv], %[ws], %[wo] offen\n" \
  "s_add_u32 %[wo], %[wo], 0x9000\n" \
  "s_cmp_eq_u32 %[wo], 0xa2000\n" \
  "s_cselect_b32 %[wo], 0, %[wo]\n" \
  "buffer_load_dwordx4 v[44:47], %[wv], %[ws], %[wo] offen\n" \
  "s_add_u32 %[wo], %[wo], 0x9000\n" \
  "s_cmp_eq_u32 %[wo], 0xa2000\n" \
  "s_cselect_b32 %[wo], 0, %[wo]\n" \
  "buffer_load_dwordx4 v[48:51], %[wv], %[ws], %[wo] offen\n" \
  "s_add_u32 %[wo], %[wo], 0x9000\n" \
  "s_cmp_eq_u32 %[wo], 0xa2000\n" \
  "s_cselect_b32 %[wo], 0, %[wo]\n" \
  "buffer_load_dwordx4 v[52:55], %[wv], %[ws], %[wo] offen\n" \
  "s_add_u32 %[wo], %[wo], 0x9000\n" \
  "s_cmp_eq_u32 %[wo], 0xa2000\n" \
  "s_cselect_b32 %[wo], 0, %[wo]\n" \
  "buffer_load_dwordx4 v[56:59], %[wv], %[ws], %[wo] offen\n" \
  "s_add_u32 %[wo], %[wo], 0x9000\n" \
  "s_cmp_eq_u32 %[wo], 0xa2000\n" \
  "s_cselect_b32 %[wo], 0, %[wo]\n" \
  "buffer_load_dwordx4 v[60:63], %[wv], %[ws], %[wo] offen\n" \
  "s_add_u32 %[wo], %[wo], 0x9000\n" \
  "s_cmp_eq_u32 %[wo], 0xa2000\n" \
  "s_cselect_b32 %[wo], 0, %[wo]\n" \
  "s_setprio 1\n"
#define QPROJ0 \
  "s_mov_b32 m0, %[xm]\n" \
  "s_waitcnt vmcnt(5)\n" \
  "s_mov_b64 exec, %[wmask]\n" \
  "ds_write_b128 %[wd], v[40:43] offset:0\n" \
  "s_mov_b64 exec, -1\n" \
  "buffer_load_dwordx4 %[xv], %[xs], %[xo] offen lds\n" \
  "s_add_u32 m0, m0, 8192\n" \
  "s_add_u32 %[xo], %[xo], 128\n" \
  "s_cmp_eq_u32 %[xo], 1152\n" \
  "s_cselect_b32 %[xo], 0, %[xo]\n" \
  "buffer_load_dwordx4 %[xv], %[xs], %[xo] offen lds\n" \
  "s_add_u32 m0, m0, 8192\n" \
  "s_add_u32 %[xo], %[xo], 128\n" \
  "s_cmp_eq_u32 %[xo], 1152\n" \
  "s_cselect_b32 %[xo], 0, %[xo]\n" \
  "s_waitcnt vmcnt(6)\n" \
  "s_mov_b64 exec, %[wmask]\n" \
  "ds_write_b128 %[wd], v[44:47] offset:4608\n" \
  "s_mov_b64 exec, -1\n" \
  "buffer_load_dwordx4 %[xv], %[xs], %[xo] offen lds\n" \
  "s_add_u32 m0, m0, 8192\n" \
  "s_add_u32 %[xo], %[xo], 128\n" \
  "s_cmp_eq_u32 %[xo], 1152\n" \
  "s_cselect_b32 %[xo], 0, %[xo]\n" \
  "buffer_load_dwordx4 %[xv], %[xs], %[xo] offen lds\n" \
  "s_add_u32 m0, m0, 8192\n" \
  "s_add_u32 %[xo], %[xo], 128\n" \
  "s_cmp_eq_u32 %[xo], 1152\n" \
  "s_cselect_b32 %[xo], 0, %[xo]\n" \
  "s_waitcnt vmcnt(7)\n" \
  "s_mov_b64 exec, %[wmask]\n" \
  "ds_write_b128 %[wd], v[48:51] offset:9216\n" \
  "s_mov_b64 exec, -1\n" \
  "buffer_load_dwordx4 v[112:115], %[wv], %[ws], %[wo] offen\n" \
  "s_add_u32 %[wo], %[wo], 0x9000\n" \
  "s_cmp_eq_u32 %[wo], 0xa2000\n" \
  "s_cselect_b32 %[wo], 0, %[wo]\n" \
  "buffer_load_dwordx4 v[116:119], %[wv], %[ws], %[wo] offen\n" \
  "s_add_u32 %[wo], %[wo], 0x9000\n" \
  "s_cmp_eq_u32 %[wo], 0xa2000\n" \
  "s_cselect_b32 %[wo], 0, %[wo]\n" \
  "s_waitcnt vmcnt(8)\n" \
  "s_mov_b64 exec, %[wmask]\n" \
  "ds_write_b128 %[wd], v[52:55] offset:13824\n" \
  "s_mov_b64 exec, -1\n" \
  "buffer_load_dwordx4 v[120:123], %[wv], %[ws], %[wo] offen\n" \
  "s_add_u32 %[wo], %[wo], 0x9000\n" \
  "s_cmp_eq_u32 %[wo], 0xa2000\n" \
  "s_cselect_b32 %[wo], 0, %[wo]\n" \
  "buffer_load_dwordx4 v[124:127], %[wv], %[ws], %[wo] offen\n" \
  "s_add_u32 %[wo], %[wo], 0x9000\n" \
  "s_cmp_eq_u32 %[wo], 0xa2000\n" \
  "s_cselect_b32 %[wo], 0, %[wo]\n" \
  "s_waitcnt vmcnt(9)\n" \
  "s_mov_b64 exec, %[wmask]\n" \
  "ds_write_b128 %[wd], v[56:59] offset:18432\n" \
  "s_mov_b64 exec, -1\n" \
  "buffer_load_dwordx4 v[128:131], %[wv], %[ws], %[wo] offen\n" \
  "s_add_u32 %[wo], %[wo], 0x9000\n" \
  "s_cmp_eq_u32 %[wo], 0xa2000\n" \
  "s_cselect_b32 %[wo], 0, %[wo]\n" \
  "buffer_load_dwordx4 v[132:135], %[wv], %[ws], %[wo] offen\n" \
  "s_add_u32 %[wo], %[wo], 0x9000\n" \
  "s_cmp_eq_u32 %[wo], 0xa2000\n" \
  "s_cselect_b32 %[wo], 0, %[wo]\n" \
  "s_waitcnt vmcnt(10)\n" \
  "s_mov_b64 exec, %[wmask]\n" \
  "ds_write_b128 %[wd], v[60:63] offset:23040\n" \
  "s_mov_b64 exec, -1\n" \
  "buffer_load_dwordx4 v[136:139], %[wv], %[ws], %[wo] offen\n" \
  "s_add_u32 %[wo], %[wo], 0x9000\n" \
  "s_cmp_eq_u32 %[wo], 0xa2000\n" \
  "s_cselect_b32 %[wo], 0, %[wo]\n" \
  "buffer_load_dwordx4 v[140:143], %[wv], %[ws], %[wo] offen\n" \
  "s_add_u32 %[wo], %[wo], 0x9000\n" \
  "s_cmp_eq_u32 %[wo], 0xa2000\n" \
  "s_cselect_b32 %[wo], 0, %[wo]\n" \
  "s_waitcnt lgkmcnt(0)\n" \
  "s_mov_b64 exec, 1\n" \
  "ds_add_u32 %[ctl], %[one] offset:52\n" \
  "s_mov_b64 exec, -1\n" \
  "s_setprio 0\n" \
  "buffer_load_dwordx4 %[xv], %[xs], %[xo] offen lds\n" \
  "s_add_u32 m0, m0, 8192\n" \
  "s_add_u32 %[xo], %[xo], 128\n" \
  "s_cmp_eq_u32 %[xo], 1152\n" \
  "s_cselect_b32 %[xo], 0, %[xo]\n" \
  "buffer_load_dwordx4 %[xv], %[xs], %[xo] offen lds\n" \
  "s_add_u32 m0, m0, 8192\n" \
  "s_add_u32 %[xo], %[xo], 128\n" \
  "s_cmp_eq_u32 %[xo], 1152\n" \
  "s_cselect_b32 %[xo], 0, %[xo]\n" \
  "buffer_load_dwordx4 v[144:147], %[wv], %[ws], %[wo] offen\n" \
  "s_add_u32 %[wo], %[wo], 0x9000\n" \
  "s_cmp_eq_u32 %[wo], 0xa2000\n" \
  "s_cselect_b32 %[wo], 0, %[wo]\n" \
  "buffer_load_dwordx4 v[148:151], %[wv], %[ws], %[wo] offen\n" \
  "s_add_u32 %[wo], %[wo], 0x9000\n" \
  "s_cmp_eq_u32 %[wo], 0xa2000\n" \
  "s_cselect_b32 %[wo], 0, %[wo]\n" \
  "buffer_load_dwordx4 v[152:155], %[wv], %[ws], %[wo] offen\n" \
  "s_add_u32 %[wo], %[wo], 0x9000\n" \
  "s_cmp_eq_u32 %[wo], 0xa2000\n" \
  "s_cselect_b32 %[wo], 0, %[wo]\n" \
  "buffer_load_dwordx4 v[156:159], %[wv], %[ws], %[wo] offen\n" \
  "s_add_u32 %[wo], %[wo], 0x9000\n" \
  "s_cmp_eq_u32 %[wo], 0xa2000\n" \
  "s_cselect_b32 %[wo], 0, %[wo]\n" \
  "s_waitcnt lgkmcnt(0)\n" \
  "s_mov_b64 exec, 1\n" \
  "ds_add_u32 %[ctl], %[one] offset:84\n" \
  "s_mov_b64 exec, -1\n" \
  "s_branch 49f\n" \
  "48:\n" \
  "s_sleep 1\n" \
  "49:\n" \
  "ds_read_b32 v35, %[ctl] offset:52\n" \
  "s_waitcnt lgkmcnt(0)\n" \
  "v_readfirstlane_b32 %[st], v35\n" \
  "s_nop 3\n" \
  "s_cmp_ge_u32 %[st], 8\n" \
  "s_cbranch_scc0 48b\n" \
  "ds_read_b128 v[36:39], %[x] offset:0\n" \
  "ds_read_b64_tr_b16 v[40:41], %[w] offset:0\n" \
  "ds_read_b64_tr_b16 v[42:43], %[w] offset:1024\n" \
  "ds_read_b64_tr_b16 v[44:45], %[w] offset:256\n" \
  "ds_read_b64_tr_b16 v[46:47], %[w] offset:1280\n" \
  "ds_read_b64_tr_b16 v[48:49], %[w] offset:512\n" \
  "ds_read_b64_tr_b16 v[50:51], %[w] offset:1536\n" \
  "ds_read_b128 v[52:55], %[y] offset:0\n" \
  "ds_read_b64_tr_b16 v[56:57], %[w] offset:4608\n" \
  "ds_read_b64_tr_b16 v[58:59], %[w] offset:5632\n" \
  "ds_read_b64_tr_b16 v[60:61], %[w] offset:4864\n" \
  "ds_read_b64_tr_b16 v[62:63], %[w] offset:5888\n" \
  "ds_read_b64_tr_b16 v[64:65], %[w] offset:5120\n" \
  "ds_read_b64_tr_b16 v[66:67], %[w] offset:6144\n" \
  "ds_read_b128 v[68:71], %[x] offset:8192\n" \
  "ds_read_b64_tr_b16 v[72:73], %[w] offset:9216\n" \
  "ds_read_b64_tr_b16 v[74:75], %[w] offset:10240\n" \
  "ds_read_b64_tr_b16 v[76:77], %[w] offset:9472\n" \
  "ds_read_b64_tr_b16 v[78:79], %[w] offset:10496\n" \
  "ds_read_b64_tr_b16 v[80:81], %[w] offset:9728\n" \
  "ds_read_b64_tr_b16 v[82:83], %[w] offset:10752\n" \
  "s_waitcnt lgkmcnt(14)\n" \
  "v_mfma_f32_16x16x32_bf16 v[100:103], v[40:43], v[36:39], 0\n" \
  "ds_read_b128 v[84:87], %[y] offset:8192\n" \
  "ds_read_b64_tr_b16 v[88:89], %[w] offset:13824\n" \
  "v_mfma_f32_16x16x32_bf16 v[104:107], v[44:47], v[36:39], 0\n" \
  "ds_read_b64_tr_b16 v[90:91], %[w] offset:14848\n" \
  "ds_read_b64_tr_b16 v[92:93], %[w] offset:14080\n" \
  "v_mfma_f32_16x16x32_bf16 v[108:111], v[48:51], v[36:39], 0\n" \
  "ds_read_b64_tr_b16 v[94:95], %[w] offset:15104\n" \
  "ds_read_b64_tr_b16 v[96:97], %[w] offset:14336\n" \
  "ds_read_b64_tr_b16 v[98:99], %[w] offset:15360\n" \
  "s_waitcnt lgkmcnt(14)\n" \
  "v_mfma_f32_16x16x32_bf16 v[100:103], v[56:59], v[52:55], v[100:103]\n" \
  "ds_read_b128 v[36:39], %[x] offset:16384\n" \
  "ds_read_b64_tr_b16 v[40:41], %[w] offset:18432\n" \
  "v_mfma_f32_16x16x32_bf16 v[104:107], v[60:63], v[52:55], v[104:107]\n" \
  "ds_read_b64_tr_b16 v[42:43], %[w] offset:19456\n" \
  "ds_read_b64_tr_b16 v[44:45], %[w] offset:18688\n" \
  "v_mfma_f32_16x16x32_bf16 v[108:111], v[64:67], v[52:55], v[108:111]\n" \
  "ds_read_b64_tr_b16 v[46:47], %[w] offset:19712\n" \
  "ds_read_b64_tr_b16 v[48:49], %[w] offset:18944\n" \
  "ds_read_b64_tr_b16 v[50:51], %[w] offset:19968\n" \
  "s_waitcnt lgkmcnt(14)\n" \
  "v_mfma_f32_16x16x32_bf16 v[100:103], v[72:75], v[68:71], v[100:103]\n" \
  "ds_read_b128 v[52:55], %[y] offset:16384\n" \
  "ds_read_b64_tr_b16 v[56:57], %[w] offset:23040\n" \
  "v_mfma_f32_16x16x32_bf16 v[104:107], v[76:79], v[68:71], v[104:107]\n" \
  "ds_read_b64_tr_b16 v[58:59], %[w] offset:24064\n" \
  "ds_read_b64_tr_b16 v[60:61], %[w] offset:23296\n" \
  "v_mfma_f32_16x16x32_bf16 v[108:111], v[80:83], v[68:71], v[108:111]\n" \
  "ds_read_b64_tr_b16 v[62:63], %[w] offset:24320\n" \
  "ds_read_b64_tr_b16 v[64:65], %[w] offset:23552\n" \
  "ds_read_b64_tr_b16 v[66:67], %[w] offset:24576\n" \
  "s_waitcnt lgkmcnt(14)\n" \
  "v_mfma_f32_16x16x32_bf16 v[100:103], v[88:91], v[84:87], v[100:103]\n" \
  "v_mfma_f32_16x16x32_bf16 v[104:107], v[92:95], v[84:87], v[104:107]\n" \
  "v_mfma_f32_16x16x32_bf16 v[108:111], v[96:99], v[84:87], v[108:111]\n" \
  "s_waitcnt lgkmcnt(7)\n" \
  "v_mfma_f32_16x16x32_bf16 v[100:103], v[40:43], v[36:39], v[100:103]\n" \
  "v_mfma_f32_16x16x32_bf16 v[104:107], v[44:47], v[36:39], v[104:107]\n" \
  "v_mfma_f32_16x16x32_bf16 v[108:111], v[48:51], v[36:39], v[108:111]\n" \
  "s_waitcnt lgkmcnt(0)\n" \
  "v_mfma_f32_16x16x32_bf16 v[100:103], v[56:59], v[52:55], v[100:103]\n" \
  "v_mfma_f32_16x16x32_bf16 v[104:107], v[60:63], v[52:55], v[104:107]\n" \
  "v_mfma_f32_16x16x32_bf16 v[108:111], v[64:67], v[52:55], v[108:111]\n" \
  "s_waitcnt lgkmcnt(0)\n" \
  "s_mov_b64 exec, 1\n" \
  "ds_add_u32 %[ctl], %[one] offset:64\n" \
  "s_mov_b64 exec, -1\n" \
  "s_branch 51f\n" \
  "50:\n" \
  "s_sleep 1\n" \
  "51:\n" \
  "ds_read_b32 v35, %[ctl] offset:40\n" \
  "s_waitcnt lgkmcnt(0)\n" \
  "v_readfirstlane_b32 %[st], v35\n" \
  "s_nop 3\n" \
  "s_cmp_ge_u32 %[st], 2\n" \
  "s_cbranch_scc0 50b\n" \
  "s_waitcnt vmcnt(13)\n" \
  "s_mov_b64 exec, %[wmask]\n" \
  "ds_write_b128 %[wd], v[112:115] offset:27648\n" \
  "s_mov_b64 exec, -1\n" \
  "s_waitcnt vmcnt(12)\n" \
  "s_mov_b64 exec, %[wmask]\n" \
  "ds_write_b128 %[wd], v[116:119] offset:32256\n" \
  "s_mov_b64 exec, -1\n" \
  "s_waitcnt vmcnt(11)\n" \
  "s_mov_b64 exec, %[wmask]\n" \
  "ds_write_b128 %[wd], v[120:123] offset:36864\n" \
  "s_mov_b64 exec, -1\n" \
  "s_waitcnt vmcnt(10)\n" \
  "s_mov_b64 exec, %[wmask]\n" \
  "ds_write_b128 %[wd], v[124:127] offset:41472\n" \
  "s_mov_b64 exec, -1\n" \
  "s_waitcnt vmcnt(9)\n" \
  "s_mov_b64 exec, %[wmask]\n" \
  "ds_write_b128 %[wd], v[128:131] offset:46080\n" \
  "s_mov_b64 exec, -1\n" \
  "s_waitcnt vmcnt(8)\n" \
  "s_mov_b64 exec, %[wmask]\n" \
  "ds_write_b128 %[wd], v[132:135] offset:50688\n" \
  "s_mov_b64 exec, -1\n" \
  "s_waitcnt vmcnt(7)\n" \
  "s_mov_b64 exec, %[wmask]\n" \
  "ds_write_b128 %[wd], v[136:139] offset:55296\n" \
  "s_mov_b64 exec, -1\n" \
  "s_waitcnt vmcnt(6)\n" \
  "s_mov_b64 exec, %[wmask]\n" \
  "ds_write_b128 %[wd], v[140:143] offset:59904\n" \
  "s_mov_b64 exec, -1\n" \
  "s_waitcnt lgkmcnt(0)\n" \
  "s_mov_b64 exec, 1\n" \
  "ds_add_u32 %[ctl], %[one] offset:56\n" \
  "s_mov_b64 exec, -1\n" \
  "s_waitcnt vmcnt(3)\n" \
  "s_mov_b64 exec, %[wmask]\n" \
  "ds_write_b128 %[wd2], v[144:147] offset:0\n" \
  "s_mov_b64 exec, -1\n" \
  "s_waitcnt vmcnt(2)\n" \
  "s_mov_b64 exec, %[wmask]\n" \
  "ds_write_b128 %[wd2], v[148:151] offset:4608\n" \
  "s_mov_b64 exec, -1\n" \
  "s_waitcnt vmcnt(1)\n" \
  "s_mov_b64 exec, %[wmask]\n" \
  "ds_write_b128 %[wd2], v[152:155] offset:9216\n" \
  "s_mov_b64 exec, -1\n" \
  "s_waitcnt vmcnt(0)\n" \
  "s_mov_b64 exec, %[wmask]\n" \
  "ds_write_b128 %[wd2], v[156:159] offset:13824\n" \
  "s_mov_b64 exec, -1\n" \
  "s_waitcnt lgkmcnt(0)\n" \
  "s_mov_b64 exec, 1\n" \
  "ds_add_u32 %[ctl], %[one] offset:60\n" \
  "s_mov_b64 exec, -1\n" \
  "s_branch 53f\n" \
  "52:\n" \
  "s_sleep 1\n" \
  "53:\n" \
  "ds_read_b32 v35, %[ctl] offset:56\n" \
  "s_waitcnt lgkmcnt(0)\n" \
  "v_readfirstlane_b32 %[st], v35\n" \
  "s_nop 3\n" \
  "s_cmp_ge_u32 %[st], 8\n" \
  "s_cbranch_scc0 52b\n" \
  "ds_read_b128 v[68:71], %[x] offset:24576\n" \
  "ds_read_b64_tr_b16 v[72:73], %[w] offset:27648\n" \
  "ds_read_b64_tr_b16 v[74:75], %[w] offset:28672\n" \
  "ds_read_b64_tr_b16 v[76:77], %[w] offset:27904\n" \
  "ds_read_b64_tr_b16 v[78:79], %[w] offset:28928\n" \
  "ds_read_b64_tr_b16 v[80:81], %[w] offset:28160\n" \
  "ds_read_b64_tr_b16 v[82:83], %[w] offset:29184\n" \
  "ds_read_b128 v[84:87], %[y] offset:24576\n" \
  "ds_read_b64_tr_b16 v[88:89], %[w] offset:32256\n" \
  "ds_read_b64_tr_b16 v[90:91], %[w] offset:33280\n" \
  "ds_read_b64_tr_b16 v[92:93], %[w] offset:32512\n" \
  "ds_read_b64_tr_b16 v[94:95], %[w] offset:33536\n" \
  "ds_read_b64_tr_b16 v[96:97], %[w] offset:32768\n" \
  "ds_read_b64_tr_b16 v[98:99], %[w] offset:33792\n" \
  "ds_read_b128 v[36:39], %[x] offset:32768\n" \
  "ds_read_b64_tr_b16 v[40:41], %[w] offset:36864\n" \
  "ds_read_b64_tr_b16 v[42:43], %[w] offset:37888\n" \
  "ds_read_b64_tr_b16 v[44:45], %[w] offset:37120\n" \
  "ds_read_b64_tr_b16 v[46:47], %[w] offset:38144\n" \
  "ds_read_b64_tr_b16 v[48:49], %[w] offset:37376\n" \
  "ds_read_b64_tr_b16 v[50:51], %[w] offset:38400\n" \
  "s_waitcnt lgkmcnt(14)\n" \
  "v_mfma_f32_16x16x32_bf16 v[100:103], v[72:75], v[68:71], v[100:103]\n" \
  "ds_read_b128 v[52:55], %[y] offset:32768\n" \
  "ds_read_b64_tr_b16 v[56:57], %[w] offset:41472\n" \
  "v_mfma_f32_16x16x32_bf16 v[104:107], v[76:79], v[68:71], v[104:107]\n" \
  "ds_read_b64_tr_b16 v[58:59], %[w] offset:42496\n" \
  "ds_read_b64_tr_b16 v[60:61], %[w] offset:41728\n" \
  "v_mfma_f32_16x16x32_bf16 v[108:111], v[80:83], v[68:71], v[108:111]\n" \
  "ds_read_b64_tr_b16 v[62:63], %[w] offset:42752\n" \
  "ds_read_b64_tr_b16 v[64:65], %[w] offset:41984\n" \
  "ds_read_b64_tr_b16 v[66:67], %[w] offset:43008\n" \
  "s_waitcnt lgkmcnt(14)\n" \
  "v_mfma_f32_16x16x32_bf16 v[100:103], v[88:91], v[84:87], v[100:103]\n" \
  "ds_read_b128 v[68:71], %[x] offset:40960\n" \
  "ds_read_b64_tr_b16 v[72:73], %[w] offset:46080\n" \
  "v_mfma_f32_16x16x32_bf16 v[104:107], v[92:95], v[84:87], v[104:107]\n" \
  "ds_read_b64_tr_b16 v[74:75], %[w] offset:47104\n" \
  "ds_read_b64_tr_b16 v[76:77], %[w] offset:46336\n" \
  "v_mfma_f32_16x16x32_bf16 v[108:111], v[96:99], v[84:87], v[108:111]\n" \
  "ds_read_b64_tr_b16 v[78:79], %[w] offset:47360\n" \
  "ds_read_b64_tr_b16 v[80:81], %[w] offset:46592\n" \
  "ds_read_b64_tr_b16 v[82:83], %[w] offset:47616\n" \
  "s_waitcnt lgkmcnt(14)\n" \
  "v_mfma_f32_16x16x32_bf16 v[100:103], v[40:43], v[36:39], v[100:103]\n" \
  "ds_read_b128 v[84:87], %[y] offset:40960\n" \
  "ds_read_b64_tr_b16 v[88:89], %[w] offset:50688\n" \
  "v_mfma_f32_16x16x32_bf16 v[104:107], v[44:47], v[36:39], v[104:107]\n" \
  "ds_read_b64_tr_b16 v[90:91], %[w] offset:51712\n" \
  "ds_read_b64_tr_b16 v[92:93], %[w] offset:50944\n" \
  "v_mfma_f32_16x16x32_bf16 v[108:111], v[48:51], v[36:39], v[108:111]\n" \
  "ds_read_b64_tr_b16 v[94:95], %[w] offset:51968\n" \
  "ds_read_b64_tr_b16 v[96:97], %[w] offset:51200\n" \
  "ds_read_b64_tr_b16 v[98:99], %[w] offset:52224\n" \
  "s_waitcnt lgkmcnt(14)\n" \
  "v_mfma_f32_16x16x32_bf16 v[100:103], v[56:59], v[52:55], v[100:103]\n" \
  "ds_read_b128 v[36:39], %[x] offset:49152\n" \
  "ds_read_b64_tr_b16 v[40:41], %[w] offset:55296\n" \
  "v_mfma_f32_16x16x32_bf16 v[104:107], v[60:63], v[52:55], v[104:107]\n" \
  "ds_read_b64_tr_b16 v[42:43], %[w] offset:56320\n" \
  "ds_read_b64_tr_b16 v[44:45], %[w] offset:55552\n" \
  "v_mfma_f32_16x16x32_bf16 v[108:111], v[64:67], v[52:55], v[108:111]\n" \
  "ds_read_b64_tr_b16 v[46:47], %[w] offset:56576\n" \
  "ds_read_b64_tr_b16 v[48:49], %[w] offset:55808\n" \
  "ds_read_b64_tr_b16 v[50:51], %[w] offset:56832\n" \
  "s_waitcnt lgkmcnt(14)\n" \
  "v_mfma_f32_16x16x32_bf16 v[100:103], v[72:75], v[68:71], v[100:103]\n" \
  "ds_read_b128 v[52:55], %[y] offset:49152\n" \
  "ds_read_b64_tr_b16 v[56:57], %[w] offset:59904\n" \
  "v_mfma_f32_16x16x32_bf16 v[104:107], v[76:79], v[68:71], v[104:107]\n" \
  "ds_read_b64_tr_b16 v[58:59], %[w] offset:60928\n" \
  "ds_read_b64_tr_b16 v[60:61], %[w] offset:60160\n" \
  "v_mfma_f32_16x16x32_bf16 v[108:111], v[80:83], v[68:71], v[108:111]\n" \
  "ds_read_b64_tr_b16 v[62:63], %[w] offset:61184\n" \
  "ds_read_b64_tr_b16 v[64:65], %[w] offset:60416\n" \
  "ds_read_b64_tr_b16 v[66:67], %[w] offset:61440\n" \
  "s_waitcnt lgkmcnt(14)\n" \
  "v_mfma_f32_16x16x32_bf16 v[100:103], v[88:91], v[84:87], v[100:103]\n" \
  "v_mfma_f32_16x16x32_bf16 v[104:107], v[92:95], v[84:87], v[104:107]\n" \
  "v_mfma_f32_16x16x32_bf16 v[108:111], v[96:99], v[84:87], v[108:111]\n" \
  "s_waitcnt lgkmcnt(7)\n" \
  "v_mfma_f32_16x16x32_bf16 v[100:103], v[40:43], v[36:39], v[100:103]\n" \
  "v_mfma_f32_16x16x32_bf16 v[104:107], v[44:47], v[36:39], v[104:107]\n" \
  "v_mfma_f32_16x16x32_bf16 v[108:111], v[48:51], v[36:39], v[108:111]\n" \
  "s_waitcnt lgkmcnt(0)\n" \
  "v_mfma_f32_16x16x32_bf16 v[100:103], v[56:59], v[52:55], v[100:103]\n" \
  "v_mfma_f32_16x16x32_bf16 v[104:107], v[60:63], v[52:55], v[104:107]\n" \
  "v_mfma_f32_16x16x32_bf16 v[108:111], v[64:67], v[52:55], v[108:111]\n" \
  "v_add_u32 %[x], 0xe000, %[x]\n" \
  "v_add_u32 %[y], 0xe000, %[y]\n" \
  "v_mov_b32 v144, %[vone]\n" \
  "v_mov_b32 v145, %[vone]\n" \
  "v_mov_b32 v146, %[vone]\n" \
  "v_mov_b32 v147, %[vone]\n" \
  "v_mov_b32 v148, %[vone]\n" \
  "v_mov_b32 v149, %[vone]\n" \
  "v_mov_b32 v150, %[vone]\n" \
  "v_mov_b32 v151, %[vone]\n" \
  "v_mov_b32 v152, %[vone]\n" \
  "v_mov_b32 v153, %[vone]\n" \
  "v_mov_b32 v154, %[vone]\n" \
  "v_mov_b32 v155, %[vone]\n" \
  "v_mov_b32 v156, %[vone]\n" \
  "v_mov_b32 v157, %[vone]\n" \
  "v_mov_b32 v158, %[vone]\n" \
  "v_mov_b32 v159, %[vone]\n" \
  "v_mov_b32 v112, 0\n" \
  "v_mov_b32 v113, 0\n" \
  "v_mov_b32 v114, 0\n" \
  "v_mov_b32 v115, 0\n" \
  "v_mov_b32 v116, 0\n" \
  "v_mov_b32 v117, 0\n" \
  "v_mov_b32 v118, 0\n" \
  "v_mov_b32 v119, 0\n" \
  "v_mov_b32 v120, 0\n" \
  "v_mov_b32 v121, 0\n" \
  "v_mov_b32 v122, 0\n" \
  "v_mov_b32 v123, 0\n" \
  "v_mov_b32 v124, 0\n" \
  "v_mov_b32 v125, 0\n" \
  "v_mov_b32 v126, 0\n" \
  "v_mov_b32 v127, 0\n" \
  "v_mov_b32 v128, 0\n" \
  "v_mov_b32 v129, 0\n" \
  "v_mov_b32 v130, 0\n" \
  "v_mov_b32 v131, 0\n" \
  "v_mov_b32 v132, 0\n" \
  "v_mov_b32 v133, 0\n" \
  "v_mov_b32 v134, 0\n" \
  "v_mov_b32 v135, 0\n" \
  "v_mov_b32 v136, 0\n" \
  "v_mov_b32 v137, 0\n" \
  "v_mov_b32 v138, 0\n" \
  "v_mov_b32 v139, 0\n" \
  "v_mov_b32 v140, 0\n" \
  "v_mov_b32 v141, 0\n" \
  "v_mov_b32 v142, 0\n" \
  "v_mov_b32 v143, 0\n" \
  "s_branch 55f\n" \
  "54:\n" \
  "s_sleep 1\n" \
  "55:\n" \
  "ds_read_b32 v35, %[ctl] offset:60\n" \
  "s_waitcnt lgkmcnt(0)\n" \
  "v_readfirstlane_b32 %[st], v35\n" \
  "s_nop 3\n" \
  "s_cmp_ge_u32 %[st], 8\n" \
  "s_cbranch_scc0 54b\n" \
  "ds_read_b128 v[68:71], %[x] offset:0\n" \
  "ds_read_b64_tr_b16 v[72:73], %[u] offset:0\n" \
  "ds_read_b64_tr_b16 v[74:75], %[u] offset:1024\n" \
  "ds_read_b64_tr_b16 v[76:77], %[u] offset:256\n" \
  "ds_read_b64_tr_b16 v[78:79], %[u] offset:1280\n" \
  "ds_read_b64_tr_b16 v[80:81], %[u] offset:512\n" \
  "ds_read_b64_tr_b16 v[82:83], %[u] offset:1536\n" \
  "ds_read_b128 v[84:87], %[y] offset:0\n" \
  "ds_read_b64_tr_b16 v[88:89], %[u] offset:4608\n" \
  "ds_read_b64_tr_b16 v[90:91], %[u] offset:5632\n" \
  "ds_read_b64_tr_b16 v[92:93], %[u] offset:4864\n" \
  "ds_read_b64_tr_b16 v[94:95], %[u] offset:5888\n" \
  "ds_read_b64_tr_b16 v[96:97], %[u] offset:5120\n" \
  "ds_read_b64_tr_b16 v[98:99], %[u] offset:6144\n" \
  "ds_read_b128 v[36:39], %[x] offset:8192\n" \
  "ds_read_b64_tr_b16 v[40:41], %[u] offset:9216\n" \
  "ds_read_b64_tr_b16 v[42:43], %[u] offset:10240\n" \
  "ds_read_b64_tr_b16 v[44:45], %[u] offset:9472\n" \
  "ds_read_b64_tr_b16 v[46:47], %[u] offset:10496\n" \
  "ds_read_b64_tr_b16 v[48:49], %[u] offset:9728\n" \
  "ds_read_b64_tr_b16 v[50:51], %[u] offset:10752\n" \
  "s_waitcnt lgkmcnt(14)\n" \
  "v_mfma_f32_16x16x32_bf16 v[100:103], v[72:75], v[68:71], v[100:103]\n" \
  "ds_read_b128 v[52:55], %[y] offset:8192\n" \
  "ds_read_b64_tr_b16 v[56:57], %[u] offset:13824\n" \
  "v_mfma_f32_16x16x32_bf16 v[104:107], v[76:79], v[68:71], v[104:107]\n" \
  "ds_read_b64_tr_b16 v[58:59], %[u] offset:14848\n" \
  "ds_read_b64_tr_b16 v[60:61], %[u] offset:14080\n" \
  "v_mfma_f32_16x16x32_bf16 v[108:111], v[80:83], v[68:71], v[108:111]\n" \
  "ds_read_b64_tr_b16 v[62:63], %[u] offset:15104\n" \
  "ds_read_b64_tr_b16 v[64:65], %[u] offset:14336\n" \
  "ds_read_b64_tr_b16 v[66:67], %[u] offset:15360\n" \
  "s_waitcnt lgkmcnt(14)\n" \
  "v_mfma_f32_16x16x32_bf16 v[100:103], v[88:91], v[84:87], v[100:103]\n" \
  "v_mfma_f32_16x16x32_bf16 v[104:107], v[92:95], v[84:87], v[104:107]\n" \
  "v_mfma_f32_16x16x32_bf16 v[108:111], v[96:99], v[84:87], v[108:111]\n" \
  "s_waitcnt lgkmcnt(7)\n" \
  "v_mfma_f32_16x16x32_bf16 v[100:103], v[40:43], v[36:39], v[100:103]\n" \
  "v_mfma_f32_16x16x32_bf16 v[104:107], v[44:47], v[36:39], v[104:107]\n" \
  "v_mfma_f32_16x16x32_bf16 v[108:111], v[48:51], v[36:39], v[108:111]\n" \
  "s_waitcnt lgkmcnt(0)\n" \
  "v_mfma_f32_16x16x32_bf16 v[100:103], v[56:59], v[52:55], v[100:103]\n" \
  "v_mfma_f32_16x16x32_bf16 v[104:107], v[60:63], v[52:55], v[104:107]\n" \
  "v_mfma_f32_16x16x32_bf16 v[108:111], v[64:67], v[52:55], v[108:111]\n" \
  "s_nop 7\n" \
  "v_mul_f32 v100, 0x3eae1a92, v100\n" \
  "v_mul_f32 v101, 0x3eae1a92, v101\n" \
  "v_mul_f32 v102, 0x3eae1a92, v102\n" \
  "v_mul_f32 v103, 0x3eae1a92, v103\n" \
  "v_cvt_pk_bf16_f32 v100, v100, v101\n" \
  "v_cvt_pk_bf16_f32 v101, v102, v103\n" \
  "ds_write_b64 %[qt], v[100:101] offset:0\n" \
  "v_mul_f32 v104, 0x3eae1a92, v104\n" \
  "v_mul_f32 v105, 0x3eae1a92, v105\n" \
  "v_mul_f32 v106, 0x3eae1a92, v106\n" \
  "v_mul_f32 v107, 0x3eae1a92, v107\n" \
  "v_cvt_pk_bf16_f32 v104, v104, v105\n" \
  "v_cvt_pk_bf16_f32 v105, v106, v107\n" \
  "ds_write_b64 %[qt], v[104:105] offset:32\n" \
  "v_mul_f32 v108, 0x3eae1a92, v108\n" \
  "v_mul_f32 v109, 0x3eae1a92, v109\n" \
  "v_mul_f32 v110, 0x3eae1a92, v110\n" \
  "v_mul_f32 v111, 0x3eae1a92, v111\n" \
  "v_cvt_pk_bf16_f32 v108, v108, v109\n" \
  "v_cvt_pk_bf16_f32 v109, v110, v111\n" \
  "ds_write_b64 %[qt], v[108:109] offset:64\n" \
  "s_mov_b32 exec_hi, -1\n" \
  "s_waitcnt lgkmcnt(0)\n" \
  "s_mov_b64 exec, 1\n" \
  "ds_add_u32 %[ctl], %[one] offset:68\n" \
  "s_mov_b64 exec, -1\n"
#define QPROJ1 \
  "s_mov_b32 m0, %[xm]\n" \
  "s_waitcnt vmcnt(5)\n" \
  "s_mov_b64 exec, %[wmask]\n" \
  "ds_write_b128 %[wd], v[40:43] offset:0\n" \
  "s_mov_b64 exec, -1\n" \
  "buffer_load_dwordx4 %[xv], %[xs], %[xo] offen lds\n" \
  "s_add_u32 m0, m0, 8192\n" \
  "s_add_u32 %[xo], %[xo], 128\n" \
  "s_cmp_eq_u32 %[xo], 1152\n" \
  "s_cselect_b32 %[xo], 0, %[xo]\n" \
  "buffer_load_dwordx4 %[xv], %[xs], %[xo] offen lds\n" \
  "s_add_u32 m0, m0, 8192\n" \
  "s_add_u32 %[xo], %[xo], 128\n" \
  "s_cmp_eq_u32 %[xo], 1152\n" \
  "s_cselect_b32 %[xo], 0, %[xo]\n" \
  "s_waitcnt vmcnt(6)\n" \
  "s_mov_b64 exec, %[wmask]\n" \
  "ds_write_b128 %[wd], v[44:47] offset:4608\n" \
  "s_mov_b64 exec, -1\n" \
  "buffer_load_dwordx4 %[xv], %[xs], %[xo] offen lds\n" \
  "s_add_u32 m0, m0, 8192\n" \
  "s_add_u32 %[xo], %[xo], 128\n" \
  "s_cmp_eq_u32 %[xo], 1152\n" \
  "s_cselect_b32 %[xo], 0, %[xo]\n" \
  "buffer_load_dwordx4 %[xv], %[xs], %[xo] offen lds\n" \
  "s_add_u32 m0, m0, 8192\n" \
  "s_add_u32 %[xo], %[xo], 128\n" \
  "s_cmp_eq_u32 %[xo], 1152\n" \
  "s_cselect_b32 %[xo], 0, %[xo]\n" \
  "s_waitcnt vmcnt(7)\n" \
  "s_mov_b64 exec, %[wmask]\n" \
  "ds_write_b128 %[wd], v[48:51] offset:9216\n" \
  "s_mov_b64 exec, -1\n" \
  "buffer_load_dwordx4 v[112:115], %[wv], %[ws], %[wo] offen\n" \
  "s_add_u32 %[wo], %[wo], 0x9000\n" \
  "s_cmp_eq_u32 %[wo], 0xa2000\n" \
  "s_cselect_b32 %[wo], 0, %[wo]\n" \
  "buffer_load_dwordx4 v[116:119], %[wv], %[ws], %[wo] offen\n" \
  "s_add_u32 %[wo], %[wo], 0x9000\n" \
  "s_cmp_eq_u32 %[wo], 0xa2000\n" \
  "s_cselect_b32 %[wo], 0, %[wo]\n" \
  "s_waitcnt vmcnt(8)\n" \
  "s_mov_b64 exec, %[wmask]\n" \
  "ds_write_b128 %[wd], v[52:55] offset:13824\n" \
  "s_mov_b64 exec, -1\n" \
  "buffer_load_dwordx4 v[120:123], %[wv], %[ws], %[wo] offen\n" \
  "s_add_u32 %[wo], %[wo], 0x9000\n" \
  "s_cmp_eq_u32 %[wo], 0xa2000\n" \
  "s_cselect_b32 %[wo], 0, %[wo]\n" \
  "buffer_load_dwordx4 v[124:127], %[wv], %[ws], %[wo] offen\n" \
  "s_add_u32 %[wo], %[wo], 0x9000\n" \
  "s_cmp_eq_u32 %[wo], 0xa2000\n" \
  "s_cselect_b32 %[wo], 0, %[wo]\n" \
  "s_waitcnt vmcnt(9)\n" \
  "s_mov_b64 exec, %[wmask]\n" \
  "ds_write_b128 %[wd], v[56:59] offset:18432\n" \
  "s_mov_b64 exec, -1\n" \
  "buffer_load_dwordx4 v[128:131], %[wv], %[ws], %[wo] offen\n" \
  "s_add_u32 %[wo], %[wo], 0x9000\n" \
  "s_cmp_eq_u32 %[wo], 0xa2000\n" \
  "s_cselect_b32 %[wo], 0, %[wo]\n" \
  "buffer_load_dwordx4 v[132:135], %[wv], %[ws], %[wo] offen\n" \
  "s_add_u32 %[wo], %[wo], 0x9000\n" \
  "s_cmp_eq_u32 %[wo], 0xa2000\n" \
  "s_cselect_b32 %[wo], 0, %[wo]\n" \
  "s_waitcnt vmcnt(10)\n" \
  "s_mov_b64 exec, %[wmask]\n" \
  "ds_write_b128 %[wd], v[60:63] offset:23040\n" \
  "s_mov_b64 exec, -1\n" \
  "buffer_load_dwordx4 v[136:139], %[wv], %[ws], %[wo] offen\n" \
  "s_add_u32 %[wo], %[wo], 0x9000\n" \
  "s_cmp_eq_u32 %[wo], 0xa2000\n" \
  "s_cselect_b32 %[wo], 0, %[wo]\n" \
  "buffer_load_dwordx4 v[140:143], %[wv], %[ws], %[wo] offen\n" \
  "s_add_u32 %[wo], %[wo], 0x9000\n" \
  "s_cmp_eq_u32 %[wo], 0xa2000\n" \
  "s_cselect_b32 %[wo], 0, %[wo]\n" \
  "s_waitcnt lgkmcnt(0)\n" \
  "s_mov_b64 exec, 1\n" \
  "ds_add_u32 %[ctl], %[one] offset:52\n" \
  "s_mov_b64 exec, -1\n" \
  "s_setprio 0\n" \
  "buffer_load_dwordx4 %[xv], %[xs], %[xo] offen lds\n" \
  "s_add_u32 m0, m0, 8192\n" \
  "s_add_u32 %[xo], %[xo], 128\n" \
  "s_cmp_eq_u32 %[xo], 1152\n" \
  "s_cselect_b32 %[xo], 0, %[xo]\n" \
  "buffer_load_dwordx4 %[xv], %[xs], %[xo] offen lds\n" \
  "s_add_u32 m0, m0, 8192\n" \
  "s_add_u32 %[xo], %[xo], 128\n" \
  "s_cmp_eq_u32 %[xo], 1152\n" \
  "s_cselect_b32 %[xo], 0, %[xo]\n" \
  "buffer_load_dwordx4 v[144:147], %[wv], %[ws], %[wo] offen\n" \
  "s_add_u32 %[wo], %[wo], 0x9000\n" \
  "s_cmp_eq_u32 %[wo], 0xa2000\n" \
  "s_cselect_b32 %[wo], 0, %[wo]\n" \
  "buffer_load_dwordx4 v[148:151], %[wv], %[ws], %[wo] offen\n" \
  "s_add_u32 %[wo], %[wo], 0x9000\n" \
  "s_cmp_eq_u32 %[wo], 0xa2000\n" \
  "s_cselect_b32 %[wo], 0, %[wo]\n" \
  "buffer_load_dwordx4 v[152:155], %[wv], %[ws], %[wo] offen\n" \
  "s_add_u32 %[wo], %[wo], 0x9000\n" \
  "s_cmp_eq_u32 %[wo], 0xa2000\n" \
  "s_cselect_b32 %[wo], 0, %[wo]\n" \
  "buffer_load_dwordx4 v[156:159], %[wv], %[ws], %[wo] offen\n" \
  "s_add_u32 %[wo], %[wo], 0x9000\n" \
  "s_cmp_eq_u32 %[wo], 0xa2000\n" \
  "s_cselect_b32 %[wo], 0, %[wo]\n" \
  "s_waitcnt lgkmcnt(0)\n" \
  "s_mov_b64 exec, 1\n" \
  "ds_add_u32 %[ctl], %[one] offset:84\n" \
  "s_mov_b64 exec, -1\n" \
  "s_branch 57f\n" \
  "56:\n" \
  "s_sleep 1\n" \
  "57:\n" \
  "ds_read_b32 v35, %[ctl] offset:52\n" \
  "s_waitcnt lgkmcnt(0)\n" \
  "v_readfirstlane_b32 %[st], v35\n" \
  "s_nop 3\n" \
  "s_cmp_ge_u32 %[st], 8\n" \
  "s_cbranch_scc0 56b\n" \
  "ds_read_b128 v[36:39], %[x] offset:0\n" \
  "ds_read_b64_tr_b16 v[40:41], %[w] offset:768\n" \
  "ds_read_b64_tr_b16 v[42:43], %[w] offset:1792\n" \
  "ds_read_b64_tr_b16 v[44:45], %[e] offset:0\n" \
  "ds_read_b64_tr_b16 v[46:47], %[e] offset:64\n" \
  "ds_read_b128 v[48:51], %[y] offset:0\n" \
  "ds_read_b64_tr_b16 v[52:53], %[w] offset:5376\n" \
  "ds_read_b64_tr_b16 v[54:55], %[w] offset:6400\n" \
  "ds_read_b64_tr_b16 v[56:57], %[e] offset:4608\n" \
  "ds_read_b64_tr_b16 v[58:59], %[e] offset:4672\n" \
  "ds_read_b128 v[60:63], %[x] offset:8192\n" \
  "ds_read_b64_tr_b16 v[64:65], %[w] offset:9984\n" \
  "ds_read_b64_tr_b16 v[66:67], %[w] offset:11008\n" \
  "ds_read_b64_tr_b16 v[68:69], %[e] offset:9216\n" \
  "ds_read_b64_tr_b16 v[70:71], %[e] offset:9280\n" \
  "ds_read_b128 v[72:75], %[y] offset:8192\n" \
  "ds_read_b64_tr_b16 v[76:77], %[w] offset:14592\n" \
  "ds_read_b64_tr_b16 v[78:79], %[w] offset:15616\n" \
  "ds_read_b64_tr_b16 v[80:81], %[e] offset:13824\n" \
  "ds_read_b64_tr_b16 v[82:83], %[e] offset:13888\n" \
  "s_waitcnt lgkmcnt(15)\n" \
  "v_mfma_f32_16x16x32_bf16 v[100:103], v[40:43], v[36:39], 0\n" \
  "ds_read_b128 v[84:87], %[x] offset:16384\n" \
  "ds_read_b64_tr_b16 v[88:89], %[w] offset:19200\n" \
  "v_mfma_f32_16x16x32_bf16 v[104:107], v[44:47], v[36:39], 0\n" \
  "ds_read_b64_tr_b16 v[90:91], %[w] offset:20224\n" \
  "ds_read_b64_tr_b16 v[92:93], %[e] offset:18432\n" \
  "ds_read_b64_tr_b16 v[94:95], %[e] offset:18496\n" \
  "s_waitcnt lgkmcnt(15)\n" \
  "v_mfma_f32_16x16x32_bf16 v[100:103], v[52:55], v[48:51], v[100:103]\n" \
  "ds_read_b128 v[36:39], %[y] offset:16384\n" \
  "ds_read_b64_tr_b16 v[40:41], %[w] offset:23808\n" \
  "v_mfma_f32_16x16x32_bf16 v[104:107], v[56:59], v[48:51], v[104:107]\n" \
  "ds_read_b64_tr_b16 v[42:43], %[w] offset:24832\n" \
  "ds_read_b64_tr_b16 v[44:45], %[e] offset:23040\n" \
  "ds_read_b64_tr_b16 v[46:47], %[e] offset:23104\n" \
  "s_waitcnt lgkmcnt(15)\n" \
  "v_mfma_f32_16x16x32_bf16 v[100:103], v[64:67], v[60:63], v[100:103]\n" \
  "v_mfma_f32_16x16x32_bf16 v[104:107], v[68:71], v[60:63], v[104:107]\n" \
  "s_waitcnt lgkmcnt(10)\n" \
  "v_mfma_f32_16x16x32_bf16 v[100:103], v[76:79], v[72:75], v[100:103]\n" \
  "v_mfma_f32_16x16x32_bf16 v[104:107], v[80:83], v[72:75], v[104:107]\n" \
  "s_waitcnt lgkmcnt(5)\n" \
  "v_mfma_f32_16x16x32_bf16 v[100:103], v[88:91], v[84:87], v[100:103]\n" \
  "v_mfma_f32_16x16x32_bf16 v[104:107], v[92:95], v[84:87], v[104:107]\n" \
  "s_waitcnt lgkmcnt(0)\n" \
  "v_mfma_f32_16x16x32_bf16 v[100:103], v[40:43], v[36:39], v[100:103]\n" \
  "v_mfma_f32_16x16x32_bf16 v[104:107], v[44:47], v[36:39], v[104:107]\n" \
  "s_waitcnt lgkmcnt(0)\n" \
  "s_mov_b64 exec, 1\n" \
  "ds_add_u32 %[ctl], %[one] offset:64\n" \
  "s_mov_b64 exec, -1\n" \
  "s_branch 59f\n" \
  "58:\n" \
  "s_sleep 1\n" \
  "59:\n" \
  "ds_read_b32 v35, %[ctl] offset:40\n" \
  "s_waitcnt lgkmcnt(0)\n" \
  "v_readfirstlane_b32 %[st], v35\n" \
  "s_nop 3\n" \
  "s_cmp_ge_u32 %[st], 2\n" \
  "s_cbranch_scc0 58b\n" \
  "s_waitcnt vmcnt(13)\n" \
  "s_mov_b64 exec, %[wmask]\n" \
  "ds_write_b128 %[wd], v[112:115] offset:27648\n" \
  "s_mov_b64 exec, -1\n" \
  "s_waitcnt vmcnt(12)\n" \
  "s_mov_b64 exec, %[wmask]\n" \
  "ds_write_b128 %[wd], v[116:119] offset:32256\n" \
  "s_mov_b64 exec, -1\n" \
  "s_waitcnt vmcnt(11)\n" \
  "s_mov_b64 exec, %[wmask]\n" \
  "ds_write_b128 %[wd], v[120:123] offset:36864\n" \
  "s_mov_b64 exec, -1\n" \
  "s_waitcnt vmcnt(10)\n" \
  "s_mov_b64 exec, %[wmask]\n" \
  "ds_write_b128 %[wd], v[124:127] offset:41472\n" \
  "s_mov_b64 exec, -1\n" \
  "s_waitcnt vmcnt(9)\n" \
  "s_mov_b64 exec, %[wmask]\n" \
  "ds_write_b128 %[wd], v[128:131] offset:46080\n" \
  "s_mov_b64 exec, -1\n" \
  "s_waitcnt vmcnt(8)\n" \
  "s_mov_b64 exec, %[wmask]\n" \
  "ds_write_b128 %[wd], v[132:135] offset:50688\n" \
  "s_mov_b64 exec, -1\n" \
  "s_waitcnt vmcnt(7)\n" \
  "s_mov_b64 exec, %[wmask]\n" \
  "ds_write_b128 %[wd], v[136:139] offset:55296\n" \
  "s_mov_b64 exec, -1\n" \
  "s_waitcnt vmcnt(6)\n" \
  "s_mov_b64 exec, %[wmask]\n" \
  "ds_write_b128 %[wd], v[140:143] offset:59904\n" \
  "s_mov_b64 exec, -1\n" \
  "s_waitcnt lgkmcnt(0)\n" \
  "s_mov_b64 exec, 1\n" \
  "ds_add_u32 %[ctl], %[one] offset:56\n" \
  "s_mov_b64 exec, -1\n" \
  "s_waitcnt vmcnt(3)\n" \
  "s_mov_b64 exec, %[wmask]\n" \
  "ds_write_b128 %[wd2], v[144:147] offset:0\n" \
  "s_mov_b64 exec, -1\n" \
  "s_waitcnt vmcnt(2)\n" \
  "s_mov_b64 exec, %[wmask]\n" \
  "ds_write_b128 %[wd2], v[148:151] offset:4608\n" \
  "s_mov_b64 exec, -1\n" \
  "s_waitcnt vmcnt(1)\n" \
  "s_mov_b64 exec, %[wmask]\n" \
  "ds_write_b128 %[wd2], v[152:155] offset:9216\n" \
  "s_mov_b64 exec, -1\n" \
  "s_waitcnt vmcnt(0)\n" \
  "s_mov_b64 exec, %[wmask]\n" \
  "ds_write_b128 %[wd2], v[156:159] offset:13824\n" \
  "s_mov_b64 exec, -1\n" \
  "s_waitcnt lgkmcnt(0)\n" \
  "s_mov_b64 exec, 1\n" \
  "ds_add_u32 %[ctl], %[one] offset:60\n" \
  "s_mov_b64 exec, -1\n" \
  "s_branch 61f\n" \
  "60:\n" \
  "s_sleep 1\n" \
  "61:\n" \
  "ds_read_b32 v35, %[ctl] offset:56\n" \
  "s_waitcnt lgkmcnt(0)\n" \
  "v_readfirstlane_b32 %[st], v35\n" \
  "s_nop 3\n" \
  "s_cmp_ge_u32 %[st], 8\n" \
  "s_cbranch_scc0 60b\n" \
  "ds_read_b128 v[48:51], %[x] offset:24576\n" \
  "ds_read_b64_tr_b16 v[52:53], %[w] offset:28416\n" \
  "ds_read_b64_tr_b16 v[54:55], %[w] offset:29440\n" \
  "ds_read_b64_tr_b16 v[56:57], %[e] offset:27648\n" \
  "ds_read_b64_tr_b16 v[58:59], %[e] offset:27712\n" \
  "ds_read_b128 v[60:63], %[y] offset:24576\n" \
  "ds_read_b64_tr_b16 v[64:65], %[w] offset:33024\n" \
  "ds_read_b64_tr_b16 v[66:67], %[w] offset:34048\n" \
  "ds_read_b64_tr_b16 v[68:69], %[e] offset:32256\n" \
  "ds_read_b64_tr_b16 v[70:71], %[e] offset:32320\n" \
  "ds_read_b128 v[72:75], %[x] offset:32768\n" \
  "ds_read_b64_tr_b16 v[76:77], %[w] offset:37632\n" \
  "ds_read_b64_tr_b16 v[78:79], %[w] offset:38656\n" \
  "ds_read_b64_tr_b16 v[80:81], %[e] offset:36864\n" \
  "ds_read_b64_tr_b16 v[82:83], %[e] offset:36928\n" \
  "ds_read_b128 v[84:87], %[y] offset:32768\n" \
  "ds_read_b64_tr_b16 v[88:89], %[w] offset:42240\n" \
  "ds_read_b64_tr_b16 v[90:91], %[w] offset:43264\n" \
  "ds_read_b64_tr_b16 v[92:93], %[e] offset:41472\n" \
  "ds_read_b64_tr_b16 v[94:95], %[e] offset:41536\n" \
  "s_waitcnt lgkmcnt(15)\n" \
  "v_mfma_f32_16x16x32_bf16 v[100:103], v[52:55], v[48:51], v[100:103]\n" \
  "ds_read_b128 v[36:39], %[x] offset:40960\n" \
  "ds_read_b64_tr_b16 v[40:41], %[w] offset:46848\n" \
  "v_mfma_f32_16x16x32_bf16 v[104:107], v[56:59], v[48:51], v[104:107]\n" \
  "ds_read_b64_tr_b16 v[42:43], %[w] offset:47872\n" \
  "ds_read_b64_tr_b16 v[44:45], %[e] offset:46080\n" \
  "ds_read_b64_tr_b16 v[46:47], %[e] offset:46144\n" \
  "s_waitcnt lgkmcnt(15)\n" \
  "v_mfma_f32_16x16x32_bf16 v[100:103], v[64:67], v[60:63], v[100:103]\n" \
  "ds_read_b128 v[48:51], %[y] offset:40960\n" \
  "ds_read_b64_tr_b16 v[52:53], %[w] offset:51456\n" \
  "v_mfma_f32_16x16x32_bf16 v[104:107], v[68:71], v[60:63], v[104:107]\n" \
  "ds_read_b64_tr_b16 v[54:55], %[w] offset:52480\n" \
  "ds_read_b64_tr_b16 v[56:57], %[e] offset:50688\n" \
  "ds_read_b64_tr_b16 v[58:59], %[e] offset:50752\n" \
  "s_waitcnt lgkmcnt(15)\n" \
  "v_mfma_f32_16x16x32_bf16 v[100:103], v[76:79], v[72:75], v[100:103]\n" \
  "ds_read_b128 v[60:63], %[x] offset:49152\n" \
  "ds_read_b64_tr_b16 v[64:65], %[w] offset:56064\n" \
  "v_mfma_f32_16x16x32_bf16 v[104:107], v[80:83], v[72:75], v[104:107]\n" \
  "ds_read_b64_tr_b16 v[66:67], %[w] offset:57088\n" \
  "ds_read_b64_tr_b16 v[68:69], %[e] offset:55296\n" \
  "ds_read_b64_tr_b16 v[70:71], %[e] offset:55360\n" \
  "s_waitcnt lgkmcnt(15)\n" \
  "v_mfma_f32_16x16x32_bf16 v[100:103], v[88:91], v[84:87], v[100:103]\n" \
  "ds_read_b128 v[72:75], %[y] offset:49152\n" \
  "ds_read_b64_tr_b16 v[76:77], %[w] offset:60672\n" \
  "v_mfma_f32_16x16x32_bf16 v[104:107], v[92:95], v[84:87], v[104:107]\n" \
  "ds_read_b64_tr_b16 v[78:79], %[w] offset:61696\n" \
  "ds_read_b64_tr_b16 v[80:81], %[e] offset:59904\n" \
  "ds_read_b64_tr_b16 v[82:83], %[e] offset:59968\n" \
  "s_waitcnt lgkmcnt(15)\n" \
  "v_mfma_f32_16x16x32_bf16 v[100:103], v[40:43], v[36:39], v[100:103]\n" \
  "v_mfma_f32_16x16x32_bf16 v[104:107], v[44:47], v[36:39], v[104:107]\n" \
  "s_waitcnt lgkmcnt(10)\n" \
  "v_mfma_f32_16x16x32_bf16 v[100:103], v[52:55], v[48:51], v[100:103]\n" \
  "v_mfma_f32_16x16x32_bf16 v[104:107], v[56:59], v[48:51], v[104:107]\n" \
  "s_waitcnt lgkmcnt(5)\n" \
  "v_mfma_f32_16x16x32_bf16 v[100:103], v[64:67], v[60:63], v[100:103]\n" \
  "v_mfma_f32_16x16x32_bf16 v[104:107], v[68:71], v[60:63], v[104:107]\n" \
  "s_waitcnt lgkmcnt(0)\n" \
  "v_mfma_f32_16x16x32_bf16 v[100:103], v[76:79], v[72:75], v[100:103]\n" \
  "v_mfma_f32_16x16x32_bf16 v[104:107], v[80:83], v[72:75], v[104:107]\n" \
  "v_add_u32 %[x], 0xe000, %[x]\n" \
  "v_add_u32 %[y], 0xe000, %[y]\n" \
  "v_mov_b32 v144, %[vone]\n" \
  "v_mov_b32 v145, %[vone]\n" \
  "v_mov_b32 v146, %[vone]\n" \
  "v_mov_b32 v147, %[vone]\n" \
  "v_mov_b32 v148, %[vone]\n" \
  "v_mov_b32 v149, %[vone]\n" \
  "v_mov_b32 v150, %[vone]\n" \
  "v_mov_b32 v151, %[vone]\n" \
  "v_mov_b32 v152, %[vone]\n" \
  "v_mov_b32 v153, %[vone]\n" \
  "v_mov_b32 v154, %[vone]\n" \
  "v_mov_b32 v155, %[vone]\n" \
  "v_mov_b32 v156, %[vone]\n" \
  "v_mov_b32 v157, %[vone]\n" \
  "v_mov_b32 v158, %[vone]\n" \
  "v_mov_b32 v159, %[vone]\n" \
  "v_mov_b32 v112, 0\n" \
  "v_mov_b32 v113, 0\n" \
  "v_mov_b32 v114, 0\n" \
  "v_mov_b32 v115, 0\n" \
  "v_mov_b32 v116, 0\n" \
  "v_mov_b32 v117, 0\n" \
  "v_mov_b32 v118, 0\n" \
  "v_mov_b32 v119, 0\n" \
  "v_mov_b32 v120, 0\n" \
  "v_mov_b32 v121, 0\n" \
  "v_mov_b32 v122, 0\n" \
  "v_mov_b32 v123, 0\n" \
  "v_mov_b32 v124, 0\n" \
  "v_mov_b32 v125, 0\n" \
  "v_mov_b32 v126, 0\n" \
  "v_mov_b32 v127, 0\n" \
  "v_mov_b32 v128, 0\n" \
  "v_mov_b32 v129, 0\n" \
  "v_mov_b32 v130, 0\n" \
  "v_mov_b32 v131, 0\n" \
  "v_mov_b32 v132, 0\n" \
  "v_mov_b32 v133, 0\n" \
  "v_mov_b32 v134, 0\n" \
  "v_mov_b32 v135, 0\n" \
  "v_mov_b32 v136, 0\n" \
  "v_mov_b32 v137, 0\n" \
  "v_mov_b32 v138, 0\n" \
  "v_mov_b32 v139, 0\n" \
  "v_mov_b32 v140, 0\n" \
  "v_mov_b32 v141, 0\n" \
  "v_mov_b32 v142, 0\n" \
  "v_mov_b32 v143, 0\n" \
  "s_branch 63f\n" \
  "62:\n" \
  "s_sleep 1\n" \
  "63:\n" \
  "ds_read_b32 v35, %[ctl] offset:60\n" \
  "s_waitcnt lgkmcnt(0)\n" \
  "v_readfirstlane_b32 %[st], v35\n" \
  "s_nop 3\n" \
  "s_cmp_ge_u32 %[st], 8\n" \
  "s_cbranch_scc0 62b\n" \
  "ds_read_b128 v[84:87], %[x] offset:0\n" \
  "ds_read_b64_tr_b16 v[88:89], %[u] offset:768\n" \
  "ds_read_b64_tr_b16 v[90:91], %[u] offset:1792\n" \
  "ds_read_b64_tr_b16 v[92:93], %[f] offset:0\n" \
  "ds_read_b64_tr_b16 v[94:95], %[f] offset:64\n" \
  "ds_read_b128 v[36:39], %[y] offset:0\n" \
  "ds_read_b64_tr_b16 v[40:41], %[u] offset:5376\n" \
  "ds_read_b64_tr_b16 v[42:43], %[u] offset:6400\n" \
  "ds_read_b64_tr_b16 v[44:45], %[f] offset:4608\n" \
  "ds_read_b64_tr_b16 v[46:47], %[f] offset:4672\n" \
  "ds_read_b128 v[48:51], %[x] offset:8192\n" \
  "ds_read_b64_tr_b16 v[52:53], %[u] offset:9984\n" \
  "ds_read_b64_tr_b16 v[54:55], %[u] offset:11008\n" \
  "ds_read_b64_tr_b16 v[56:57], %[f] offset:9216\n" \
  "ds_read_b64_tr_b16 v[58:59], %[f] offset:9280\n" \
  "ds_read_b128 v[60:63], %[y] offset:8192\n" \
  "ds_read_b64_tr_b16 v[64:65], %[u] offset:14592\n" \
  "ds_read_b64_tr_b16 v[66:67], %[u] offset:15616\n" \
  "ds_read_b64_tr_b16 v[68:69], %[f] offset:13824\n" \
  "ds_read_b64_tr_b16 v[70:71], %[f] offset:13888\n" \
  "s_waitcnt lgkmcnt(15)\n" \
  "v_mfma_f32_16x16x32_bf16 v[100:103], v[88:91], v[84:87], v[100:103]\n" \
  "v_mfma_f32_16x16x32_bf16 v[104:107], v[92:95], v[84:87], v[104:107]\n" \
  "s_waitcnt lgkmcnt(10)\n" \
  "v_mfma_f32_16x16x32_bf16 v[100:103], v[40:43], v[36:39], v[100:103]\n" \
  "v_mfma_f32_16x16x32_bf16 v[104:107], v[44:47], v[36:39], v[104:107]\n" \
  "s_waitcnt lgkmcnt(5)\n" \
  "v_mfma_f32_16x16x32_bf16 v[100:103], v[52:55], v[48:51], v[100:103]\n" \
  "v_mfma_f32_16x16x32_bf16 v[104:107], v[56:59], v[48:51], v[104:107]\n" \
  "s_waitcnt lgkmcnt(0)\n" \
  "v_mfma_f32_16x16x32_bf16 v[100:103], v[64:67], v[60:63], v[100:103]\n" \
  "v_mfma_f32_16x16x32_bf16 v[104:107], v[68:71], v[60:63], v[104:107]\n" \
  "s_nop 7\n" \
  "v_mul_f32 v100, 0x3eae1a92, v100\n" \
  "v_mul_f32 v101, 0x3eae1a92, v101\n" \
  "v_mul_f32 v102, 0x3eae1a92, v102\n" \
  "v_mul_f32 v103, 0x3eae1a92, v103\n" \
  "v_cvt_pk_bf16_f32 v100, v100, v101\n" \
  "v_cvt_pk_bf16_f32 v101, v102, v103\n" \
  "ds_write_b64 %[qt], v[100:101] offset:0\n" \
  "v_mul_f32 v104, 0x3eae1a92, v104\n" \
  "v_mul_f32 v105, 0x3eae1a92, v105\n" \
  "v_mul_f32 v106, 0x3eae1a92, v106\n" \
  "v_mul_f32 v107, 0x3eae1a92, v107\n" \
  "v_cvt_pk_bf16_f32 v104, v104, v105\n" \
  "v_cvt_pk_bf16_f32 v105, v106, v107\n" \
  "s_mov_b32 exec_hi, 0\n" \
  "ds_write_b64 %[qt], v[104:105] offset:32\n" \
  "s_mov_b32 exec_hi, -1\n" \
  "s_waitcnt lgkmcnt(0)\n" \
  "s_mov_b64 exec, 1\n" \
  "ds_add_u32 %[ctl], %[one] offset:68\n" \
  "s_mov_b64 exec, -1\n"
#define QPROJ_CLOB "v35","v36","v37","v38","v39","v64","v65","v66","v67","v68","v69","v70","v71","v72","v73","v74","v75","v76","v77","v78","v79","v80","v81","v82","v83","v84","v85","v86","v87","v88","v89","v90","v91","v92","v93","v94","v95","v96","v97","v98","v99","v100","v101","v102","v103","v104","v105","v106","v107","v108","v109","v110","v111"
#define OPRE0 \
  "ds_read_b64_tr_b16 v[60:61], %[w] offset:0\n" \
  "ds_read_b64_tr_b16 v[62:63], %[w] offset:1024\n" \
  "ds_read_b64_tr_b16 v[64:65], %[w] offset:256\n" \
  "ds_read_b64_tr_b16 v[66:67], %[w] offset:1280\n" \
  "ds_read_b64_tr_b16 v[68:69], %[w] offset:512\n" \
  "ds_read_b64_tr_b16 v[70:71], %[w] offset:1536\n" \
  "ds_read_b64_tr_b16 v[72:73], %[w] offset:4096\n" \
  "ds_read_b64_tr_b16 v[74:75], %[w] offset:5120\n" \
  "ds_read_b64_tr_b16 v[76:77], %[w] offset:4352\n" \
  "ds_read_b64_tr_b16 v[78:79], %[w] offset:5376\n" \
  "ds_read_b64_tr_b16 v[80:81], %[w] offset:4608\n" \
  "ds_read_b64_tr_b16 v[82:83], %[w] offset:5632\n" \
  "ds_read_b64_tr_b16 v[84:85], %[w] offset:8192\n" \
  "ds_read_b64_tr_b16 v[86:87], %[w] offset:9216\n" \
  "ds_read_b64_tr_b16 v[88:89], %[w] offset:8448\n" \
  "ds_read_b64_tr_b16 v[90:91], %[w] offset:9472\n" \
  "ds_read_b64_tr_b16 v[92:93], %[w] offset:8704\n" \
  "ds_read_b64_tr_b16 v[94:95], %[w] offset:9728\n" \
  "ds_read_b64_tr_b16 v[96:97], %[w] offset:12288\n" \
  "ds_read_b64_tr_b16 v[98:99], %[w] offset:13312\n" \
  "ds_read_b64_tr_b16 v[100:101], %[w] offset:12544\n" \
  "ds_read_b64_tr_b16 v[102:103], %[w] offset:13568\n" \
  "ds_read_b64_tr_b16 v[104:105], %[w] offset:12800\n" \
  "ds_read_b64_tr_b16 v[106:107], %[w] offset:13824\n" \
  "ds_read_b64_tr_b16 v[108:109], %[w] offset:16384\n" \
  "ds_read_b64_tr_b16 v[110:111], %[w] offset:17408\n" \
  "ds_read_b64_tr_b16 v[112:113], %[w] offset:16640\n" \
  "ds_read_b64_tr_b16 v[114:115], %[w] offset:17664\n" \
  "ds_read_b64_tr_b16 v[116:117], %[w] offset:16896\n" \
  "ds_read_b64_tr_b16 v[118:119], %[w] offset:17920\n" \
  "ds_read_b64_tr_b16 v[120:121], %[w] offset:20480\n" \
  "ds_read_b64_tr_b16 v[122:123], %[w] offset:21504\n" \
  "ds_read_b64_tr_b16 v[124:125], %[w] offset:20736\n" \
  "ds_read_b64_tr_b16 v[126:127], %[w] offset:21760\n" \
  "ds_read_b64_tr_b16 v[128:129], %[w] offset:20992\n" \
  "ds_read_b64_tr_b16 v[130:131], %[w] offset:22016\n" \
  "ds_read_b64_tr_b16 v[132:133], %[u] offset:0\n" \
  "ds_read_b64_tr_b16 v[134:135], %[u] offset:1024\n" \
  "ds_read_b64_tr_b16 v[136:137], %[u] offset:256\n" \
  "ds_read_b64_tr_b16 v[138:139], %[u] offset:1280\n" \
  "ds_read_b64_tr_b16 v[140:141], %[u] offset:512\n" \
  "ds_read_b64_tr_b16 v[142:143], %[u] offset:1536\n" \
  "ds_read_b64_tr_b16 v[144:145], %[u] offset:4096\n" \
  "ds_read_b64_tr_b16 v[146:147], %[u] offset:5120\n" \
  "ds_read_b64_tr_b16 v[148:149], %[u] offset:4352\n" \
  "ds_read_b64_tr_b16 v[150:151], %[u] offset:5376\n" \
  "ds_read_b64_tr_b16 v[152:153], %[u] offset:4608\n" \
  "ds_read_b64_tr_b16 v[154:155], %[u] offset:5632\n" \
  "ds_read_b64_tr_b16 v[156:157], %[u] offset:8192\n" \
  "ds_read_b64_tr_b16 v[158:159], %[u] offset:9216\n" \
  "ds_read_b64_tr_b16 v[160:161], %[u] offset:8448\n" \
  "ds_read_b64_tr_b16 v[162:163], %[u] offset:9472\n" \
  "ds_read_b64_tr_b16 v[164:165], %[u] offset:8704\n" \
  "ds_read_b64_tr_b16 v[166:167], %[u] offset:9728\n"
#define OGEMM0 \
  "ds_read_b128 v[24:27], %[x] offset:0\n" \
  "ds_read_b128 v[28:31], %[x] offset:1024\n" \
  "ds_read_b128 v[32:35], %[x] offset:2048\n" \
  "ds_read_b128 v[36:39], %[x] offset:3072\n" \
  "ds_read_b128 v[40:43], %[x] offset:4096\n" \
  "s_waitcnt lgkmcnt(3)\n" \
  "v_mfma_f32_16x16x32_bf16 v[48:51], v[60:63], v[24:27], 0\n" \
  "ds_read_b128 v[44:47], %[x] offset:5120\n" \
  "v_mfma_f32_16x16x32_bf16 v[52:55], v[64:67], v[24:27], 0\n" \
  "v_mfma_f32_16x16x32_bf16 v[56:59], v[68:71], v[24:27], 0\n" \
  "v_mfma_f32_16x16x32_bf16 v[48:51], v[72:75], v[28:31], v[48:51]\n" \
  "ds_read_b128 v[24:27], %[x] offset:6144\n" \
  "ds_read_b64_tr_b16 v[60:61], %[u] offset:12288\n" \
  "v_mfma_f32_16x16x32_bf16 v[52:55], v[76:79], v[28:31], v[52:55]\n" \
  "ds_read_b64_tr_b16 v[62:63], %[u] offset:13312\n" \
  "ds_read_b64_tr_b16 v[64:65], %[u] offset:12544\n" \
  "v_mfma_f32_16x16x32_bf16 v[56:59], v[80:83], v[28:31], v[56:59]\n" \
  "ds_read_b64_tr_b16 v[66:67], %[u] offset:13568\n" \
  "s_waitcnt lgkmcnt(7)\n" \
  "v_mfma_f32_16x16x32_bf16 v[48:51], v[84:87], v[32:35], v[48:51]\n" \
  "ds_read_b128 v[28:31], %[x] offset:7168\n" \
  "ds_read_b64_tr_b16 v[68:69], %[u] offset:12800\n" \
  "v_mfma_f32_16x16x32_bf16 v[52:55], v[88:91], v[32:35], v[52:55]\n" \
  "ds_read_b64_tr_b16 v[70:71], %[u] offset:13824\n" \
  "ds_read_b64_tr_b16 v[72:73], %[u] offset:16384\n" \
  "v_mfma_f32_16x16x32_bf16 v[56:59], v[92:95], v[32:35], v[56:59]\n" \
  "ds_read_b64_tr_b16 v[74:75], %[u] offset:17408\n" \
  "ds_read_b64_tr_b16 v[76:77], %[u] offset:16640\n" \
  "v_mfma_f32_16x16x32_bf16 v[48:51], v[96:99], v[36:39], v[48:51]\n" \
  "ds_read_b128 v[32:35], %[x] offset:8192\n" \
  "ds_read_b64_tr_b16 v[78:79], %[u] offset:17664\n" \
  "v_mfma_f32_16x16x32_bf16 v[52:55], v[100:103], v[36:39], v[52:55]\n" \
  "ds_read_b64_tr_b16 v[80:81], %[u] offset:16896\n" \
  "ds_read_b64_tr_b16 v[82:83], %[u] offset:17920\n" \
  "v_mfma_f32_16x16x32_bf16 v[56:59], v[104:107], v[36:39], v[56:59]\n" \
  "ds_read_b64_tr_b16 v[84:85], %[u] offset:20480\n" \
  "s_waitcnt lgkmcnt(15)\n" \
  "v_mfma_f32_16x16x32_bf16 v[48:51], v[108:111], v[40:43], v[48:51]\n" \
  "ds_read_b128 v[36:39], %[y] offset:0\n" \
  "ds_read_b64_tr_b16 v[86:87], %[u] offset:21504\n" \
  "v_mfma_f32_16x16x32_bf16 v[52:55], v[112:115], v[40:43], v[52:55]\n" \
  "ds_read_b64_tr_b16 v[88:89], %[u] offset:20736\n" \
  "ds_read_b64_tr_b16 v[90:91], %[u] offset:21760\n" \
  "v_mfma_f32_16x16x32_bf16 v[56:59], v[116:119], v[40:43], v[56:59]\n" \
  "ds_read_b64_tr_b16 v[92:93], %[u] offset:20992\n" \
  "ds_read_b64_tr_b16 v[94:95], %[u] offset:22016\n" \
  "v_mfma_f32_16x16x32_bf16 v[48:51], v[120:123], v[44:47], v[48:51]\n" \
  "ds_read_b128 v[40:43], %[y] offset:1024\n" \
  "ds_read_b64_tr_b16 v[96:97], %[u] offset:24576\n" \
  "v_mfma_f32_16x16x32_bf16 v[52:55], v[124:127], v[44:47], v[52:55]\n" \
  "ds_read_b64_tr_b16 v[98:99], %[u] offset:25600\n" \
  "ds_read_b64_tr_b16 v[100:101], %[u] offset:24832\n" \
  "v_mfma_f32_16x16x32_bf16 v[56:59], v[128:131], v[44:47], v[56:59]\n" \
  "ds_read_b64_tr_b16 v[102:103], %[u] offset:25856\n" \
  "s_waitcnt lgkmcnt(15)\n" \
  "v_mfma_f32_16x16x32_bf16 v[48:51], v[132:135], v[24:27], v[48:51]\n" \
  "ds_read_b128 v[44:47], %[y] offset:2048\n" \
  "ds_read_b64_tr_b16 v[104:105], %[u] offset:25088\n" \
  "v_mfma_f32_16x16x32_bf16 v[52:55], v[136:139], v[24:27], v[52:55]\n" \
  "ds_read_b64_tr_b16 v[106:107], %[u] offset:26112\n" \
  "ds_read_b64_tr_b16 v[108:109], %[u] offset:28672\n" \
  "v_mfma_f32_16x16x32_bf16 v[56:59], v[140:143], v[24:27], v[56:59]\n" \
  "ds_read_b64_tr_b16 v[110:111], %[u] offset:29696\n" \
  "ds_read_b64_tr_b16 v[112:113], %[u] offset:28928\n" \
  "v_mfma_f32_16x16x32_bf16 v[48:51], v[144:147], v[28:31], v[48:51]\n" \
  "ds_read_b128 v[24:27], %[y] offset:3072\n" \
  "ds_read_b64_tr_b16 v[114:115], %[u] offset:29952\n" \
  "v_mfma_f32_16x16x32_bf16 v[52:55], v[148:151], v[28:31], v[52:55]\n" \
  "ds_read_b64_tr_b16 v[116:117], %[u] offset:29184\n" \
  "ds_read_b64_tr_b16 v[118:119], %[u] offset:30208\n" \
  "v_mfma_f32_16x16x32_bf16 v[56:59], v[152:155], v[28:31], v[56:59]\n" \
  "ds_read_b64_tr_b16 v[120:121], %[u] offset:32768\n" \
  "s_waitcnt lgkmcnt(15)\n" \
  "v_mfma_f32_16x16x32_bf16 v[48:51], v[156:159], v[32:35], v[48:51]\n" \
  "ds_read_b128 v[28:31], %[y] offset:4096\n" \
  "ds_read_b64_tr_b16 v[122:123], %[u] offset:33792\n" \
  "v_mfma_f32_16x16x32_bf16 v[52:55], v[160:163], v[32:35], v[52:55]\n" \
  "ds_read_b64_tr_b16 v[124:125], %[u] offset:33024\n" \
  "ds_read_b64_tr_b16 v[126:127], %[u] offset:34048\n" \
  "v_mfma_f32_16x16x32_bf16 v[56:59], v[164:167], v[32:35], v[56:59]\n" \
  "ds_read_b64_tr_b16 v[128:129], %[u] offset:33280\n" \
  "ds_read_b64_tr_b16 v[130:131], %[u] offset:34304\n" \
  "v_mfma_f32_16x16x32_bf16 v[48:51], v[60:63], v[36:39], v[48:51]\n" \
  "ds_read_b128 v[32:35], %[y] offset:5120\n" \
  "ds_read_b64_tr_b16 v[132:133], %[u] offset:36864\n" \
  "v_mfma_f32_16x16x32_bf16 v[52:55], v[64:67], v[36:39], v[52:55]\n" \
  "ds_read_b64_tr_b16 v[134:135], %[u] offset:37888\n" \
  "ds_read_b64_tr_b16 v[136:137], %[u] offset:37120\n" \
  "v_mfma_f32_16x16x32_bf16 v[56:59], v[68:71], v[36:39], v[56:59]\n" \
  "ds_read_b64_tr_b16 v[138:139], %[u] offset:38144\n" \
  "s_waitcnt lgkmcnt(15)\n" \
  "v_mfma_f32_16x16x32_bf16 v[48:51], v[72:75], v[40:43], v[48:51]\n" \
  "ds_read_b128 v[36:39], %[y] offset:6144\n" \
  "ds_read_b64_tr_b16 v[140:141], %[u] offset:37376\n" \
  "v_mfma_f32_16x16x32_bf16 v[52:55], v[76:79], v[40:43], v[52:55]\n" \
  "ds_read_b64_tr_b16 v[142:143], %[u] offset:38400\n" \
  "ds_read_b64_tr_b16 v[144:145], %[u] offset:40960\n" \
  "v_mfma_f32_16x16x32_bf16 v[56:59], v[80:83], v[40:43], v[56:59]\n" \
  "ds_read_b64_tr_b16 v[146:147], %[u] offset:41984\n" \
  "ds_read_b64_tr_b16 v[148:149], %[u] offset:41216\n" \
  "v_mfma_f32_16x16x32_bf16 v[48:51], v[84:87], v[44:47], v[48:51]\n" \
  "ds_read_b128 v[40:43], %[y] offset:7168\n" \
  "ds_read_b64_tr_b16 v[150:151], %[u] offset:42240\n" \
  "v_mfma_f32_16x16x32_bf16 v[52:55], v[88:91], v[44:47], v[52:55]\n" \
  "ds_read_b64_tr_b16 v[152:153], %[u] offset:41472\n" \
  "ds_read_b64_tr_b16 v[154:155], %[u] offset:42496\n" \
  "v_mfma_f32_16x16x32_bf16 v[56:59], v[92:95], v[44:47], v[56:59]\n" \
  "ds_read_b64_tr_b16 v[156:157], %[u] offset:45056\n" \
  "s_waitcnt lgkmcnt(15)\n" \
  "v_mfma_f32_16x16x32_bf16 v[48:51], v[96:99], v[24:27], v[48:51]\n" \
  "ds_read_b128 v[44:47], %[y] offset:8192\n" \
  "ds_read_b64_tr_b16 v[158:159], %[u] offset:46080\n" \
  "v_mfma_f32_16x16x32_bf16 v[52:55], v[100:103], v[24:27], v[52:55]\n" \
  "ds_read_b64_tr_b16 v[160:161], %[u] offset:45312\n" \
  "ds_read_b64_tr_b16 v[162:163], %[u] offset:46336\n" \
  "v_mfma_f32_16x16x32_bf16 v[56:59], v[104:107], v[24:27], v[56:59]\n" \
  "ds_read_b64_tr_b16 v[164:165], %[u] offset:45568\n" \
  "ds_read_b64_tr_b16 v[166:167], %[u] offset:46592\n" \
  "v_mfma_f32_16x16x32_bf16 v[48:51], v[108:111], v[28:31], v[48:51]\n" \
  "v_mfma_f32_16x16x32_bf16 v[52:55], v[112:115], v[28:31], v[52:55]\n" \
  "v_mfma_f32_16x16x32_bf16 v[56:59], v[116:119], v[28:31], v[56:59]\n" \
  "s_waitcnt lgkmcnt(14)\n" \
  "v_mfma_f32_16x16x32_bf16 v[48:51], v[120:123], v[32:35], v[48:51]\n" \
  "v_mfma_f32_16x16x32_bf16 v[52:55], v[124:127], v[32:35], v[52:55]\n" \
  "v_mfma_f32_16x16x32_bf16 v[56:59], v[128:131], v[32:35], v[56:59]\n" \
  "v_mfma_f32_16x16x32_bf16 v[48:51], v[132:135], v[36:39], v[48:51]\n" \
  "v_mfma_f32_16x16x32_bf16 v[52:55], v[136:139], v[36:39], v[52:55]\n" \
  "v_mfma_f32_16x16x32_bf16 v[56:59], v[140:143], v[36:39], v[56:59]\n" \
  "s_waitcnt lgkmcnt(0)\n" \
  "v_mfma_f32_16x16x32_bf16 v[48:51], v[144:147], v[40:43], v[48:51]\n" \
  "v_mfma_f32_16x16x32_bf16 v[52:55], v[148:151], v[40:43], v[52:55]\n" \
  "v_mfma_f32_16x16x32_bf16 v[56:59], v[152:155], v[40:43], v[56:59]\n" \
  "v_mfma_f32_16x16x32_bf16 v[48:51], v[156:159], v[44:47], v[48:51]\n" \
  "v_mfma_f32_16x16x32_bf16 v[52:55], v[160:163], v[44:47], v[52:55]\n" \
  "v_mfma_f32_16x16x32_bf16 v[56:59], v[164:167], v[44:47], v[56:59]\n" \
  "s_nop 7\n"
#define OPRE1 \
  "ds_read_b64_tr_b16 v[60:61], %[w] offset:768\n" \
  "ds_read_b64_tr_b16 v[62:63], %[w] offset:1792\n" \
  "ds_read_b64_tr_b16 v[64:65], %[e] offset:0\n" \
  "ds_read_b64_tr_b16 v[66:67], %[e] offset:64\n" \
  "ds_read_b64_tr_b16 v[68:69], %[w] offset:4864\n" \
  "ds_read_b64_tr_b16 v[70:71], %[w] offset:5888\n" \
  "ds_read_b64_tr_b16 v[72:73], %[e] offset:512\n" \
  "ds_read_b64_tr_b16 v[74:75], %[e] offset:576\n" \
  "ds_read_b64_tr_b16 v[76:77], %[w] offset:8960\n" \
  "ds_read_b64_tr_b16 v[78:79], %[w] offset:9984\n" \
  "ds_read_b64_tr_b16 v[80:81], %[e] offset:1024\n" \
  "ds_read_b64_tr_b16 v[82:83], %[e] offset:1088\n" \
  "ds_read_b64_tr_b16 v[84:85], %[w] offset:13056\n" \
  "ds_read_b64_tr_b16 v[86:87], %[w] offset:14080\n" \
  "ds_read_b64_tr_b16 v[88:89], %[e] offset:1536\n" \
  "ds_read_b64_tr_b16 v[90:91], %[e] offset:1600\n" \
  "ds_read_b64_tr_b16 v[92:93], %[w] offset:17152\n" \
  "ds_read_b64_tr_b16 v[94:95], %[w] offset:18176\n" \
  "ds_read_b64_tr_b16 v[96:97], %[e] offset:2048\n" \
  "ds_read_b64_tr_b16 v[98:99], %[e] offset:2112\n" \
  "ds_read_b64_tr_b16 v[100:101], %[w] offset:21248\n" \
  "ds_read_b64_tr_b16 v[102:103], %[w] offset:22272\n" \
  "ds_read_b64_tr_b16 v[104:105], %[e] offset:2560\n" \
  "ds_read_b64_tr_b16 v[106:107], %[e] offset:2624\n" \
  "ds_read_b64_tr_b16 v[108:109], %[u] offset:768\n" \
  "ds_read_b64_tr_b16 v[110:111], %[u] offset:1792\n" \
  "ds_read_b64_tr_b16 v[112:113], %[f] offset:0\n" \
  "ds_read_b64_tr_b16 v[114:115], %[f] offset:64\n" \
  "ds_read_b64_tr_b16 v[116:117], %[u] offset:4864\n" \
  "ds_read_b64_tr_b16 v[118:119], %[u] offset:5888\n" \
  "ds_read_b64_tr_b16 v[120:121], %[f] offset:512\n" \
  "ds_read_b64_tr_b16 v[122:123], %[f] offset:576\n" \
  "ds_read_b64_tr_b16 v[124:125], %[u] offset:8960\n" \
  "ds_read_b64_tr_b16 v[126:127], %[u] offset:9984\n" \
  "ds_read_b64_tr_b16 v[128:129], %[f] offset:1024\n" \
  "ds_read_b64_tr_b16 v[130:131], %[f] offset:1088\n" \
  "ds_read_b64_tr_b16 v[132:133], %[u] offset:13056\n" \
  "ds_read_b64_tr_b16 v[134:135], %[u] offset:14080\n" \
  "ds_read_b64_tr_b16 v[136:137], %[f] offset:1536\n" \
  "ds_read_b64_tr_b16 v[138:139], %[f] offset:1600\n" \
  "ds_read_b64_tr_b16 v[140:141], %[u] offset:17152\n" \
  "ds_read_b64_tr_b16 v[142:143], %[u] offset:18176\n" \
  "ds_read_b64_tr_b16 v[144:145], %[f] offset:2048\n" \
  "ds_read_b64_tr_b16 v[146:147], %[f] offset:2112\n" \
  "ds_read_b64_tr_b16 v[148:149], %[u] offset:21248\n" \
  "ds_read_b64_tr_b16 v[150:151], %[u] offset:22272\n" \
  "ds_read_b64_tr_b16 v[152:153], %[f] offset:2560\n" \
  "ds_read_b64_tr_b16 v[154:155], %[f] offset:2624\n" \
  "ds_read_b64_tr_b16 v[156:157], %[u] offset:25344\n" \
  "ds_read_b64_tr_b16 v[158:159], %[u] offset:26368\n" \
  "ds_read_b64_tr_b16 v[160:161], %[f] offset:3072\n" \
  "ds_read_b64_tr_b16 v[162:163], %[f] offset:3136\n"
#define OGEMM1 \
  "s_setprio 1\n" \
  "ds_read_b128 v[24:27], %[x] offset:0\n" \
  "ds_read_b128 v[28:31], %[x] offset:1024\n" \
  "ds_read_b128 v[32:35], %[x] offset:2048\n" \
  "ds_read_b128 v[36:39], %[x] offset:3072\n" \
  "ds_read_b128 v[40:43], %[x] offset:4096\n" \
  "s_waitcnt lgkmcnt(3)\n" \
  "v_mfma_f32_16x16x32_bf16 v[48:51], v[60:63], v[24:27], 0\n" \
  "ds_read_b128 v[44:47], %[x] offset:5120\n" \
  "v_mfma_f32_16x16x32_bf16 v[52:55], v[64:67], v[24:27], 0\n" \
  "v_mfma_f32_16x16x32_bf16 v[48:51], v[68:71], v[28:31], v[48:51]\n" \
  "ds_read_b128 v[24:27], %[x] offset:6144\n" \
  "ds_read_b64_tr_b16 v[60:61], %[u] offset:29440\n" \
  "v_mfma_f32_16x16x32_bf16 v[52:55], v[72:75], v[28:31], v[52:55]\n" \
  "ds_read_b64_tr_b16 v[62:63], %[u] offset:30464\n" \
  "s_waitcnt lgkmcnt(5)\n" \
  "v_mfma_f32_16x16x32_bf16 v[48:51], v[76:79], v[32:35], v[48:51]\n" \
  "ds_read_b128 v[28:31], %[x] offset:7168\n" \
  "ds_read_b64_tr_b16 v[64:65], %[f] offset:3584\n" \
  "v_mfma_f32_16x16x32_bf16 v[52:55], v[80:83], v[32:35], v[52:55]\n" \
  "ds_read_b64_tr_b16 v[66:67], %[f] offset:3648\n" \
  "ds_read_b64_tr_b16 v[68:69], %[u] offset:33536\n" \
  "v_mfma_f32_16x16x32_bf16 v[48:51], v[84:87], v[36:39], v[48:51]\n" \
  "ds_read_b128 v[32:35], %[x] offset:8192\n" \
  "ds_read_b64_tr_b16 v[70:71], %[u] offset:34560\n" \
  "v_mfma_f32_16x16x32_bf16 v[52:55], v[88:91], v[36:39], v[52:55]\n" \
  "ds_read_b64_tr_b16 v[72:73], %[f] offset:4096\n" \
  "s_waitcnt lgkmcnt(10)\n" \
  "v_mfma_f32_16x16x32_bf16 v[48:51], v[92:95], v[40:43], v[48:51]\n" \
  "ds_read_b128 v[36:39], %[y] offset:0\n" \
  "ds_read_b64_tr_b16 v[74:75], %[f] offset:4160\n" \
  "v_mfma_f32_16x16x32_bf16 v[52:55], v[96:99], v[40:43], v[52:55]\n" \
  "ds_read_b64_tr_b16 v[76:77], %[u] offset:37632\n" \
  "ds_read_b64_tr_b16 v[78:79], %[u] offset:38656\n" \
  "v_mfma_f32_16x16x32_bf16 v[48:51], v[100:103], v[44:47], v[48:51]\n" \
  "ds_read_b128 v[40:43], %[y] offset:1024\n" \
  "ds_read_b64_tr_b16 v[80:81], %[f] offset:4608\n" \
  "v_mfma_f32_16x16x32_bf16 v[52:55], v[104:107], v[44:47], v[52:55]\n" \
  "ds_read_b64_tr_b16 v[82:83], %[f] offset:4672\n" \
  "s_waitcnt lgkmcnt(13)\n" \
  "v_mfma_f32_16x16x32_bf16 v[48:51], v[108:111], v[24:27], v[48:51]\n" \
  "ds_read_b128 v[44:47], %[y] offset:2048\n" \
  "ds_read_b64_tr_b16 v[84:85], %[u] offset:41728\n" \
  "v_mfma_f32_16x16x32_bf16 v[52:55], v[112:115], v[24:27], v[52:55]\n" \
  "ds_read_b64_tr_b16 v[86:87], %[u] offset:42752\n" \
  "ds_read_b64_tr_b16 v[88:89], %[f] offset:5120\n" \
  "v_mfma_f32_16x16x32_bf16 v[48:51], v[116:119], v[28:31], v[48:51]\n" \
  "ds_read_b128 v[24:27], %[y] offset:3072\n" \
  "ds_read_b64_tr_b16 v[90:91], %[f] offset:5184\n" \
  "v_mfma_f32_16x16x32_bf16 v[52:55], v[120:123], v[28:31], v[52:55]\n" \
  "ds_read_b64_tr_b16 v[92:93], %[u] offset:45824\n" \
  "s_waitcnt lgkmcnt(13)\n" \
  "v_mfma_f32_16x16x32_bf16 v[48:51], v[124:127], v[32:35], v[48:51]\n" \
  "ds_read_b128 v[28:31], %[y] offset:4096\n" \
  "ds_read_b64_tr_b16 v[94:95], %[u] offset:46848\n" \
  "v_mfma_f32_16x16x32_bf16 v[52:55], v[128:131], v[32:35], v[52:55]\n" \
  "ds_read_b64_tr_b16 v[96:97], %[f] offset:5632\n" \
  "ds_read_b64_tr_b16 v[98:99], %[f] offset:5696\n" \
  "v_mfma_f32_16x16x32_bf16 v[48:51], v[132:135], v[36:39], v[48:51]\n" \
  "ds_read_b128 v[32:35], %[y] offset:5120\n" \
  "v_mfma_f32_16x16x32_bf16 v[52:55], v[136:139], v[36:39], v[52:55]\n" \
  "s_waitcnt lgkmcnt(11)\n" \
  "v_mfma_f32_16x16x32_bf16 v[48:51], v[140:143], v[40:43], v[48:51]\n" \
  "ds_read_b128 v[36:39], %[y] offset:6144\n" \
  "v_mfma_f32_16x16x32_bf16 v[52:55], v[144:147], v[40:43], v[52:55]\n" \
  "v_mfma_f32_16x16x32_bf16 v[48:51], v[148:151], v[44:47], v[48:51]\n" \
  "ds_read_b128 v[40:43], %[y] offset:7168\n" \
  "v_mfma_f32_16x16x32_bf16 v[52:55], v[152:155], v[44:47], v[52:55]\n" \
  "s_waitcnt lgkmcnt(6)\n" \
  "v_mfma_f32_16x16x32_bf16 v[48:51], v[156:159], v[24:27], v[48:51]\n" \
  "ds_read_b128 v[44:47], %[y] offset:8192\n" \
  "v_mfma_f32_16x16x32_bf16 v[52:55], v[160:163], v[24:27], v[52:55]\n" \
  "v_mfma_f32_16x16x32_bf16 v[48:51], v[60:63], v[28:31], v[48:51]\n" \
  "v_mfma_f32_16x16x32_bf16 v[52:55], v[64:67], v[28:31], v[52:55]\n" \
  "s_waitcnt lgkmcnt(2)\n" \
  "v_mfma_f32_16x16x32_bf16 v[48:51], v[68:71], v[32:35], v[48:51]\n" \
  "v_mfma_f32_16x16x32_bf16 v[52:55], v[72:75], v[32:35], v[52:55]\n" \
  "v_mfma_f32_16x16x32_bf16 v[48:51], v[76:79], v[36:39], v[48:51]\n" \
  "v_mfma_f32_16x16x32_bf16 v[52:55], v[80:83], v[36:39], v[52:55]\n" \
  "s_waitcnt lgkmcnt(0)\n" \
  "v_mfma_f32_16x16x32_bf16 v[48:51], v[84:87], v[40:43], v[48:51]\n" \
  "v_mfma_f32_16x16x32_bf16 v[52:55], v[88:91], v[40:43], v[52:55]\n" \
  "v_mfma_f32_16x16x32_bf16 v[48:51], v[92:95], v[44:47], v[48:51]\n" \
  "v_mfma_f32_16x16x32_bf16 v[52:55], v[96:99], v[44:47], v[52:55]\n" \
  "s_nop 7\n"
#define OFETCH \
  "s_mov_b32 %[cnt], 0x20000\n" \
  "s_sleep %[dly]\n" \
  "s_branch 153f\n" \
  "150:\n" \
  "s_sleep 2\n" \
  "buffer_load_dword v24, %[pv], %[rs], %[so] offen sc1\n" \
  "s_sub_u32 %[cnt], %[cnt], 1\n" \
  "s_waitcnt vmcnt(0)\n" \
  "v_cmp_eq_u32 vcc, -1, v24\n" \
  "s_cbranch_scc1 153f\n" \
  "s_nop 3\n" \
  "s_cbranch_vccnz 150b\n" \
  "153:\n" \
  "buffer_load_dwordx4 v[24:27], %[lv], %[rs], %[so] offen offset:0 sc1\n" \
  "buffer_load_dwordx4 v[28:31], %[lv], %[rs], %[so] offen offset:1024 sc1\n" \
  "buffer_load_dwordx4 v[32:35], %[lv], %[rs], %[so] offen offset:2048 sc1\n" \
  "buffer_load_dwordx4 v[36:39], %[lv], %[rs], %[so] offen offset:3072 sc1\n" \
  "buffer_load_dwordx4 v[40:43], %[lv2], %[rs], %[so] offen offset:0 sc1\n" \
  "buffer_load_dwordx4 v[44:47], %[lv2], %[rs], %[so] offen offset:1024 sc1\n" \
  "buffer_load_dwordx4 v[48:51], %[lv2], %[rs], %[so] offen offset:2048 sc1\n" \
  "buffer_load_dwordx4 v[52:55], %[lv2], %[rs], %[so] offen offset:3072 sc1\n" \
  "buffer_load_dwordx4 v[56:59], %[lv3], %[rs], %[so] offen offset:0 sc1\n" \
  "s_sub_u32 %[cnt], %[cnt], 1\n" \
  "s_waitcnt vmcnt(8)\n" \
  "v_max3_u32 %[t], v24, v25, v26\n" \
  "v_max_u32 %[t], %[t], v27\n" \
  "ds_write_b128 %[da], v[24:27] offset:0\n" \
  "s_waitcnt vmcnt(7)\n" \
  "v_max3_u32 %[t], v28, v29, %[t]\n" \
  "v_max3_u32 %[t], v30, v31, %[t]\n" \
  "ds_write_b128 %[da], v[28:31] offset:256\n" \
  "s_waitcnt vmcnt(6)\n" \
  "v_max3_u32 %[t], v32, v33, %[t]\n" \
  "v_max3_u32 %[t], v34, v35, %[t]\n" \
  "ds_write_b128 %[da], v[32:35] offset:512\n" \
  "s_waitcnt vmcnt(5)\n" \
  "v_max3_u32 %[t], v36, v37, %[t]\n" \
  "v_max3_u32 %[t], v38, v39, %[t]\n" \
  "ds_write_b128 %[da], v[36:39] offset:768\n" \
  "s_waitcnt vmcnt(4)\n" \
  "v_max3_u32 %[t], v40, v41, %[t]\n" \
  "v_max3_u32 %[t], v42, v43, %[t]\n" \
  "ds_write_b128 %[da], v[40:43] offset:1024\n" \
  "s_waitcnt vmcnt(3)\n" \
  "v_max3_u32 %[t], v44, v45, %[t]\n" \
  "v_max3_u32 %[t], v46, v47, %[t]\n" \
  "ds_write_b128 %[da], v[44:47] offset:1280\n" \
  "s_waitcnt vmcnt(2)\n" \
  "v_max3_u32 %[t], v48, v49, %[t]\n" \
  "v_max3_u32 %[t], v50, v51, %[t]\n" \
  "ds_write_b128 %[da], v[48:51] offset:1536\n" \
  "s_waitcnt vmcnt(1)\n" \
  "v_max3_u32 %[t], v52, v53, %[t]\n" \
  "v_max3_u32 %[t], v54, v55, %[t]\n" \
  "ds_write_b128 %[da], v[52:55] offset:1792\n" \
  "s_waitcnt vmcnt(0)\n" \
  "v_max3_u32 %[t], v56, v57, %[t]\n" \
  "v_max3_u32 %[t], v58, v59, %[t]\n" \
  "ds_write_b128 %[da], v[56:59] offset:2048\n" \
  "v_cmp_eq_u32 vcc, -1, %[t]\n" \
  "s_cbranch_scc1 154f\n" \
  "s_nop 3\n" \
  "s_cbranch_vccnz 150b\n" \
  "154:\n" \
  "s_waitcnt lgkmcnt(0)\n" \
  "s_barrier\n"
#define OTAIL_CLOB "v24","v25","v26","v27","v28","v29","v30","v31","v32","v33","v34","v35","v36","v37","v38","v39","v40","v41","v42","v43","v44","v45","v46","v47","v60","v61","v62","v63","v64","v65","v66","v67","v68","v69","v70","v71","v72","v73","v74","v75","v76","v77","v78","v79","v80","v81","v82","v83","v84","v85","v86","v87","v88","v89","v90","v91","v92","v93","v94","v95","v96","v97","v98","v99","v100","v101","v102","v103","v104","v105","v106","v107","v108","v109","v110","v111","v112","v113","v114","v115","v116","v117","v118","v119","v120","v121","v122","v123","v124","v125","v126","v127","v128","v129","v130","v131","v132","v133","v134","v135","v136","v137","v138","v139","v140","v141","v142","v143","v144","v145","v146","v147","v148","v149","v150","v151","v152","v153","v154","v155","v156","v157","v158","v159","v160","v161","v162","v163","v164","v165","v166","v167"
#define KVPROJ \
  "s_waitcnt vmcnt(0)\n" \
  "s_mov_b64 exec, 1\n" \
  "ds_add_u32 %[cl], %[one]\n" \
  "s_mov_b64 exec, -1\n" \
  "s_branch 182f\n" \
  "181:\n" \
  "s_sleep 1\n" \
  "182:\n" \
  "ds_read_b32 v40, %[cl]\n" \
  "s_waitcnt lgkmcnt(0)\n" \
  "v_readfirstlane_b32 %[st], v40\n" \
  "s_nop 3\n" \
  "s_cmp_ge_u32 %[st], 4\n" \
  "s_cbranch_scc0 181b\n" \
  "180:\n" \
  "s_cmp_eq_u32 %[bw], 1\n" \
  "s_cbranch_scc1 175f\n" \
  "s_cmp_eq_u32 %[bw], 2\n" \
  "s_cbranch_scc1 176f\n" \
  "s_cmp_eq_u32 %[bw], 3\n" \
  "s_cbranch_scc1 177f\n" \
  "ds_read_b128 v[40:43], %[x] offset:0\n" \
  "ds_read_b128 v[44:47], %[y] offset:0\n" \
  "ds_read_b64_tr_b16 v[112:113], %[w] offset:0\n" \
  "ds_read_b64_tr_b16 v[114:115], %[w] offset:288\n" \
  "ds_read_b64_tr_b16 v[116:117], %[w] offset:32\n" \
  "ds_read_b64_tr_b16 v[118:119], %[w] offset:320\n" \
  "ds_read_b64_tr_b16 v[120:121], %[w] offset:64\n" \
  "ds_read_b64_tr_b16 v[122:123], %[w] offset:352\n" \
  "ds_read_b128 v[48:51], %[x] offset:1024\n" \
  "ds_read_b128 v[52:55], %[y] offset:1024\n" \
  "ds_read_b64_tr_b16 v[124:125], %[w] offset:2304\n" \
  "ds_read_b64_tr_b16 v[126:127], %[w] offset:2592\n" \
  "ds_read_b64_tr_b16 v[128:129], %[w] offset:2336\n" \
  "ds_read_b64_tr_b16 v[130:131], %[w] offset:2624\n" \
  "ds_read_b64_tr_b16 v[132:133], %[w] offset:2368\n" \
  "ds_read_b64_tr_b16 v[134:135], %[w] offset:2656\n" \
  "s_waitcnt lgkmcnt(8)\n" \
  "v_perm_b32 v40, v41, v40, %[sel]\n" \
  "v_perm_b32 v41, v43, v42, %[sel]\n" \
  "v_perm_b32 v42, v45, v44, %[sel]\n" \
  "v_perm_b32 v43, v47, v46, %[sel]\n" \
  "ds_read_b128 v[56:59], %[x] offset:2048\n" \
  "ds_read_b128 v[60:63], %[y] offset:2048\n" \
  "v_mfma_f32_16x16x32_bf16 v[148:151], v[112:115], v[40:43], 0\n" \
  "ds_read_b64_tr_b16 v[136:137], %[w] offset:4608\n" \
  "ds_read_b64_tr_b16 v[138:139], %[w] offset:4896\n" \
  "v_mfma_f32_16x16x32_bf16 v[152:155], v[116:119], v[40:43], 0\n" \
  "ds_read_b64_tr_b16 v[140:141], %[w] offset:4640\n" \
  "ds_read_b64_tr_b16 v[142:143], %[w] offset:4928\n" \
  "v_mfma_f32_16x16x32_bf16 v[156:159], v[120:123], v[40:43], 0\n" \
  "ds_read_b64_tr_b16 v[144:145], %[w] offset:4672\n" \
  "ds_read_b64_tr_b16 v[146:147], %[w] offset:4960\n" \
  "s_waitcnt lgkmcnt(8)\n" \
  "v_perm_b32 v48, v49, v48, %[sel]\n" \
  "v_perm_b32 v49, v51, v50, %[sel]\n" \
  "v_perm_b32 v50, v53, v52, %[sel]\n" \
  "v_perm_b32 v51, v55, v54, %[sel]\n" \
  "ds_read_b128 v[64:67], %[x] offset:3072\n" \
  "ds_read_b128 v[68:71], %[y] offset:3072\n" \
  "v_mfma_f32_16x16x32_bf16 v[148:151], v[124:127], v[48:51], v[148:151]\n" \
  "ds_read_b64_tr_b16 v[112:113], %[w] offset:6912\n" \
  "ds_read_b64_tr_b16 v[114:115], %[w] offset:7200\n" \
  "v_mfma_f32_16x16x32_bf16 v[152:155], v[128:131], v[48:51], v[152:155]\n" \
  "ds_read_b64_tr_b16 v[116:117], %[w] offset:6944\n" \
  "ds_read_b64_tr_b16 v[118:119], %[w] offset:7232\n" \
  "v_mfma_f32_16x16x32_bf16 v[156:159], v[132:135], v[48:51], v[156:159]\n" \
  "ds_read_b64_tr_b16 v[120:121], %[w] offset:6976\n" \
  "ds_read_b64_tr_b16 v[122:123], %[w] offset:7264\n" \
  "s_waitcnt lgkmcnt(8)\n" \
  "v_perm_b32 v56, v57, v56, %[sel]\n" \
  "v_perm_b32 v57, v59, v58, %[sel]\n" \
  "v_perm_b32 v58, v61, v60, %[sel]\n" \
  "v_perm_b32 v59, v63, v62, %[sel]\n" \
  "ds_read_b128 v[72:75], %[x] offset:4096\n" \
  "ds_read_b128 v[76:79], %[y] offset:4096\n" \
  "v_mfma_f32_16x16x32_bf16 v[148:151], v[136:139], v[56:59], v[148:151]\n" \
  "ds_read_b64_tr_b16 v[124:125], %[w] offset:9216\n" \
  "ds_read_b64_tr_b16 v[126:127], %[w] offset:9504\n" \
  "v_mfma_f32_16x16x32_bf16 v[152:155], v[140:143], v[56:59], v[152:155]\n" \
  "ds_read_b64_tr_b16 v[128:129], %[w] offset:9248\n" \
  "ds_read_b64_tr_b16 v[130:131], %[w] offset:9536\n" \
  "v_mfma_f32_16x16x32_bf16 v[156:159], v[144:147], v[56:59], v[156:159]\n" \
  "ds_read_b64_tr_b16 v[132:133], %[w] offset:9280\n" \
  "ds_read_b64_tr_b16 v[134:135], %[w] offset:9568\n" \
  "s_waitcnt lgkmcnt(8)\n" \
  "v_perm_b32 v64, v65, v64, %[sel]\n" \
  "v_perm_b32 v65, v67, v66, %[sel]\n" \
  "v_perm_b32 v66, v69, v68, %[sel]\n" \
  "v_perm_b32 v67, v71, v70, %[sel]\n" \
  "ds_read_b128 v[80:83], %[x] offset:5120\n" \
  "ds_read_b128 v[84:87], %[y] offset:5120\n" \
  "v_mfma_f32_16x16x32_bf16 v[148:151], v[112:115], v[64:67], v[148:151]\n" \
  "ds_read_b64_tr_b16 v[136:137], %[w] offset:11520\n" \
  "ds_read_b64_tr_b16 v[138:139], %[w] offset:11808\n" \
  "v_mfma_f32_16x16x32_bf16 v[152:155], v[116:119], v[64:67], v[152:155]\n" \
  "ds_read_b64_tr_b16 v[140:141], %[w] offset:11552\n" \
  "ds_read_b64_tr_b16 v[142:143], %[w] offset:11840\n" \
  "v_mfma_f32_16x16x32_bf16 v[156:159], v[120:123], v[64:67], v[156:159]\n" \
  "ds_read_b64_tr_b16 v[144:145], %[w] offset:11584\n" \
  "ds_read_b64_tr_b16 v[146:147], %[w] offset:11872\n" \
  "s_waitcnt lgkmcnt(8)\n" \
  "v_perm_b32 v72, v73, v72, %[sel]\n" \
  "v_perm_b32 v73, v75, v74, %[sel]\n" \
  "v_perm_b32 v74, v77, v76, %[sel]\n" \
  "v_perm_b32 v75, v79, v78, %[sel]\n" \
  "s_nop 1\n" \
  "v_mfma_f32_16x16x32_bf16 v[148:151], v[124:127], v[72:75], v[148:151]\n" \
  "v_mfma_f32_16x16x32_bf16 v[152:155], v[128:131], v[72:75], v[152:155]\n" \
  "v_mfma_f32_16x16x32_bf16 v[156:159], v[132:135], v[72:75], v[156:159]\n" \
  "s_waitcnt lgkmcnt(0)\n" \
  "v_perm_b32 v80, v81, v80, %[sel]\n" \
  "v_perm_b32 v81, v83, v82, %[sel]\n" \
  "v_perm_b32 v82, v85, v84, %[sel]\n" \
  "v_perm_b32 v83, v87, v86, %[sel]\n" \
  "s_nop 1\n" \
  "v_mfma_f32_16x16x32_bf16 v[148:151], v[136:139], v[80:83], v[148:151]\n" \
  "v_mfma_f32_16x16x32_bf16 v[152:155], v[140:143], v[80:83], v[152:155]\n" \
  "v_mfma_f32_16x16x32_bf16 v[156:159], v[144:147], v[80:83], v[156:159]\n" \
  "s_nop 5\n" \
  "ds_write_b128 %[p], v[148:151] offset:0\n" \
  "ds_write_b128 %[p], v[152:155] offset:64\n" \
  "ds_write_b128 %[p], v[156:159] offset:128\n" \
  "s_branch 178f\n" \
  "175:\n" \
  "ds_read_b128 v[40:43], %[x] offset:0\n" \
  "ds_read_b128 v[44:47], %[y] offset:0\n" \
  "ds_read_b64_tr_b16 v[112:113], %[w] offset:0\n" \
  "ds_read_b64_tr_b16 v[114:115], %[w] offset:288\n" \
  "ds_read_b64_tr_b16 v[116:117], %[w] offset:32\n" \
  "ds_read_b64_tr_b16 v[118:119], %[w] offset:320\n" \
  "ds_read_b64_tr_b16 v[120:121], %[w] offset:64\n" \
  "ds_read_b64_tr_b16 v[122:123], %[w] offset:352\n" \
  "ds_read_b128 v[48:51], %[x] offset:1024\n" \
  "ds_read_b128 v[52:55], %[y] offset:1024\n" \
  "ds_read_b64_tr_b16 v[124:125], %[w] offset:2304\n" \
  "ds_read_b64_tr_b16 v[126:127], %[w] offset:2592\n" \
  "ds_read_b64_tr_b16 v[128:129], %[w] offset:2336\n" \
  "ds_read_b64_tr_b16 v[130:131], %[w] offset:2624\n" \
  "ds_read_b64_tr_b16 v[132:133], %[w] offset:2368\n" \
  "ds_read_b64_tr_b16 v[134:135], %[w] offset:2656\n" \
  "s_waitcnt lgkmcnt(8)\n" \
  "v_perm_b32 v40, v41, v40, %[sel]\n" \
  "v_perm_b32 v41, v43, v42, %[sel]\n" \
  "v_perm_b32 v42, v45, v44, %[sel]\n" \
  "v_perm_b32 v43, v47, v46, %[sel]\n" \
  "ds_read_b128 v[56:59], %[x] offset:2048\n" \
  "ds_read_b128 v[60:63], %[y] offset:2048\n" \
  "v_mfma_f32_16x16x32_bf16 v[148:151], v[40:43], v[112:115], 0\n" \
  "ds_read_b64_tr_b16 v[136:137], %[w] offset:4608\n" \
  "ds_read_b64_tr_b16 v[138:139], %[w] offset:4896\n" \
  "v_mfma_f32_16x16x32_bf16 v[152:155], v[40:43], v[116:119], 0\n" \
  "ds_read_b64_tr_b16 v[140:141], %[w] offset:4640\n" \
  "ds_read_b64_tr_b16 v[142:143], %[w] offset:4928\n" \
  "v_mfma_f32_16x16x32_bf16 v[156:159], v[40:43], v[120:123], 0\n" \
  "ds_read_b64_tr_b16 v[144:145], %[w] offset:4672\n" \
  "ds_read_b64_tr_b16 v[146:147], %[w] offset:4960\n" \
  "s_waitcnt lgkmcnt(8)\n" \
  "v_perm_b32 v48, v49, v48, %[sel]\n" \
  "v_perm_b32 v49, v51, v50, %[sel]\n" \
  "v_perm_b32 v50, v53, v52, %[sel]\n" \
  "v_perm_b32 v51, v55, v54, %[sel]\n" \
  "ds_read_b128 v[64:67], %[x] offset:3072\n" \
  "ds_read_b128 v[68:71], %[y] offset:3072\n" \
  "v_mfma_f32_16x16x32_bf16 v[148:151], v[48:51], v[124:127], v[148:151]\n" \
  "ds_read_b64_tr_b16 v[112:113], %[w] offset:6912\n" \
  "ds_read_b64_tr_b16 v[114:115], %[w] offset:7200\n" \
  "v_mfma_f32_16x16x32_bf16 v[152:155], v[48:51], v[128:131], v[152:155]\n" \
  "ds_read_b64_tr_b16 v[116:117], %[w] offset:6944\n" \
  "ds_read_b64_tr_b16 v[118:119], %[w] offset:7232\n" \
  "v_mfma_f32_16x16x32_bf16 v[156:159], v[48:51], v[132:135], v[156:159]\n" \
  "ds_read_b64_tr_b16 v[120:121], %[w] offset:6976\n" \
  "ds_read_b64_tr_b16 v[122:123], %[w] offset:7264\n" \
  "s_waitcnt lgkmcnt(8)\n" \
  "v_perm_b32 v56, v57, v56, %[sel]\n" \
  "v_perm_b32 v57, v59, v58, %[sel]\n" \
  "v_perm_b32 v58, v61, v60, %[sel]\n" \
  "v_perm_b32 v59, v63, v62, %[sel]\n" \
  "ds_read_b128 v[72:75], %[x] offset:4096\n" \
  "ds_read_b128 v[76:79], %[y] offset:4096\n" \
  "v_mfma_f32_16x16x32_bf16 v[148:151], v[56:59], v[136:139], v[148:151]\n" \
  "ds_read_b64_tr_b16 v[124:125], %[w] offset:9216\n" \
  "ds_read_b64_tr_b16 v[126:127], %[w] offset:9504\n" \
  "v_mfma_f32_16x16x32_bf16 v[152:155], v[56:59], v[140:143], v[152:155]\n" \
  "ds_read_b64_tr_b16 v[128:129], %[w] offset:9248\n" \
  "ds_read_b64_tr_b16 v[130:131], %[w] offset:9536\n" \
  "v_mfma_f32_16x16x32_bf16 v[156:159], v[56:59], v[144:147], v[156:159]\n" \
  "ds_read_b64_tr_b16 v[132:133], %[w] offset:9280\n" \
  "ds_read_b64_tr_b16 v[134:135], %[w] offset:9568\n" \
  "s_waitcnt lgkmcnt(8)\n" \
  "v_perm_b32 v64, v65, v64, %[sel]\n" \
  "v_perm_b32 v65, v67, v66, %[sel]\n" \
  "v_perm_b32 v66, v69, v68, %[sel]\n" \
  "v_perm_b32 v67, v71, v70, %[sel]\n" \
  "ds_read_b128 v[80:83], %[x] offset:5120\n" \
  "ds_read_b128 v[84:87], %[y] offset:5120\n" \
  "v_mfma_f32_16x16x32_bf16 v[148:151], v[64:67], v[112:115], v[148:151]\n" \
  "ds_read_b64_tr_b16 v[136:137], %[w] offset:11520\n" \
  "ds_read_b64_tr_b16 v[138:139], %[w] offset:11808\n" \
  "v_mfma_f32_16x16x32_bf16 v[152:155], v[64:67], v[116:119], v[152:155]\n" \
  "ds_read_b64_tr_b16 v[140:141], %[w] offset:11552\n" \
  "ds_read_b64_tr_b16 v[142:143], %[w] offset:11840\n" \
  "v_mfma_f32_16x16x32_bf16 v[156:159], v[64:67], v[120:123], v[156:159]\n" \
  "ds_read_b64_tr_b16 v[144:145], %[w] offset:11584\n" \
  "ds_read_b64_tr_b16 v[146:147], %[w] offset:11872\n" \
  "s_waitcnt lgkmcnt(8)\n" \
  "v_perm_b32 v72, v73, v72, %[sel]\n" \
  "v_perm_b32 v73, v75, v74, %[sel]\n" \
  "v_perm_b32 v74, v77, v76, %[sel]\n" \
  "v_perm_b32 v75, v79, v78, %[sel]\n" \
  "s_nop 1\n" \
  "v_mfma_f32_16x16x32_bf16 v[148:151], v[72:75], v[124:127], v[148:151]\n" \
  "v_mfma_f32_16x16x32_bf16 v[152:155], v[72:75], v[128:131], v[152:155]\n" \
  "v_mfma_f32_16x16x32_bf16 v[156:159], v[72:75], v[132:135], v[156:159]\n" \
  "s_waitcnt lgkmcnt(0)\n" \
  "v_perm_b32 v80, v81, v80, %[sel]\n" \
  "v_perm_b32 v81, v83, v82, %[sel]\n" \
  "v_perm_b32 v82, v85, v84, %[sel]\n" \
  "v_perm_b32 v83, v87, v86, %[sel]\n" \
  "s_nop 1\n" \
  "v_mfma_f32_16x16x32_bf16 v[148:151], v[80:83], v[136:139], v[148:151]\n" \
  "v_mfma_f32_16x16x32_bf16 v[152:155], v[80:83], v[140:143], v[152:155]\n" \
  "v_mfma_f32_16x16x32_bf16 v[156:159], v[80:83], v[144:147], v[156:159]\n" \
  "s_nop 5\n" \
  "ds_write_b128 %[p], v[148:151] offset:0\n" \
  "ds_write_b128 %[p], v[152:155] offset:512\n" \
  "ds_write_b128 %[p], v[156:159] offset:1024\n" \
  "178:\n" \
  "s_branch 184f\n" \
  "174:\n" \
  "s_sleep 1\n" \
  "184:\n" \
  "ds_read_b32 v40, %[ctl]\n" \
  "s_waitcnt lgkmcnt(0)\n" \
  "v_readfirstlane_b32 %[st], v40\n" \
  "s_nop 3\n" \
  "s_cmp_ge_u32 %[st], 1\n" \
  "s_cbranch_scc0 174b\n" \
  "ds_read_b64 v[48:49], %[a0] offset:0\n" \
  "ds_read_b64 v[56:57], %[a1] offset:0\n" \
  "ds_read_b64 v[50:51], %[a0] offset:8\n" \
  "ds_read_b64 v[58:59], %[a1] offset:8\n" \
  "ds_read_b64 v[52:53], %[a0] offset:16\n" \
  "ds_read_b64 v[60:61], %[a1] offset:16\n" \
  "ds_read_b64 v[54:55], %[a0] offset:24\n" \
  "ds_read_b64 v[62:63], %[a1] offset:24\n" \
  "ds_read_b64 v[64:65], %[a0] offset:13824\n" \
  "ds_read_b64 v[72:73], %[a1] offset:13824\n" \
  "ds_read_b64 v[66:67], %[a0] offset:13832\n" \
  "ds_read_b64 v[74:75], %[a1] offset:13832\n" \
  "ds_read_b64 v[68:69], %[a0] offset:13840\n" \
  "ds_read_b64 v[76:77], %[a1] offset:13840\n" \
  "ds_read_b64 v[70:71], %[a0] offset:13848\n" \
  "ds_read_b64 v[78:79], %[a1] offset:13848\n" \
  "s_waitcnt lgkmcnt(8)\n" \
  "v_add_f32 v48, v48, v56\n" \
  "v_add_f32 v49, v49, v57\n" \
  "v_add_f32 v50, v50, v58\n" \
  "v_add_f32 v51, v51, v59\n" \
  "v_add_f32 v52, v52, v60\n" \
  "v_add_f32 v53, v53, v61\n" \
  "v_add_f32 v54, v54, v62\n" \
  "v_add_f32 v55, v55, v63\n" \
  "s_waitcnt lgkmcnt(0)\n" \
  "v_add_f32 v64, v64, v72\n" \
  "v_add_f32 v65, v65, v73\n" \
  "v_add_f32 v48, v48, v64\n" \
  "v_add_f32 v49, v49, v65\n" \
  "v_cvt_pk_bf16_f32 v44, v48, v49\n" \
  "v_add_f32 v66, v66, v74\n" \
  "v_add_f32 v67, v67, v75\n" \
  "v_add_f32 v50, v50, v66\n" \
  "v_add_f32 v51, v51, v67\n" \
  "v_cvt_pk_bf16_f32 v45, v50, v51\n" \
  "v_add_f32 v68, v68, v76\n" \
  "v_add_f32 v69, v69, v77\n" \
  "v_add_f32 v52, v52, v68\n" \
  "v_add_f32 v53, v53, v69\n" \
  "v_cvt_pk_bf16_f32 v46, v52, v53\n" \
  "v_add_f32 v70, v70, v78\n" \
  "v_add_f32 v71, v71, v79\n" \
  "v_add_f32 v54, v54, v70\n" \
  "v_add_f32 v55, v55, v71\n" \
  "v_cvt_pk_bf16_f32 v47, v54, v55\n" \
  "s_mov_b64 exec, %[m16]\n" \
  "buffer_store_dwordx4 v[44:47], %[o16], %[rs], %[so] offen sc1\n" \
  "s_mov_b64 exec, %[m4]\n" \
  "buffer_store_dword v44, %[o16], %[rs], %[so] offen sc1\n" \
  "s_mov_b64 exec, -1\n" \
  "s_branch 179f\n" \
  "176:\n" \
  "ds_read_b128 v[88:91], %[x] offset:6144\n" \
  "ds_read_b128 v[92:95], %[y] offset:6144\n" \
  "ds_read_b64_tr_b16 v[112:113], %[w] offset:13824\n" \
  "ds_read_b64_tr_b16 v[114:115], %[w] offset:14112\n" \
  "ds_read_b64_tr_b16 v[116:117], %[w] offset:13856\n" \
  "ds_read_b64_tr_b16 v[118:119], %[w] offset:14144\n" \
  "ds_read_b64_tr_b16 v[120:121], %[w] offset:13888\n" \
  "ds_read_b64_tr_b16 v[122:123], %[w] offset:14176\n" \
  "ds_read_b128 v[96:99], %[x] offset:7168\n" \
  "ds_read_b128 v[100:103], %[y] offset:7168\n" \
  "ds_read_b64_tr_b16 v[124:125], %[w] offset:16128\n" \
  "ds_read_b64_tr_b16 v[126:127], %[w] offset:16416\n" \
  "ds_read_b64_tr_b16 v[128:129], %[w] offset:16160\n" \
  "ds_read_b64_tr_b16 v[130:131], %[w] offset:16448\n" \
  "ds_read_b64_tr_b16 v[132:133], %[w] offset:16192\n" \
  "ds_read_b64_tr_b16 v[134:135], %[w] offset:16480\n" \
  "s_waitcnt lgkmcnt(8)\n" \
  "v_perm_b32 v88, v89, v88, %[sel]\n" \
  "v_perm_b32 v89, v91, v90, %[sel]\n" \
  "v_perm_b32 v90, v93, v92, %[sel]\n" \
  "v_perm_b32 v91, v95, v94, %[sel]\n" \
  "ds_read_b128 v[104:107], %[x] offset:8192\n" \
  "ds_read_b128 v[108:111], %[y] offset:8192\n" \
  "v_mfma_f32_16x16x32_bf16 v[148:151], v[112:115], v[88:91], 0\n" \
  "ds_read_b64_tr_b16 v[136:137], %[w] offset:18432\n" \
  "ds_read_b64_tr_b16 v[138:139], %[w] offset:18720\n" \
  "v_mfma_f32_16x16x32_bf16 v[152:155], v[116:119], v[88:91], 0\n" \
  "ds_read_b64_tr_b16 v[140:141], %[w] offset:18464\n" \
  "ds_read_b64_tr_b16 v[142:143], %[w] offset:18752\n" \
  "v_mfma_f32_16x16x32_bf16 v[156:159], v[120:123], v[88:91], 0\n" \
  "ds_read_b64_tr_b16 v[144:145], %[w] offset:18496\n" \
  "ds_read_b64_tr_b16 v[146:147], %[w] offset:18784\n" \
  "s_waitcnt lgkmcnt(8)\n" \
  "v_perm_b32 v96, v97, v96, %[sel]\n" \
  "v_perm_b32 v97, v99, v98, %[sel]\n" \
  "v_perm_b32 v98, v101, v100, %[sel]\n" \
  "v_perm_b32 v99, v103, v102, %[sel]\n" \
  "s_nop 1\n" \
  "v_mfma_f32_16x16x32_bf16 v[148:151], v[124:127], v[96:99], v[148:151]\n" \
  "v_mfma_f32_16x16x32_bf16 v[152:155], v[128:131], v[96:99], v[152:155]\n" \
  "v_mfma_f32_16x16x32_bf16 v[156:159], v[132:135], v[96:99], v[156:159]\n" \
  "s_waitcnt lgkmcnt(0)\n" \
  "v_perm_b32 v104, v105, v104, %[sel]\n" \
  "v_perm_b32 v105, v107, v106, %[sel]\n" \
  "v_perm_b32 v106, v109, v108, %[sel]\n" \
  "v_perm_b32 v107, v111, v110, %[sel]\n" \
  "s_nop 1\n" \
  "v_mfma_f32_16x16x32_bf16 v[148:151], v[136:139], v[104:107], v[148:151]\n" \
  "v_mfma_f32_16x16x32_bf16 v[152:155], v[140:143], v[104:107], v[152:155]\n" \
  "v_mfma_f32_16x16x32_bf16 v[156:159], v[144:147], v[104:107], v[156:159]\n" \
  "s_nop 5\n" \
  "ds_write_b128 %[p], v[148:151] offset:0\n" \
  "ds_write_b128 %[p], v[152:155] offset:64\n" \
  "ds_write_b128 %[p], v[156:159] offset:128\n" \
  "s_waitcnt lgkmcnt(0)\n" \
  "s_mov_b64 exec, 1\n" \
  "ds_add_u32 %[ctl], %[one]\n" \
  "s_mov_b64 exec, -1\n" \
  "s_branch 179f\n" \
  "177:\n" \
  "ds_read_b128 v[88:91], %[x] offset:6144\n" \
  "ds_read_b128 v[92:95], %[y] offset:6144\n" \
  "ds_read_b64_tr_b16 v[112:113], %[w] offset:13824\n" \
  "ds_read_b64_tr_b16 v[114:115], %[w] offset:14112\n" \
  "ds_read_b64_tr_b16 v[116:117], %[w] offset:13856\n" \
  "ds_read_b64_tr_b16 v[118:119], %[w] offset:14144\n" \
  "ds_read_b64_tr_b16 v[120:121], %[w] offset:13888\n" \
  "ds_read_b64_tr_b16 v[122:123], %[w] offset:14176\n" \
  "ds_read_b128 v[96:99], %[x] offset:7168\n" \
  "ds_read_b128 v[100:103], %[y] offset:7168\n" \
  "ds_read_b64_tr_b16 v[124:125], %[w] offset:16128\n" \
  "ds_read_b64_tr_b16 v[126:127], %[w] offset:16416\n" \
  "ds_read_b64_tr_b16 v[128:129], %[w] offset:16160\n" \
  "ds_read_b64_tr_b16 v[130:131], %[w] offset:16448\n" \
  "ds_read_b64_tr_b16 v[132:133], %[w] offset:16192\n" \
  "ds_read_b64_tr_b16 v[134:135], %[w] offset:16480\n" \
  "s_waitcnt lgkmcnt(8)\n" \
  "v_perm_b32 v88, v89, v88, %[sel]\n" \
  "v_perm_b32 v89, v91, v90, %[sel]\n" \
  "v_perm_b32 v90, v93, v92, %[sel]\n" \
  "v_perm_b32 v91, v95, v94, %[sel]\n" \
  "ds_read_b128 v[104:107], %[x] offset:8192\n" \
  "ds_read_b128 v[108:111], %[y] offset:8192\n" \
  "v_mfma_f32_16x16x32_bf16 v[148:151], v[88:91], v[112:115], 0\n" \
  "ds_read_b64_tr_b16 v[136:137], %[w] offset:18432\n" \
  "ds_read_b64_tr_b16 v[138:139], %[w] offset:18720\n" \
  "v_mfma_f32_16x16x32_bf16 v[152:155], v[88:91], v[116:119], 0\n" \
  "ds_read_b64_tr_b16 v[140:141], %[w] offset:18464\n" \
  "ds_read_b64_tr_b16 v[142:143], %[w] offset:18752\n" \
  "v_mfma_f32_16x16x32_bf16 v[156:159], v[88:91], v[120:123], 0\n" \
  "ds_read_b64_tr_b16 v[144:145], %[w] offset:18496\n" \
  "ds_read_b64_tr_b16 v[146:147], %[w] offset:18784\n" \
  "s_waitcnt lgkmcnt(8)\n" \
  "v_perm_b32 v96, v97, v96, %[sel]\n" \
  "v_perm_b32 v97, v99, v98, %[sel]\n" \
  "v_perm_b32 v98, v101, v100, %[sel]\n" \
  "v_perm_b32 v99, v103, v102, %[sel]\n" \
  "s_nop 1\n" \
  "v_mfma_f32_16x16x32_bf16 v[148:151], v[96:99], v[124:127], v[148:151]\n" \
  "v_mfma_f32_16x16x32_bf16 v[152:155], v[96:99], v[128:131], v[152:155]\n" \
  "v_mfma_f32_16x16x32_bf16 v[156:159], v[96:99], v[132:135], v[156:159]\n" \
  "s_waitcnt lgkmcnt(0)\n" \
  "v_perm_b32 v104, v105, v104, %[sel]\n" \
  "v_perm_b32 v105, v107, v106, %[sel]\n" \
  "v_perm_b32 v106, v109, v108, %[sel]\n" \
  "v_perm_b32 v107, v111, v110, %[sel]\n" \
  "s_nop 1\n" \
  "v_mfma_f32_16x16x32_bf16 v[148:151], v[104:107], v[136:139], v[148:151]\n" \
  "v_mfma_f32_16x16x32_bf16 v[152:155], v[104:107], v[140:143], v[152:155]\n" \
  "v_mfma_f32_16x16x32_bf16 v[156:159], v[104:107], v[144:147], v[156:159]\n" \
  "s_nop 5\n" \
  "ds_write_b128 %[p], v[148:151] offset:0\n" \
  "ds_write_b128 %[p], v[152:155] offset:512\n" \
  "ds_write_b128 %[p], v[156:159] offset:1024\n" \
  "s_waitcnt lgkmcnt(0)\n" \
  "s_mov_b64 exec, 1\n" \
  "ds_add_u32 %[ctl], %[one]\n" \
  "s_mov_b64 exec, -1\n" \
  "s_branch 179f\n" \
  "179:\n"
#define KV_CLOB "v40","v41","v42","v43","v44","v45","v46","v47","v48","v49","v50","v51","v52","v53","v54","v55","v56","v57","v58","v59","v60","v61","v62","v63","v64","v65","v66","v67","v68","v69","v70","v71","v72","v73","v74","v75","v76","v77","v78","v79","v80","v81","v82","v83","v84","v85","v86","v87","v88","v89","v90","v91","v92","v93","v94","v95","v96","v97","v98","v99","v100","v101","v102","v103","v104","v105","v106","v107","v108","v109","v110","v111","v112","v113","v114","v115","v116","v117","v118","v119","v120","v121","v122","v123","v124","v125","v126","v127","v128","v129","v130","v131","v132","v133","v134","v135","v136","v137","v138","v139","v140","v141","v142","v143","v144","v145","v146","v147","v148","v149","v150","v151","v152","v153","v154","v155","v156","v157","v158","v159"


constexpr unsigned WS_F1 = 4096, WS_F2 = WS_F1 + 32768, WS_F3 = WS_F2 + 32768;
constexpr unsigned WS_IMG = 262144, WS_KVB = 524288, WS_ZT = 3145728, WS_OX = 5767168;
constexpr unsigned WS_BYTES = 8388608;
constexpr int CH = 9216, IMG = 16 * CH;
constexpr int L_WQ = 73728, L_KVS = L_WQ + 6 * 4608, L_KVX = L_KVS + 43008;
constexpr int L_QT = 10 * CH;
constexpr int L_H = 5 * CH, HCOPY = 4160;
constexpr int L_MISC = IMG, USTR = 4608;
constexpr int L_SCR = L_MISC + 3 * USTR;
constexpr int L_CTL = L_SCR + 2048;
constexpr int L_OEX = 3 * CH, L_OT = L_OEX + 24576;
constexpr int L_OUT = L_MISC + 9216;
constexpr int LDS_BYTES = 163840;
constexpr int C_LAND = 0, C_PROG = 4, C_L0 = 8, C_T = 12, C_Z = 16, C_BAD = 20, C_KV = 24, C_U = 28, C_CV = 32, C_KVD = 40, C_KVL = 44, C_G = 64, C_Q = 68, C_I = 84,
              C_LANDB = 88, C_PROGB = 92, C_KH = 104;
constexpr unsigned SPIN_CAP = 1u << 17;

__device__ __forceinline__ unsigned pk2(float lo, float hi) { f32x2 v = {lo, hi}; return __builtin_bit_cast(unsigned, __builtin_convertvector(v, bf16x2)); }
__device__ __forceinline__ float blo(unsigned u) { return __uint_as_float(u << 16); }
__device__ __forceinline__ float bhi(unsigned u) { return __uint_as_float(u & 0xffff0000u); }
__device__ __forceinline__ void dma16(const GAS char* base, unsigned voff, LAS char* l) {
    asm volatile("s_mov_b32 m0, %2\n\ts_nop 0\n\tglobal_load_lds_dwordx4 %0, %1" :: "v"(voff), "s"(base), "s"((unsigned)(size_t)l) : "memory"); }
__device__ __forceinline__ void dma16_sc1(const GAS char* base, unsigned voff, LAS char* l) {
    asm volatile("s_mov_b32 m0, %2\n\ts_nop 0\n\tglobal_load_lds_dwordx4 %0, %1 sc1" :: "v"(voff), "s"(base), "s"((unsigned)(size_t)l) : "memory"); }
#define GLL(v, o) "global_load_lds_dwordx4 " v ", %3 offset:" #o " sc1\n\t"
__device__ __forceinline__ void dma_chunk(const GAS char* base, unsigned v0, unsigned v1, unsigned v2, LAS char* l) {
    asm volatile("s_mov_b32 m0, %4\n\ts_nop 0\n\t" GLL("%0", 0) GLL("%0", 1024) GLL("%0", 2048) GLL("%0", 3072)
                 "s_add_u32 m0, m0, 0x1000\n\ts_nop 0\n\t" GLL("%1", 0) GLL("%1", 1024) GLL("%1", 2048) GLL("%1", 3072)
                 "s_add_u32 m0, m0, 0x1000\n\ts_nop 0\n\t" GLL("%2", 0)
                 :: "v"(v0), "v"(v1), "v"(v2), "s"(base), "s"((unsigned)(size_t)l) : "memory", "scc"); }
__device__ __forceinline__ bf16x8 asbf(v4u v) { return __builtin_bit_cast(bf16x8, v); }
#define BY_KH(a, b) "s_cmp_eq_u32 %[kh], 0\n\ts_cbranch_scc0 198f\n" a "s_branch 199f\n198:\n" b "199:\n"
#define BAR() asm volatile("s_waitcnt lgkmcnt(0)\n\ts_barrier" ::: "memory")
#define BAR_VM() asm volatile("s_waitcnt vmcnt(0) lgkmcnt(0)\n\ts_barrier" ::: "memory")
#define DRAIN() asm volatile("s_waitcnt vmcnt(0)" ::: "memory")

__device__ __forceinline__ v4u mkrs(const char* p, unsigned bytes) { const u64 a = (u64)p; return (v4u){(unsigned)a, (unsigned)(a >> 32) & 0xffffu, bytes, 0x00020000u}; }
__device__ __forceinline__ int fresh_lane() { int l; asm volatile("v_mbcnt_lo_u32_b32 %0, -1, 0\n\tv_mbcnt_hi_u32_b32 %0, -1, %0" : "=v"(l)); __builtin_assume(l >= 0 && l < 64); return l; }
template <int N, int SLEEP>
__device__ __forceinline__ void poll_flags(const GAS char* f, const int (&idx)[N], u64 want) {
#pragma unroll 1
    for (unsigned spins = 0; spins < SPIN_CAP; ++spins) {
        bool ok = true;
#pragma unroll
        for (int q = 0; q < N; ++q) ok &= (__hip_atomic_load((const gu64*)(f + idx[q] * 128), RLX_AGENT) == want);
        if (__all(ok)) break;
        __builtin_amdgcn_s_sleep(SLEEP);
    }
    asm volatile("" ::: "memory");
}
__device__ __forceinline__ void lds_add(LAS char* lds, int word, int) {
    asm volatile("s_waitcnt lgkmcnt(0)\n\ts_mov_b64 exec, 1\n\tds_add_u32 %0, %1\n\ts_mov_b64 exec, -1" :: "v"((unsigned)(size_t)lds + L_CTL + word), "v"(1u) : "memory");
}
__device__ __forceinline__ void signal_last(LAS char* lds, int word, unsigned n, GAS char* flag, u64 val, int lane) {
    DRAIN();
    if (lane == 0) {
        const unsigned old = __hip_atomic_fetch_add((LAS unsigned*)(lds + L_CTL + word), 1u, RLX_WG);
        if (old == n - 1) __hip_atomic_store((gu64*)flag, val, RLX_AGENT);
    }
}
template <int SLEEP>
__device__ __forceinline__ void lds_wait(LAS char* lds, int word, unsigned n) {
    unsigned t, sv;
    asm volatile("s_branch 2f\n1:\n\ts_sleep %4\n2:\n\tds_read_b32 %0, %2\n\ts_waitcnt lgkmcnt(0)\n\tv_readfirstlane_b32 %1, %0\n\ts_nop 3\n\ts_cmp_ge_u32 %1, %3\n\ts_cbranch_scc0 1b"
                 : "=&v"(t), "=&s"(sv) : "v"((unsigned)(size_t)lds + L_CTL + word), "s"(n), "i"(SLEEP) : "memory", "scc");
}

__device__ __forceinline__ unsigned w_lane(int lane) { const int a = (lane >> 1) & 7; return ((a & 3) + 8 * (a >> 2)) * 1152 + (lane >> 4) * 32 + (lane & 1) * 16; }
__device__ __forceinline__ int w_row(int p) { return 32 * (p >> 2) + 16 * ((p >> 1) & 1) + 4 * (p & 1); }
__device__ __forceinline__ void stage_wo(const GAS char* wsl, LAS char* lds, int first, int last, int step, int lane) {
    const unsigned wl = w_lane(lane);
#pragma unroll 1
    for (int gr = first; gr < last; gr += step) {
        LAS char* dst = lds + (gr < 3 ? gr * 8192 : 8 * CH + (gr - 3) * 8192);
#pragma unroll
        for (int p = 0; p < 8; ++p) dma16(wsl + gr * 73728 + w_row(p) * 1152, wl, dst + p * 1024);
        dma16(wsl + gr * 73728 + 128, lane * 1152, lds + (gr < 3 ? 24576 + gr * 1024 : 8 * CH + 49152 + (gr - 3) * 1024));
    }
}
#define GW(o) "global_load_lds_dwordx4 %0, %1 offset:" #o "\n\t"
__device__ __forceinline__ void kv_pieces(const unsigned short* x_, const unsigned short* wk_, const unsigned short* wv_, LAS char* lds, int b, int bw, int lane) {
    const unsigned xo = (lane >> 3) * 1152 + ((lane & 7) ^ (((lane >> 4) & 1) | ((lane >> 3) & 4))) * 16, l0 = (unsigned)(size_t)lds;
    const unsigned v0 = bw * 5120 + lane * 16, v1 = v0 + 4096, v2 = 20480 + (lane & 15) * 16;
#pragma unroll
    for (int m = 0; m < 2; ++m) {
        const GAS char* w = (const GAS char*)(m ? wv_ : wk_);
        asm volatile("s_mov_b32 m0, %2\n\ts_nop 0\n\t" GW(0) GW(1024) GW(2048) GW(3072) "s_add_u32 m0, m0, 0x1000\n\ts_nop 0\n\tglobal_load_lds_dwordx4 %3, %1"
                     :: "v"(v0), "s"(w), "s"(l0 + L_KVS + m * 21504 + bw * 5120), "v"(v1) : "memory", "scc");
        if (bw == 2 + m) dma16(w, v2, lds + L_KVS + m * 21504 + 20480);
    }
    const int p0 = bw < 2 ? bw * 3 : 2 * bw + 2;
    asm volatile("s_mov_b32 m0, %2\n\ts_nop 0\n\t" GW(0) :: "v"(xo), "s"((const GAS char*)x_ + b * 9216 + p0 * 128), "s"(l0 + L_KVX + p0 * 1024) : "memory");
    if (bw < 3) dma16((const GAS char*)x_ + b * 9216 + p0 * 128 + 128, xo, lds + L_KVX + p0 * 1024 + 1024);
    if (bw < 2) dma16((const GAS char*)x_ + b * 9216 + p0 * 128 + 256, xo, lds + L_KVX + p0 * 1024 + 2048);
}
__global__ void __launch_bounds__(768, 3) fused_h3_mqa(const unsigned short* Aq_, const unsigned short* Ak_, const unsigned short* Av_, const unsigned short* Ash_,
                                                      const unsigned short* Ah_, const unsigned short* AD_, const unsigned short* x_, const unsigned short* wq_,
                                                      const unsigned short* wk_, const unsigned short* wv_, const unsigned short* wo_, unsigned short* out_, char* ws_) {
    extern __shared__ __attribute__((aligned(16))) char smem[];
    LAS char* lds = (LAS char*)smem;
    asm volatile("ds_write_b32 %0, %1\n\ts_waitcnt lgkmcnt(0)\n\ts_barrier" :: "v"((unsigned)(size_t)lds + L_CTL + (threadIdx.x & 31) * 4), "v"(0u) : "memory");
    GAS char* ws = (GAS char*)ws_;
    const int wid = __builtin_amdgcn_readfirstlane(threadIdx.x >> 6), b = blockIdx.x;
    const int hg = (b >> 3) & 7, rb = (b & 7) * 4 + (b >> 6);
    const int rot = (b >> 3) & 7;
    const __amdgpu_buffer_rsrc_t wsr = __builtin_amdgcn_make_buffer_rsrc(ws_, 0, WS_BYTES, 0x00020000);
    const GAS char* img = ws + WS_IMG;

    if (__builtin_expect(wid >= 8, 1)) {
        __builtin_amdgcn_s_setprio(3);
        kv_pieces(x_, wk_, wv_, lds, b, wid - 8, fresh_lane());
    }
    asm volatile("" ::: "memory");
    if (wid >= 8) {
        const int lane = fresh_lane();
        const int bw = wid - 8;
        const u64 ep = __hip_atomic_load((const gu64*)ws, RLX_AGENT);
        {
            const int g = lane >> 4, par = (lane >> 3) & 1, row = lane & 7, q = (lane & 15) >> 2, p = lane & 3, mv = bw & 1;
            const unsigned l0 = (unsigned)(size_t)lds, pp = l0 + L_KVS + mv * 21504;
            const int hh = (lane >> 3) > 2 ? 2 : (lane >> 3), n = lane < 18 ? lane : 0;
            const unsigned pw = (mv ? pp + (((g >> 1) * 48 + (lane & 15)) * 8 + 4 * (g & 1)) * 4 : pp + ((row * 2 + par) * 48 + 4 * g) * 4) + (bw >> 1) * 13824;
            const unsigned a0 = mv ? pp + n * 32 : pp + (row * 96 + 8 * hh) * 4, a1 = mv ? a0 + 66 * 32 : a0 + 66 * 4;
            const unsigned o16 = mv ? 4096 + (b & 15) * 288 + lane * 16 : lane < 16 ? ((b & 15) >> 2) * 1024 + ((lane >> 3) * 32 + 8 * (b & 3) + row) * 16 : 8704 + (8 * (b & 15) + row) * 4;
            const unsigned xa = l0 + L_KVX + row * 128 + ((2 * g) ^ (((row >> 1) & 1) | (row & 4))) * 16;
            unsigned st;
            asm volatile(KVPROJ : [st] "=&s"(st)
                         : [bw] "s"(bw), [w] "v"(l0 + L_KVS + mv * 21504 + (8 * g + q) * 72 + p * 8), [x] "v"(xa), [y] "v"(xa ^ 16), [sel] "v"(par ? 0x07060302u : 0x05040100u),
                           [p] "v"(pw), [a0] "v"(a0), [a1] "v"(a1), [o16] "v"(o16), [rs] "s"(mkrs(ws_, WS_BYTES)),
                           [so] "s"(WS_IMG + (b >> 4) * CH), [m16] "s"(mv ? 0x3ffffull : 0xffffull), [m4] "s"(mv ? 0ull : 0xff0000ull), [ctl] "v"(l0 + L_CTL + C_KH + mv * 4), [one] "v"(1u), [cl] "v"(l0 + L_CTL + C_KVL)
                         : "memory", "scc", KV_CLOB);
        }
        if (bw < 2) lds_add(lds, C_KVD, lane);
        const u64 want = (((u64)(unsigned)__builtin_amdgcn_readfirstlane((unsigned)(ep >> 32)) << 32) | (unsigned)__builtin_amdgcn_readfirstlane((unsigned)ep)) + 1;
        if (__builtin_expect(bw < 2, 1)) {
            signal_last(lds, C_KV, 2, ws + WS_F1 + b * 128, want, lane);
            __builtin_amdgcn_s_setprio(0);
        } else {
            __builtin_amdgcn_s_setprio(0);
            lds_wait<2>(lds, C_I, 6);
            v4u kk[11], vv[8], sw[4];
            const int tt = (bw - 2) * 36 + lane; const bool tact = lane < 36;
            if (tact) {
                const GAS char* kp = (const GAS char*)Ak_ + (b * 8 - 3) * 1152 + tt * 16;
                const GAS char* vp = (const GAS char*)Av_ + (b * 8) * 1152 + tt * 16;
#pragma unroll
                for (int i = 10; i >= 0; --i) kk[i] = *(const GAS v4u*)(kp + (b > 0 || i >= 3 ? i : 3) * 1152);
#pragma unroll
                for (int i = 0; i < 8; ++i) vv[i] = *(const GAS v4u*)(vp + i * 1152);
#pragma unroll
                for (int i = 0; i < 4; ++i) sw[i] = *(const GAS v4u*)((const GAS char*)Ash_ + tt * 64 + i * 16);
            }
            __builtin_amdgcn_s_setprio(3);
            if (tact) {
                if (b == 0) { kk[0] = (v4u){0u, 0u, 0u, 0u}; kk[1] = kk[0]; kk[2] = kk[0]; }
#pragma unroll
                for (int j = 0; j < 8; ++j) {
                    const unsigned w01 = sw[j >> 1][(j & 1) * 2], w23 = sw[j >> 1][(j & 1) * 2 + 1];
                    const float w0 = blo(w01), w1 = bhi(w01), w2 = blo(w23), w3 = bhi(w23);
                    float kf[11], r[8];
#pragma unroll
                    for (int i = 0; i < 11; ++i) kf[i] = (j & 1) ? bhi(kk[i][j >> 1]) : blo(kk[i][j >> 1]);
#pragma unroll
                    for (int i = 0; i < 8; ++i) {
                        const float ks = __builtin_fmaf(w0, kf[i + 3], __builtin_fmaf(w1, kf[i + 2], __builtin_fmaf(w2, kf[i + 1], w3 * kf[i])));
                        r[i] = ks * ((j & 1) ? bhi(vv[i][j >> 1]) : blo(vv[i][j >> 1]));
                    }
                    const v4u gr = {pk2(r[7], r[6]), pk2(r[5], r[4]), pk2(r[3], r[2]), pk2(r[1], r[0])};
                    __builtin_amdgcn_raw_buffer_store_b128(gr, wsr, WS_KVB + b * 9216 + (j * 72 + tt) * 16, 0, 16);
                }
            }
            {
                const v4u sent = {~0u, ~0u, ~0u, ~0u};
                const unsigned o0 = WS_OX + (rb * 8 + hg) * 9216 + (bw - 2) * 4608 + lane * 16;
#pragma unroll
                for (int i = 0; i < 4; ++i) __builtin_amdgcn_raw_buffer_store_b128(sent, wsr, o0 + i * 1024, 0, 16);
                if (lane < 32) __builtin_amdgcn_raw_buffer_store_b128(sent, wsr, o0 + 4096, 0, 16);
            }
            signal_last(lds, C_T, 2, ws + WS_F2 + b * 128, want, lane);
        }
        const int slot0 = (b >> 2) * 9 + (b & 3) * 2;
        const unsigned l16 = lane * 16;
        if (bw < 2) {
            const int li = ((lane >> 4) & 1) + bw * 8;
            const int idx[1] = {((li & 8) + ((li + rot) & 7)) * 16 + (lane & 15)};
            lds_wait<1>(lds, C_G, 8);
            poll_flags<1, 2>(ws + WS_F1, idx, want);
            dma_chunk(img + (bw * 8 + rot) * CH, l16, l16 + 4096, l16 + 8192, lds + bw * 8 * CH);
            dma_chunk(img + (bw * 8 + ((1 + rot) & 7)) * CH, l16, l16 + 4096, l16 + 8192, lds + (bw * 8 + 1) * CH);
            asm volatile("s_waitcnt vmcnt(9)" ::: "memory");
            lds_add(lds, C_L0, lane);
            DRAIN();
            lds_add(lds, bw ? C_LANDB : C_LAND, lane);
            const int la = 2 + (lane >> 4), lb = 6 + ((lane >> 4) & 1);
            const int idx2[2] = {(bw * 8 + ((la + rot) & 7)) * 16 + (lane & 15), (bw * 8 + ((lb + rot) & 7)) * 16 + (lane & 15)};
            poll_flags<2, 1>(ws + WS_F1, idx2, want);
            lds_wait<1>(lds, C_Q, 16);
            __builtin_amdgcn_s_setprio(3);
            int n = 0;
#pragma unroll 1
            for (unsigned t = bw ? 0xFEDCBAu : 0x432u; t; t >>= 4, ++n) {
                const int li = t & 15; dma_chunk(img + ((li & 8) + ((li + rot) & 7)) * CH, l16, l16 + 4096, l16 + 8192, lds + li * CH);
                if (n == 2) { asm volatile("s_waitcnt vmcnt(9)" ::: "memory"); lds_add(lds, bw ? C_LANDB : C_LAND, lane); }
            }
            DRAIN();
            lds_add(lds, bw ? C_LANDB : C_LAND, lane);
        }
        if (bw >= 2) {
            lds_wait<2>(lds, C_LANDB, 3);
            v4u hd[6]; unsigned hp[6];
#pragma unroll
            for (int c = 0; c < 6; ++c) {
                const int sl = slot0 + (c >> 1), chn = (sl % 72) * 8 + sl / 72, i = (bw - 2) * 128 + (c & 1) * 64 + lane;
                const GAS char* hr = (const GAS char*)Ah_ + chn * 4096 + i * 16;
                hd[c] = *(const GAS v4u*)hr;
                hp[c] = *(const GAS unsigned*)(hr - (i ? 4 : 0));
            }
#pragma unroll
            for (int c = 0; c < 6; ++c) {
                const int i = (bw - 2) * 128 + (c & 1) * 64 + lane;
                const v4u d = hd[c]; const unsigned pv = i ? hp[c] : 0u;
                *(LAS v4u*)(lds + L_H + (c & 6) * HCOPY + 32 + i * 16) = d;
                *(LAS v4u*)(lds + L_H + (c | 1) * HCOPY + 32 + i * 16) =
                    (v4u){__builtin_amdgcn_alignbit(d.x, pv, 16), __builtin_amdgcn_alignbit(d.y, d.x, 16), __builtin_amdgcn_alignbit(d.z, d.y, 16), __builtin_amdgcn_alignbit(d.w, d.z, 16)};
            }
            const int i = (bw - 2) * 64 + lane;
            if (i >= 96 && i < 108) *(LAS v4u*)(lds + L_H + ((i - 96) >> 1) * HCOPY + (i & 1) * 16) = (v4u){0u, 0u, 0u, 0u};
            if (i < 96) *(LAS v4u*)(lds + L_MISC + (i >> 5) * USTR + (i & 31) * 16) = (v4u){0u, 0u, 0u, 0u};
            const int idx[2] = {(bw - 2) * 128 + lane, (bw - 2) * 128 + 64 + lane};
            poll_flags<2, 4>(ws + WS_F2, idx, want);
#pragma unroll
            for (int c = 0; c < 6; ++c) dma16_sc1(ws + WS_KVB + ((bw - 2) * 2 + (c & 1)) * 589824 + (slot0 + (c >> 1)) * 16, lane * 9216, lds + L_MISC + (c >> 1) * USTR + 512 + ((bw - 2) * 2 + (c & 1)) * 1024);
            DRAIN();
        }
        lds_add(lds, C_U, lane);
        lds_wait<3>(lds, C_U, 4);
        const int cw = (bw & 1) * 2 + 1 - (bw >> 1);
        {
            const int pq = b & 3;
            unsigned tk;
            if (pq == 0) tk = cw == 0 ? 0x0D0u : cw == 1 ? (0x02Fu | (0x210u << 10)) : cw == 2 ? 0x1C8u : (0x137u | (0x208u << 10));
            else if (pq == 1) tk = cw == 0 ? 0x0E0u : cw == 1 ? (0x1A0u | (0x007u << 10)) : cw == 2 ? 0x15Du : (0x2C4u | (0x102u << 10));
            else if (pq == 2) tk = cw == 0 ? 0x2E0u : cw == 1 ? (0x1A0u | (0x207u << 10)) : cw == 2 ? 0x15Bu : (0x03Bu | (0x104u << 10));
            else tk = cw == 0 ? 0x1D0u : cw == 1 ? (0x12Fu | (0x010u << 10)) : cw == 2 ? 0x2C8u : (0x237u | (0x008u << 10));
            const int c = lane & 15, g = lane >> 4;
            const unsigned m1 = tk & 255u; const int cl1 = (tk >> 8) & 3, cl2 = (tk >> 18) & 3;
            const unsigned hl = (unsigned)(size_t)lds + L_H + ((c & 1) ^ 1) * HCOPY + (c + 8 * g + ((c & 1) ? 1 : 2)) * 2;
            const unsigned ul = (unsigned)(size_t)lds + L_MISC + 512 - 448 + (2 * c + 1 - g) * 16;
            unsigned a0 = hl + 2 * cl1 * HCOPY, a1 = a0 + 1024, a2 = a0 + 2048, a3 = a0 + 3072, bb = ul + cl1 * USTR;
            unsigned c0 = hl + 2 * cl2 * HCOPY, c1 = c0 + 1024, c2 = c0 + 2048, dd = ul + cl2 * USTR, cnt;
            const int s1 = slot0 + cl1, ch1 = (s1 % 72) * 8 + s1 / 72, s2 = slot0 + cl2, ch2 = (s2 % 72) * 8 + s2 / 72;
            const float D1 = blo((unsigned)AD_[ch1]), D2 = blo((unsigned)AD_[ch2]);
            const unsigned ue = (unsigned)(size_t)lds + L_MISC + 512 + (2 * c + (g >> 1)) * 16 + ((g & 1) ? 0 : 8), zs = (unsigned)(size_t)lds + L_SCR + bw * 512;
            asm volatile(CONV_ASM
                         : [a0] "+v"(a0), [a1] "+v"(a1), [a2] "+v"(a2), [a3] "+v"(a3), [b] "+v"(bb), [c0] "+v"(c0), [c1] "+v"(c1), [c2] "+v"(c2), [d] "+v"(dd), [cnt] "=&s"(cnt)
                         : [m1] "s"(m1), [d1] "s"(D1), [d2] "s"(D2), [ue] "v"(ue + cl1 * USTR), [uf] "v"(ue + cl2 * USTR), [zw] "v"(zs + c * 32 + g * 8), [zr] "v"(zs + (lane & 31) * 16),
                           [zo] "v"(l16), [rs] "s"(mkrs(ws_, WS_BYTES)), [z1] "s"(WS_ZT + ch1 * 4096), [z2] "s"(WS_ZT + ch2 * 4096)
                         : "memory", "scc", CONV_CLOB);
        }
        lds_add(lds, C_CV, lane);
        const GAS char* wos = (const GAS char*)wo_ + hg * 144;
        if (cw & 1) {
            lds_wait<2>(lds, C_CV, 4);
            dma_chunk(img + ((5 + (cw >> 1) + rot) & 7) * CH, l16, l16 + 4096, l16 + 8192, lds + (5 + (cw >> 1)) * CH);
            if (cw == 1) dma_chunk(img + ((7 + rot) & 7) * CH, l16, l16 + 4096, l16 + 8192, lds + 7 * CH);
            signal_last(lds, C_Z, 4, ws + WS_F3 + b * 128, want, lane);
            lds_add(lds, C_LAND, lane);
            lds_wait<8>(lds, C_PROG, 256);
            if (cw == 1) stage_wo(wos, lds, 0, 3, 1, lane);
            else {
                const unsigned t = *(const GAS unsigned*)((const GAS char*)Aq_ + (rb * 64 + lane) * 1152 + hg * 144) ^ *(const GAS unsigned*)((const GAS char*)Aq_ + (rb * 64 + lane) * 1152 + hg * 144 + 140);
                asm volatile("" :: "v"(t));
                const int idx[1] = {(lane & 28) * 8 + hg * 4 + (lane & 3)};
                poll_flags<1, 6>(ws + WS_F3, idx, want);
                if (b == 0 && lane == 0) __hip_atomic_store((gu64*)ws, want, RLX_AGENT);
#pragma unroll 1
                for (int i = 0; i < 9; ++i) dma16_sc1(ws + WS_ZT + (hg * 72 + i * 8) * 4096 + rb * 128, (lane >> 3) * 4096 + (lane & 7) * 16, lds + L_MISC + i * 1024);
            }
        } else {
            signal_last(lds, C_Z, 4, ws + WS_F3 + b * 128, want, lane);
            lds_wait<8>(lds, C_PROGB, 256);
            stage_wo(wos, lds, 3 + (cw >> 1), 9, 2, lane);
        }
        DRAIN();
        return;
    }

    const int lane = fresh_lane();
    const int rt = wid & 3, kh = wid >> 2, g = lane >> 4;
    f32x4 acc[3];
    f32x16 oa, ob, vb;
    const int r8 = wid * 8 + (lane >> 3), kk = wid * 4 + g, cw = (lane & 15) - hg;
    const unsigned l0 = (unsigned)(size_t)lds;
    const unsigned xv = (lane >> 3) * 1152 + ((lane & 7) ^ ((r8 >> 1) & 7)) * 16;
    const unsigned wv = kk * 1152 + (lane & 15) * 16;
    const int kr = (hg + 2 * (b >> 6) + 5 * (b & 7)) % 9;
    unsigned xo = kr * 128, wo = kr * 73728;
    const v4u xrs = mkrs((const char*)x_ + (rb * 64 + wid * 8) * 1152, 9216), wrs = mkrs((const char*)wq_ + hg * 128, 663552 - hg * 128);
    f32x16 w0a; f32x8 w0b;
    asm volatile(QFIRST : "=&{v[40:55]}"(w0a), "=&{v[56:63]}"(w0b), [xo] "+s"(xo), [wo] "+s"(wo)
                 : [xv] "v"(xv), [xs] "s"(xrs), [xm] "s"(l0 + wid * 1024), [wv] "v"(wv), [ws] "s"(wrs) : "memory", "scc");
    const int n0 = rt * 18 + 4 * (lane >> 5);
#define OCOL(n) (((n) >> 3) * 1024 + ((n) & 7) * 2)
    unsigned ot0 = (unsigned)(size_t)lds + L_OT + (kh * 32 + (lane & 31)) * 16, ot1 = ot0 + OCOL(n0 + 2), exr = (unsigned)(size_t)lds + L_OEX + (wid ^ 4) * 3072 + lane * 16, l16 = lane * 16;
    ot0 += OCOL(n0);
    const int gid = wid * 4 + g, gsg = gid / 9, goc = gid - gsg * 9;
    unsigned zp0 = (unsigned)(size_t)lds + L_MISC + (goc * 8 + ((lane & 15) >> 2)) * 128 + gsg * 32 + (lane & 3) * 8, gq0 = (gsg * 16 + (lane & 15)) * 1152 + goc * 16;
    unsigned zp1 = (unsigned)(size_t)lds + L_MISC + ((5 + g) * 8 + ((lane & 15) >> 2)) * 128 + 96 + (lane & 3) * 8, gq1 = (48 + (lane & 15)) * 1152 + (5 + g) * 16;
    unsigned go0 = gq0 + (gsg * 16 + (lane & 15)) * 1152, go1 = gq1 + (48 + (lane & 15)) * 1152;
    unsigned otr = l16 + (unsigned)(size_t)lds + L_OT + wid * 1024, bfa = (unsigned)(size_t)lds + L_CTL + C_BAD, osp = WS_OX + (rb * 8 + hg) * 9216 + wid * 1024;
    v4u rsw = mkrs(ws_, WS_BYTES), rsq = mkrs((const char*)Aq_ + rb * 73728 + hg * 144, 73728 - hg * 144), rso = mkrs((const char*)out_ + rb * 147456 + hg * 144, 147456 - hg * 144);
    asm volatile("" : "+v"(ot0), "+v"(ot1), "+v"(exr), "+v"(l16), "+v"(zp0), "+v"(zp1), "+v"(gq0), "+v"(gq1), "+v"(go0), "+v"(go1), "+v"(otr), "+v"(bfa), "+s"(osp), "+s"(rsw), "+s"(rsq), "+s"(rso));
    const int ca = lane & 31, ha = lane >> 5;
    const unsigned lbase = (unsigned)(size_t)lds + kh * 8 * CH;
    unsigned ak = lbase + lane * 16, av = lbase + 4096 + ha * 288 + ca * 16, ax = lbase + 8704 + ca * 4;
    unsigned af = (unsigned)(size_t)lds + L_CTL + C_LAND;
    unsigned al = af + kh * C_LANDB, exw = (unsigned)(size_t)lds + L_OEX + wid * 3072 + lane * 16;
    unsigned qr = (unsigned)(size_t)lds + L_QT + ca * 144 + rt * 36 + ha * 16, qe = (unsigned)(size_t)lds + L_QT + (ha * 32 + ca) * 144 + rt * 36 + 32;
    asm volatile("" : "+v"(ak), "+v"(av), "+v"(ax), "+v"(af), "+v"(al), "+v"(exw), "+v"(qr), "+v"(qe));
    {
        const unsigned wd = l0 + L_WQ + (cw < 8 ? ((kk >> 4) * 2 + ((kk >> 2) & 1)) * 1024 + (cw >> 1) * 256 + ((kk & 3) + 4 * ((kk >> 3) & 1)) * 32 + (cw & 1) * 16 : 4096 + kk * 16);
        const u64 wmask = 0x0001000100010001ull * (u64)(0x1ffu << hg);
        const int q = (lane & 15) >> 2, p = lane & 3, row = lane & 15;
        unsigned x0 = l0 + (rt * 2 + (row >> 3)) * 1024 + (row & 7) * 128 + (g ^ ((row >> 1) & 7)) * 16, x1 = x0 ^ 64;
        const unsigned wm = l0 + L_WQ + (g >> 1) * 2048 + (4 * (g & 1) + q) * 32 + p * 8, wx = l0 + L_WQ + 4096 + (8 * g + q) * 16 + (p & 1) * 8;
        unsigned st;
#define QPROJ_OPS : "+{v[40:55]}"(w0a), "+{v[56:63]}"(w0b), "=&{v[112:127]}"(oa), "=&{v[128:143]}"(ob), "=&{v[144:159]}"(vb), [xo] "+s"(xo), [wo] "+s"(wo), [st] "=&s"(st), [x] "+v"(x0), [y] "+v"(x1) \
                  : [kh] "s"(kh), [xv] "v"(xv), [xs] "s"(xrs), [xm] "s"(l0 + wid * 1024 + 3 * 8192), [wv] "v"(wv), [ws] "s"(wrs), [wd] "v"(wd), [wd2] "v"(wd + 14 * 4608), [wmask] "s"(wmask), \
                    [ctl] "v"(l0 + L_CTL), [one] "v"(1u), [vone] "v"((lane & 31) == 18 ? 0x3f803f80u : 0u), [qt] "v"(l0 + L_QT + (rt * 16 + (lane & 15)) * 144 + kh * 96 + g * 8), [w] "v"(wm), [u] "v"(wm + 14 * 4608), [e] "v"(wx), [f] "v"(wx + 14 * 4608) \
                  : "memory", "scc", QPROJ_CLOB
        asm volatile(BY_KH(QPROJ0, QPROJ1) QPROJ_OPS);
    }
    v4u qa, qb, qx; f32x12 got; unsigned bad;
    {
        unsigned cnt, thr, st;
        asm volatile(ATTN_ASM
                     : "+{v[112:127]}"(oa), "+{v[128:143]}"(ob), "+{v[144:159]}"(vb), "=&{v[100:103]}"(qa), "=&{v[104:107]}"(qb), "=&{v[108:111]}"(qx), "=&{v[40:51]}"(got), [ak] "+v"(ak), [av] "+v"(av), [ax] "+v"(ax), [cnt] "=&s"(cnt), [st] "=&s"(st), [thr] "=&s"(thr)
                     : [vm] "s"(0x0003ffff0003ffffull), [yw] "s"(kh), [af] "v"(af), [al] "v"(al), [exw] "v"(exw), [exr] "v"(exr), [sig] "s"(kh ? 2 : 5), [thr4] "s"(kh ? 0 : 5),
                       [qr] "v"(qr), [qe] "v"(qe)
                     : "memory", "scc", "vcc", ATTN_CLOB);
    }
    const int ln = fresh_lane();
    const int c = ln & 31, h = ln >> 5, g2 = ln >> 4;
    u64 m0;
#define RUN_ONORM(CHK) asm volatile(ONORM : "+{v[40:51]}"(got), [bad] "=&s"(bad), [m0] "=&s"(m0) : [chk] "i"(CHK), [bf] "v"(bfa), "{v[112:127]}"(oa), "{v[128:143]}"(ob), [kh] "s"(kh), [w7] "s"(wid), [ot0] "v"(ot0), [ot1] "v"(ot1), [otr] "v"(otr), \
                 [lv] "v"(l16), [rs] "s"(rsw), [sp] "s"(osp), [zp0] "v"(zp0), [zp1] "v"(zp1), [q0] "v"(gq0), [q1] "v"(gq1), \
                 [qrs] "s"(rsq), [o0] "v"(go0), [o1] "v"(go1), [ors] "s"(rso) \
                 : "memory", "scc", "vcc", ONORM_CLOB)
    RUN_ONORM(1);
    if (__builtin_expect(bad != 0, 0)) {
        DRAIN();
        BAR();
        if (wid == 0 && ln == 0) *(LAS unsigned*)(lds + L_CTL + C_BAD) = 0u;
#pragma unroll 1
        for (int i = wid; i < 144; i += 8) dma16_sc1(img + i * 1024, ln * 16, lds + i * 1024);
        BAR_VM();
        LAS char* kb = lds + kh * 8 * CH;
        const v4u vinit = c == 18 ? (v4u){0x3f803f80u, 0x3f803f80u, 0x3f803f80u, 0x3f803f80u} : (v4u){0u, 0u, 0u, 0u};
        float mx[2];
        const auto es = __builtin_amdgcn_permlane32_swap(qx.x, qx.x, false, false);
        const unsigned ea = es[0], eb = es[1];
#pragma unroll
        for (int qt = 0; qt < 2; ++qt) {
            const bf16x8 qm = asbf(qt ? qb : qa); const v2u qe = {h ? 0u : (qt ? eb : ea), 0u};
            float m = -__builtin_inff();
#pragma unroll 1
            for (int t = 0; t < 32; ++t) {
                LAS char* cb = kb + (t >> 2) * CH;
                const v2u ke = {*(LAS unsigned*)(cb + 8704 + (t & 3) * 128 + c * 4), 0u};
                f32x16 s = {0.f};
                s = __builtin_amdgcn_mfma_f32_32x32x16_bf16(asbf(*(LAS v4u*)(cb + (t & 3) * 1024 + ln * 16)), qm, s, 0, 0, 0);
                s = __builtin_amdgcn_mfma_f32_32x32x8bf16_1k(__builtin_bit_cast(s16x4, ke), __builtin_bit_cast(s16x4, qe), s, 0, 0, 0);
#pragma unroll
                for (int i = 0; i < 16; ++i) m = __builtin_fmaxf(m, s[i]);
            }
            const auto sw = __builtin_amdgcn_permlane32_swap(__float_as_uint(m), __float_as_uint(m), false, false);
            mx[qt] = __builtin_fmaxf(__uint_as_float(sw[0]), __uint_as_float(sw[1]));
        }
        *(LAS float*)(lds + L_SCR + wid * 256 + ln * 4) = h ? mx[1] : mx[0];
        BAR();
#pragma unroll
        for (int qt = 0; qt < 2; ++qt) {
            const bf16x8 qm = asbf(qt ? qb : qa); const v2u qe = {h ? 0u : (qt ? eb : ea), 0u};
            const float m = __builtin_fmaxf(mx[qt], *(LAS float*)(lds + L_SCR + (wid ^ 4) * 256 + (qt * 32 + c) * 4));
            f32x16 o = {0.f};
#pragma unroll 1
            for (int t = 0; t < 32; ++t) {
                LAS char* cb = kb + (t >> 2) * CH;
                const v2u ke = {*(LAS unsigned*)(cb + 8704 + (t & 3) * 128 + c * 4), 0u};
                f32x16 s;
#pragma unroll
                for (int i = 0; i < 16; ++i) s[i] = -m;
                s = __builtin_amdgcn_mfma_f32_32x32x16_bf16(asbf(*(LAS v4u*)(cb + (t & 3) * 1024 + ln * 16)), qm, s, 0, 0, 0);
                s = __builtin_amdgcn_mfma_f32_32x32x8bf16_1k(__builtin_bit_cast(s16x4, ke), __builtin_bit_cast(s16x4, qe), s, 0, 0, 0);
                f32x8 p0, p1;
#pragma unroll
                for (int i = 0; i < 8; ++i) { p0[i] = __builtin_amdgcn_exp2f(s[i]); p1[i] = __builtin_amdgcn_exp2f(s[8 + i]); }
                LAS char* vb = cb + 4096 + ((t & 3) * 4 + h) * 288 + (c < 18 ? c : 0) * 16;
                const v4u v0 = *(LAS v4u*)vb, v1 = *(LAS v4u*)(vb + 576);
                o = __builtin_amdgcn_smfmac_f32_32x32x32_bf16(asbf(c < 18 ? v0 : vinit), __builtin_bit_cast(bf16x16, p0), o, 0xdddd, 0, 0);
                o = __builtin_amdgcn_smfmac_f32_32x32x32_bf16(asbf(c < 18 ? v1 : vinit), __builtin_bit_cast(bf16x16, p1), o, 0xdddd, 0, 0);
            }
            if (qt) ob = o; else oa = o;
        }
        BAR();
        stage_wo((const GAS char*)wo_ + hg * 144, lds, wid, 9, 8, ln);
        {
            LAS f32x4* ex = (LAS f32x4*)(lds + L_OEX + wid * 3072 + ln * 16);
            const f32x16 give = kh ? oa : ob;
#pragma unroll
            for (int i = 0; i < 3; ++i) ex[i * 64] = (f32x4){give[4 * i], give[4 * i + 1], give[4 * i + 2], give[4 * i + 3]};
        }
        BAR_VM();
#pragma unroll
        for (int i = 0; i < 3; ++i) { const f32x4 t = *(LAS f32x4*)(lds + L_OEX + (wid ^ 4) * 3072 + ln * 16 + i * 1024); got[4 * i] = t[0]; got[4 * i + 1] = t[1]; got[4 * i + 2] = t[2]; got[4 * i + 3] = t[3]; }
        RUN_ONORM(0);
    }
    {
        const int q = (ln & 15) >> 2, p = ln & 3;
        const unsigned l0 = (unsigned)(size_t)lds;
        const unsigned wm = l0 + (g2 >> 1) * 2048 + (4 * (g2 & 1) + q) * 32 + p * 8, wx = l0 + 24576 + (8 * g2 + q) * 16 + (p & 1) * 8;
        const unsigned da = l0 + (g2 == 3 ? 14 * CH : (g2 == 2 && kh) ? L_MISC - 9216 : 3 * CH + g2 * 18432) + (ln & 15) * 16 + wid * 2304;
        const unsigned x0 = l0 + (rt == 3 ? 14 * CH : 3 * CH + rt * 18432) + ln * 16, x1 = rt == 2 ? l0 + L_MISC + ln * 16 : x0 + 9216;
        unsigned cnt, t;
        asm volatile(BY_KH(OPRE0, OPRE1) OFETCH BY_KH(OGEMM0, OGEMM1)
                     : "=&{v[48:51]}"(acc[0]), "=&{v[52:55]}"(acc[1]), "=&{v[56:59]}"(acc[2]), [cnt] "=&s"(cnt), [t] "=&v"(t)
                     : [kh] "s"(kh), [w] "v"(wm), [u] "v"(wm + 8 * CH), [e] "v"(wx), [f] "v"(wx + 8 * CH + 24576), [x] "v"(x0), [y] "v"(x1), [da] "v"(da),
                       [pv] "v"(ln * 144 + 140), [lv] "v"(ln * 16), [lv2] "v"(ln * 16 + 4096), [lv3] "v"(ln * 16 + 8192), [rs] "s"(mkrs(ws_, WS_BYTES)), [so] "s"(WS_OX + (rb * 8 + wid) * 9216), [dly] "i"(6)
                     : "memory", "scc", "vcc", OTAIL_CLOB);
        GAS char* op = (GAS char*)out_ + (rb * 64 + rt * 16) * 2304 + 1152 + hg * 144;
        if (kh == 0) {
            LAS char* ot = lds + L_OUT + rt * 1536;
#pragma unroll
            for (int i = 0; i < 3; ++i) *(LAS v2u*)(ot + (ln & 15) * 96 + i * 32 + g2 * 8) = (v2u){pk2(acc[i][0], acc[i][1]), pk2(acc[i][2], acc[i][3])};
            const int r0 = ln / 6, r1 = (ln + 64) / 6;
            *(GAS v4u*)(op + r0 * 2304 + (ln - r0 * 6) * 16) = *(LAS v4u*)(ot + ln * 16);
            if (ln < 32) *(GAS v4u*)(op + r1 * 2304 + (ln + 64 - r1 * 6) * 16) = *(LAS v4u*)(ot + 1024 + ln * 16);
        } else {
            op += (ln & 15) * 2304 + 96 + g2 * 8;
            *(GAS v2u*)op = (v2u){pk2(acc[0][0], acc[0][1]), pk2(acc[0][2], acc[0][3])};
            if (g2 < 2) *(GAS v2u*)(op + 32) = (v2u){pk2(acc[1][0], acc[1][1]), pk2(acc[1][2], acc[1][3])};
        }
    }
}

extern "C" void kernel_launch(void* const* d_in, const int* in_sizes, int n_in, void* d_out, int out_size, void* d_ws, size_t ws_size, hipStream_t stream) {
    (void)in_sizes; (void)n_in; (void)out_size; (void)ws_size;
    (void)hipFuncSetAttribute((const void*)fused_h3_mqa, hipFuncAttributeMaxDynamicSharedMemorySize, LDS_BYTES);
    fused_h3_mqa<<<256, 768, LDS_BYTES, stream>>>((const unsigned short*)d_in[0], (const unsigned short*)d_in[1], (const unsigned short*)d_in[2], (const unsigned short*)d_in[3],
                                                 (const unsigned short*)d_in[4], (const unsigned short*)d_in[5], (const unsigned short*)d_in[6], (const unsigned short*)d_in[7],
                                                 (const unsigned short*)d_in[8], (const unsigned short*)d_in[9], (const unsigned short*)d_in[10], (unsigned short*)d_out, (char*)d_ws);
}
```
